```python
import torch

def _softmax(x, axis=-1):
    return torch.softmax(x, dim=axis)

def _gelu(x):
    return torch.nn.functional.gelu(x, approximate='tanh')

def _silu(x):
    return torch.nn.functional.silu(x)

def _sigmoid(x):
    return torch.sigmoid(x)

def _layernorm(x, axis=-1, eps=1e-05):
    m = x.mean(dim=axis, keepdim=True)
    v = x.var(dim=axis, keepdim=True, unbiased=False)
    return (x - m) / torch.sqrt(v + eps)

def _scatter_add(tgt, idx, src, axis=0):
    return tgt.clone().index_add_(axis, idx, src)

def _segment_sum(src, seg, n):
    out = torch.zeros((n,) + src.shape[1:], dtype=src.dtype)
    return out.index_add_(0, seg, src)

def _pad(x, pw):
    flat = tuple((v for pair in reversed(pw) for v in pair))
    return torch.nn.functional.pad(x, flat)
kernel_name = 'adaLN_cond__sliding_block__swiglu_ffn__prompt'

def setup_inputs():

    _w = lambda a: (lambda t: t.to(torch.bfloat16) if t.is_floating_point() else t)(torch.as_tensor(a))

    def _A_setup():
        B, S, D = (1, 2048, 640)
        return {'x': _w((lambda *s: torch.randn(*s, dtype=torch.bfloat16) * 0.05)(B, S, D)), 'wqkv': _w((lambda *s: torch.randn(*s, dtype=torch.bfloat16) * 0.05)(D, 3 * D)), 'wo': _w((lambda *s: torch.randn(*s, dtype=torch.bfloat16) * 0.05)(D, D))}
    _A_in = _A_setup()

    def _B_setup():
        B, S, D, F = (1, 2048, 640, 1728)
        return {'x': _w((lambda *s: torch.randn(*s, dtype=torch.bfloat16) * 0.05)(B, S, D)), 'w_gate': _w((lambda *s: torch.randn(*s, dtype=torch.bfloat16) * 0.05)(D, F)), 'w_up': _w((lambda *s: torch.randn(*s, dtype=torch.bfloat16) * 0.05)(D, F)), 'w_down': _w((lambda *s: torch.randn(*s, dtype=torch.bfloat16) * 0.05)(F, D))}
    _B_in = _B_setup()
    return {'A_x': _A_in['x'], 'A_wqkv': _A_in['wqkv'], 'A_wo': _A_in['wo'], 'B_x': _B_in['x'], 'B_w_gate': _B_in['w_gate'], 'B_w_up': _B_in['w_up'], 'B_w_down': _B_in['w_down'], 'c': _w((lambda *s: torch.randn(*s, dtype=torch.bfloat16) * 0.1)(1, 640) * 8.0), 'w_mod': _w((lambda *s: torch.randn(*s, dtype=torch.bfloat16) * 0.1)(640, 6 * 640) * 640 ** (-0.5) * 8.0)}

def reference(A_x, A_wqkv, A_wo, B_x, B_w_gate, B_w_up, B_w_down, c, w_mod):

    def _A_ref(x, wqkv, wo):
        B, S, D = x.shape
        H = 32
        Dh = D // H
        W = 256
        qkv = (lambda x, *d: x.permute(*d))((x @ wqkv).reshape(B, S, 3, H, Dh), 2, 0, 3, 1, 4)
        q, k, v = (qkv[0], qkv[1], qkv[2])
        scores = torch.einsum('bhqd,bhkd->bhqk', q, k) * Dh ** (-0.5)
        idx = torch.arange(S)
        mask = (idx[:, None] - idx[None, :] >= 0) & (idx[:, None] - idx[None, :] < W)
        scores = torch.where(mask[None, None], scores, -1000000000.0)
        p = _softmax(scores, axis=-1)
        o = torch.einsum('bhqk,bhkd->bhqd', p, v)
        return (lambda x, *d: x.permute(*d))(o, 0, 2, 1, 3).reshape(B, S, D) @ wo

    def _B_ref(x, w_gate, w_up, w_down):
        g = _silu(x @ w_gate)
        u = x @ w_up
        return g * u @ w_down
    x = A_x
    mod = (_silu(c) @ w_mod).reshape(c.shape[0], 6, 1, -1)
    sa, ca, ga, sb, cb, gb = (mod[:, 0].clone(), mod[:, 1].clone(), mod[:, 2].clone(), mod[:, 3].clone(), mod[:, 4].clone(), mod[:, 5].clone())
    h = _layernorm(x, axis=-1) * (1 + ca) + sa
    x = x + ga * _A_ref(h, A_wqkv, A_wo)
    h = _layernorm(x, axis=-1) * (1 + cb) + sb
    x = x + gb * _B_ref(h, B_w_gate, B_w_up, B_w_down)
    return x
```

```cpp
#include <hip/hip_runtime.h>
#include <hip/hip_bf16.h>
#include <stdint.h>
typedef unsigned short u16;
constexpr int S = 2048, D = 640, H = 32, DH = 20, W = 256, F = 1728;
__device__ __forceinline__ float b2f(u16 v) { return __uint_as_float((unsigned)v << 16); }
__device__ __forceinline__ u16 f2b(float f) { __hip_bfloat16 b = __float2bfloat16(f); return *(u16*)&b; }

__global__ void k_mod(const u16* c, const u16* wm, float* mod) {
  int n = blockIdx.x * blockDim.x + threadIdx.x; if (n >= 6 * D) return;
  float s = 0; for (int k = 0; k < D; ++k) { float v = b2f(c[k]); v = v / (1.f + __expf(-v)); s += v * b2f(wm[(size_t)k * 6 * D + n]); }
  mod[n] = s;
}
__global__ void k_ln(const u16* x, const float* sh, const float* sc, u16* h) {
  int r = blockIdx.x; __shared__ float red[2][4];
  float s = 0; for (int k = threadIdx.x; k < D; k += 256) s += b2f(x[r * D + k]);
  for (int o = 32; o; o >>= 1) s += __shfl_xor(s, o);
  if ((threadIdx.x & 63) == 0) red[0][threadIdx.x >> 6] = s; __syncthreads();
  float m = (red[0][0] + red[0][1] + red[0][2] + red[0][3]) / D;
  float q = 0; for (int k = threadIdx.x; k < D; k += 256) { float d = b2f(x[r * D + k]) - m; q += d * d; }
  for (int o = 32; o; o >>= 1) q += __shfl_xor(q, o);
  if ((threadIdx.x & 63) == 0) red[1][threadIdx.x >> 6] = q; __syncthreads();
  float rs = rsqrtf((red[1][0] + red[1][1] + red[1][2] + red[1][3]) / D + 1e-5f);
  for (int k = threadIdx.x; k < D; k += 256) h[r * D + k] = f2b((b2f(x[r * D + k]) - m) * rs * (1.f + sc[k]) + sh[k]);
}
__global__ void k_gemm(const u16* A, const u16* B, u16* C, int N, int K, int mode, const u16* res, const float* g) {
  __shared__ float As[16][17], Bs[16][17];
  int tx = threadIdx.x & 15, ty = threadIdx.x >> 4; int r = blockIdx.y * 16 + ty, c = blockIdx.x * 16 + tx;
  float s = 0;
  for (int k0 = 0; k0 < K; k0 += 16) {
    As[ty][tx] = b2f(A[(size_t)r * K + k0 + tx]); Bs[ty][tx] = b2f(B[(size_t)(k0 + ty) * N + c]); __syncthreads();
    for (int k = 0; k < 16; ++k) s += As[ty][k] * Bs[k][tx];
    __syncthreads();
  }
  if (mode == 1) s = b2f(res[(size_t)r * N + c]) + g[c] * s;
  C[(size_t)r * N + c] = f2b(s);
}
__global__ void k_attn(const u16* qkv, u16* o) {
  int i = blockIdx.x * blockDim.x + threadIdx.x; int h = blockIdx.y;
  float q[DH]; for (int d = 0; d < DH; ++d) q[d] = b2f(qkv[(size_t)i * 3 * D + h * DH + d]);
  float m = -1e30f; int j0 = i - W + 1 < 0 ? 0 : i - W + 1;
  for (int j = j0; j <= i; ++j) { float s = 0; for (int d = 0; d < DH; ++d) s += q[d] * b2f(qkv[(size_t)j * 3 * D + D + h * DH + d]); m = fmaxf(m, s * 0.22360679775f); }
  float l = 0, acc[DH]; for (int d = 0; d < DH; ++d) acc[d] = 0;
  for (int j = j0; j <= i; ++j) { float s = 0; for (int d = 0; d < DH; ++d) s += q[d] * b2f(qkv[(size_t)j * 3 * D + D + h * DH + d]);
    float p = __expf(s * 0.22360679775f - m); l += p; for (int d = 0; d < DH; ++d) acc[d] += p * b2f(qkv[(size_t)j * 3 * D + 2 * D + h * DH + d]); }
  for (int d = 0; d < DH; ++d) o[(size_t)i * D + h * DH + d] = f2b(acc[d] / l);
}
__global__ void k_act(const u16* g, const u16* u, u16* a, int n) {
  int i = blockIdx.x * blockDim.x + threadIdx.x; if (i >= n) return;
  float v = b2f(g[i]); a[i] = f2b(v / (1.f + __expf(-v)) * b2f(u[i]));
}
extern "C" void kernel_launch(void* const* d_in, const int* in_sizes, int n_in, void* d_out, int out_size, void* d_ws, size_t ws_size, hipStream_t stream) {
  const u16 *x = (const u16*)d_in[0], *wqkv = (const u16*)d_in[1], *wo = (const u16*)d_in[2], *wg = (const u16*)d_in[4], *wu = (const u16*)d_in[5], *wd = (const u16*)d_in[6], *c = (const u16*)d_in[7], *wm = (const u16*)d_in[8];
  char* ws = (char*)d_ws;
  float* mod = (float*)ws; u16* h = (u16*)(ws + (1 << 20)); u16* qkv = (u16*)(ws + (8 << 20)); u16* o = (u16*)(ws + (24 << 20)); u16* x1 = (u16*)(ws + (32 << 20));
  u16* g = (u16*)(ws + (40 << 20)); u16* u = (u16*)(ws + (56 << 20)); u16* a = (u16*)(ws + (72 << 20));
  k_mod<<<15, 256, 0, stream>>>(c, wm, mod);
  k_ln<<<S, 256, 0, stream>>>(x, mod, mod + D, h);
  k_gemm<<<dim3(3 * D / 16, S / 16), 256, 0, stream>>>(h, wqkv, qkv, 3 * D, D, 0, nullptr, nullptr);
  k_attn<<<dim3(S / 64, H), 64, 0, stream>>>(qkv, o);
  k_gemm<<<dim3(D / 16, S / 16), 256, 0, stream>>>(o, wo, x1, D, D, 1, x, mod + 2 * D);
  k_ln<<<S, 256, 0, stream>>>(x1, mod + 3 * D, mod + 4 * D, h);
  k_gemm<<<dim3(F / 16, S / 16), 256, 0, stream>>>(h, wg, g, F, D, 0, nullptr, nullptr);
  k_gemm<<<dim3(F / 16, S / 16), 256, 0, stream>>>(h, wu, u, F, D, 0, nullptr, nullptr);
  k_act<<<(S * F + 255) / 256, 256, 0, stream>>>(g, u, a, S * F);
  k_gemm<<<dim3(D / 16, S / 16), 256, 0, stream>>>(a, wd, (u16*)d_out, D, F, 1, x1, mod + 5 * D);
}
```

```cpp
#include <hip/hip_runtime.h>
#include <stdint.h>

#define LAS __attribute__((address_space(3)))
#define GAS __attribute__((address_space(1)))
#define DI __device__ __forceinline__
typedef float f32x2 __attribute__((ext_vector_type(2)));
typedef float f32x4 __attribute__((ext_vector_type(4)));
typedef float f32x16 __attribute__((ext_vector_type(16)));
typedef unsigned u32x2 __attribute__((ext_vector_type(2)));
typedef unsigned u32x4 __attribute__((ext_vector_type(4)));
typedef short s16x4 __attribute__((ext_vector_type(4)));
typedef short s16x8 __attribute__((ext_vector_type(8)));
typedef __bf16 bf16x2 __attribute__((ext_vector_type(2)));
typedef __bf16 bf16x8 __attribute__((ext_vector_type(8)));
typedef unsigned long long u64;
typedef __amdgpu_buffer_rsrc_t rsrc_t;

constexpr int S_ = 2048, D_ = 640, F_ = 1728;
constexpr int T32_D = 80 * 512;
constexpr int T32_F = 216 * 512;

constexpr int WS_EPOCH = 0;
constexpr int WS_TMO = 256;
constexpr int WS_FLAG = 4096;
constexpr int FL_XN0 = 0, FL_XN1 = 1, FL_KV = 2, FL_O = 3, FL_Y = 4, FL_ACT = 5;
constexpr int WS_GRAN = 256 * 1024;
constexpr int WS_UV = 320 * 1024;
constexpr int WS_STAT = 512 * 1024;
constexpr int WS_STAMP = 768 * 1024;
constexpr int WS_XN = 1 << 20;
constexpr int WS_KV = 4 << 20;
constexpr int WS_O = 12 << 20;
constexpr int WS_Y = 16 << 20;
constexpr int WS_ACT = 20 << 20;
constexpr int WS_USED = 28 << 20;

constexpr int LDS_BYTES = 163840;
constexpr int L1_W = 0;
constexpr int L1_VS = 153600, L1_VA = 156160;
constexpr int L2_KV = 0;
constexpr int KV_IMG = 384 * 48;
constexpr int L3_W0 = 4 * KV_IMG;
constexpr int L3_NPRE = 17;
constexpr int L3_W1 = 0;
constexpr int L3_VEC = 161 * 1000 + 24;
constexpr int L4_STAGE = 28800, L4_U = 14464;
constexpr int L4_UV = 144000;
constexpr int L5_NRING = 30;
static_assert(L3_W0 + L3_NPRE * 5120 <= L3_VEC && L3_VEC + 960 <= LDS_BYTES, "P3 LDS map");
static_assert(L5_NRING * 5120 <= L3_VEC, "P5 LDS map");
static_assert(L4_UV + 1792 <= L3_VEC, "P4 LDS map");

constexpr float LOG2E = 1.4426950408889634f;
constexpr float SCORE_C = 0.22360679774997896f * 1.4426950408889634f;
constexpr float NEGBIG = -1.0e30f;
constexpr unsigned SPIN_CAP = 1u << 15;

#define SB() __builtin_amdgcn_sched_barrier(0)
#define BAR() do { asm volatile("s_waitcnt lgkmcnt(0)" ::: "memory"); __builtin_amdgcn_s_barrier(); asm volatile("" ::: "memory"); } while (0)
#define VMWAIT(n) asm volatile("s_waitcnt vmcnt(" #n ")" ::: "memory")
#define MEMFENCE() asm volatile("" ::: "memory")

DI unsigned pk2(float a, float b) { bf16x2 v = __builtin_convertvector((f32x2){a, b}, bf16x2); return __builtin_bit_cast(unsigned, v); }
DI float blo(unsigned v) { return __uint_as_float(v << 16); }
DI float bhi(unsigned v) { return __uint_as_float(v & 0xffff0000u); }
DI float bf16r(float a) { return blo(pk2(a, a)); }
DI float rcpf_(float x) { return __builtin_amdgcn_rcpf(x); }
DI float exp2_(float x) { return __builtin_amdgcn_exp2f(x); }
DI float siluf(float x) { return x * rcpf_(1.0f + exp2_(-LOG2E * x)); }
template <int CTRL> DI float dpp_add(float x) { return x + __int_as_float(__builtin_amdgcn_update_dpp(0, __float_as_int(x), CTRL, 0xf, 0xf, true)); }
DI float sum16(float x) { x = dpp_add<0xB1>(x); x = dpp_add<0x4E>(x); x = dpp_add<0x141>(x); return dpp_add<0x140>(x); }
DI float xsum16(float x) { auto r = __builtin_amdgcn_permlane16_swap(__float_as_uint(x), __float_as_uint(x), false, false); return __uint_as_float(r[0]) + __uint_as_float(r[1]); }
DI float xsum32(float x) { auto r = __builtin_amdgcn_permlane32_swap(__float_as_uint(x), __float_as_uint(x), false, false); return __uint_as_float(r[0]) + __uint_as_float(r[1]); }
DI float xmax32(float x) { auto r = __builtin_amdgcn_permlane32_swap(__float_as_uint(x), __float_as_uint(x), false, false); return fmaxf(__uint_as_float(r[0]), __uint_as_float(r[1])); }
DI float sum64(float x) { return xsum32(xsum16(sum16(x))); }
DI rsrc_t mkrsrc(const void* p, unsigned bytes) { return __builtin_amdgcn_make_buffer_rsrc((void*)p, (short)0, (int)bytes, 0x00020000); }
DI u32x4 ld16_sc1(rsrc_t r, unsigned voff, unsigned soff) { return __builtin_amdgcn_raw_buffer_load_b128(r, voff, soff, 16); }
DI u32x4 ld16(rsrc_t r, unsigned voff, unsigned soff) { return __builtin_amdgcn_raw_buffer_load_b128(r, voff, soff, 0); }
DI u32x2 ld8(rsrc_t r, unsigned voff, unsigned soff) { return __builtin_amdgcn_raw_buffer_load_b64(r, voff, soff, 0); }
DI u32x2 ld8_sc1(rsrc_t r, unsigned voff, unsigned soff) { return __builtin_amdgcn_raw_buffer_load_b64(r, voff, soff, 16); }
DI void st16_sc1(u32x4 v, rsrc_t r, unsigned voff, unsigned soff) { __builtin_amdgcn_raw_buffer_store_b128(v, r, voff, soff, 16); }
DI void dma16(rsrc_t r, unsigned voff, unsigned soff, unsigned ldsaddr) {
  asm volatile("s_mov_b32 m0, %0\n\ts_nop 0\n\tbuffer_load_dwordx4 %1, %2, %3 offen lds" :: "s"(ldsaddr), "v"(voff), "s"(r), "s"(soff) : "memory");
}
DI u64 ld_u64(const char* ws, unsigned off) { return __hip_atomic_load((GAS u64*)(ws + off), __ATOMIC_RELAXED, __HIP_MEMORY_SCOPE_AGENT); }
DI void st_u64(char* ws, unsigned off, u64 v) { __hip_atomic_store((GAS u64*)(ws + off), v, __ATOMIC_RELAXED, __HIP_MEMORY_SCOPE_AGENT); }
DI u64 flagval(unsigned tag) { return ((u64)tag << 32) | (u64)(~tag); }
DI unsigned flagoff(int kind, int mb, int j) { return WS_FLAG + ((kind * 16 + mb) * 16 + j) * 128; }
DI bool spin_fail(unsigned& spins, char* ws, unsigned tag) {
  __builtin_amdgcn_s_sleep(1);
  if (++spins > SPIN_CAP) { __hip_atomic_store((GAS unsigned*)(ws + WS_TMO), tag, __ATOMIC_RELAXED, __HIP_MEMORY_SCOPE_AGENT); return true; }
  return (spins & 127u) == 0u && (unsigned)__builtin_amdgcn_readfirstlane(__hip_atomic_load((GAS unsigned*)(ws + WS_TMO), __ATOMIC_RELAXED, __HIP_MEMORY_SCOPE_AGENT)) == tag;
}
DI void wait_flags(char* ws, unsigned off, bool on, unsigned tag) {
  const u64 want = flagval(tag);
  for (unsigned spins = 0;;) {
    u64 v = want;
    if (on) v = ld_u64(ws, off);
    if (__all(v == want)) break;
    if (spin_fail(spins, ws, tag)) break;
  }
  MEMFENCE();
}
template <int N> DI void sweep(char* ws, unsigned off0, unsigned stride, unsigned tag, unsigned (&val)[N]) {
  for (unsigned spins = 0;;) {
    bool ok = true;
#pragma unroll
    for (int k = 0; k < N; ++k) { const u64 g = ld_u64(ws, off0 + k * stride); val[k] = (unsigned)g; ok &= (unsigned)(g >> 32) == tag; }
    if (__all(ok)) break;
    if (spin_fail(spins, ws, tag)) break;
  }
  MEMFENCE();
}
DI s16x8 trfrag(unsigned a0, unsigned a1) {
  const s16x4 lo = __builtin_amdgcn_ds_read_tr16_b64_v4i16((LAS s16x4*)(size_t)a0);
  const s16x4 hi = __builtin_amdgcn_ds_read_tr16_b64_v4i16((LAS s16x4*)(size_t)a1);
  return __builtin_shufflevector(lo, hi, 0, 1, 2, 3, 4, 5, 6, 7);
}
DI f32x4 lds_f4(unsigned a) { return *(LAS f32x4*)(size_t)a; }
DI u32x4 lds_u4(unsigned a) { return *(LAS u32x4*)(size_t)a; }
DI void lds_st4(unsigned a, u32x4 v) { *(LAS u32x4*)(size_t)a = v; }
DI f32x16 mfma32(s16x8 a, u32x4 b, f32x16 c) { return __builtin_amdgcn_mfma_f32_32x32x16_bf16(__builtin_bit_cast(bf16x8, a), __builtin_bit_cast(bf16x8, b), c, 0, 0, 0); }
DI f32x16 mfma32u(u32x4 a, u32x4 b, f32x16 c) { return __builtin_amdgcn_mfma_f32_32x32x16_bf16(__builtin_bit_cast(bf16x8, a), __builtin_bit_cast(bf16x8, b), c, 0, 0, 0); }
DI f32x4 mfma16(s16x8 a, u32x4 b, f32x4 c) { return __builtin_amdgcn_mfma_f32_16x16x32_bf16(__builtin_bit_cast(bf16x8, a), __builtin_bit_cast(bf16x8, b), c, 0, 0, 0); }

#define STAMP(i) do { } while (0)

DI void mod_group(rsrc_t rc, rsrc_t rm, char* ws, int grp, int lane, unsigned tag) {
  const u32x4 c4 = ld16(rc, 20 * lane, 0);
  const unsigned c1 = __builtin_amdgcn_raw_buffer_load_b32(rc, 20 * lane + 16, 0, 0);
  u32x4 w[10];
#pragma unroll
  for (int r = 0; r < 10; ++r) w[r] = ld16(rm, (10 * lane + r) * 7680, 16 * grp);
  const unsigned cc[5] = {c4[0], c4[1], c4[2], c4[3], c1};
  float acc[8];
#pragma unroll
  for (int e = 0; e < 8; ++e) acc[e] = 0.f;
#pragma unroll
  for (int r = 0; r < 10; ++r) {
    const float cv = (r & 1) ? bhi(cc[r >> 1]) : blo(cc[r >> 1]);
    const float sc = bf16r(siluf(cv));
#pragma unroll
    for (int e = 0; e < 4; ++e) { acc[2 * e] = fmaf(sc, blo(w[r][e]), acc[2 * e]); acc[2 * e + 1] = fmaf(sc, bhi(w[r][e]), acc[2 * e + 1]); }
  }
#pragma unroll
  for (int e = 0; e < 8; ++e) acc[e] = sum64(acc[e]);
  const unsigned p0 = pk2(acc[0], acc[1]), p1 = pk2(acc[2], acc[3]), p2 = pk2(acc[4], acc[5]), p3 = pk2(acc[6], acc[7]);
  const unsigned pv = lane == 0 ? p0 : lane == 1 ? p1 : lane == 2 ? p2 : p3;
  if (lane < 4) st_u64(ws, WS_GRAN + (4 * grp + lane) * 8, ((u64)tag << 32) | pv);
}

DI int ct_of(int p, int odd) { return odd ? (p < 4 ? p + 1 : 0) : p; }
DI int unit_of(int p, int odd, int nu) { const int u = odd ? (p < 6 ? p + 1 : 0) : p; return u < nu ? u : nu - 1; }

template <class RM> DI unsigned w80_voff(int piece, int lane, int nt, RM rowmap) {
  const int odd = nt & 1;
  int g, t, q, p, hf;
  if (piece < 4) { g = piece; t = lane >> 5; const int su = (lane >> 4) & 1; q = (lane >> 2) & 3; const int c = (lane & 3) ^ (2 * (piece & 1)); p = 2 * su + (c >> 1); hf = c & 1; }
  else { t = lane >> 5; g = (lane >> 3) & 3; q = (lane >> 1) & 3; p = 4; hf = lane & 1; }
  return rowmap(g, t, q) * 1280 + 160 * nt + 32 * ct_of(p, odd) + 16 * hf;
}

__global__ __launch_bounds__(256, 1) void adaln_block_kernel(const char* __restrict__ x, const char* __restrict__ wqkv, const char* __restrict__ wo,
                                                             const char* __restrict__ wg, const char* __restrict__ wu, const char* __restrict__ wd,
                                                             const char* __restrict__ cvec, const char* __restrict__ wmod, char* out, char* ws) {
  __shared__ __attribute__((aligned(16))) char smem[LDS_BYTES];
  const unsigned L0 = (unsigned)(size_t)(LAS char*)smem;
  const int tid = threadIdx.x, lane = tid & 63;
  const int wave = __builtin_amdgcn_readfirstlane(tid >> 6);
  const int bid = blockIdx.x, xcd = bid & 7, ii = bid >> 3, mb = 2 * xcd + (ii >> 4), j = ii & 15;
  const unsigned tag = *(const GAS unsigned*)(ws + WS_EPOCH) + 1u;
  const rsrc_t rx = mkrsrc(x, S_ * D_ * 2), rws = mkrsrc(ws, WS_USED);
  STAMP(0);

  if (wave == 0) {
    if (ii < 20) mod_group(mkrsrc(cvec, D_ * 2), mkrsrc(wmod, D_ * 6 * D_ * 2), ws, 20 * xcd + ii, lane, tag);
  } else if (wave < 3) {
    const int tok = 128 * mb + 8 * j + 4 * (wave - 1) + (lane >> 4);
    u32x4 xv[5];
#pragma unroll
    for (int i = 0; i < 5; ++i) xv[i] = ld16(rx, tok * 1280 + ((lane & 15) + 16 * i) * 16, 0);
    float s = 0.f;
#pragma unroll
    for (int i = 0; i < 5; ++i)
#pragma unroll
      for (int e = 0; e < 4; ++e) s += blo(xv[i][e]) + bhi(xv[i][e]);
    const float mean = sum16(s) * (1.0f / D_);
    float q = 0.f;
#pragma unroll
    for (int i = 0; i < 5; ++i)
#pragma unroll
      for (int e = 0; e < 4; ++e) { const float a = blo(xv[i][e]) - mean, b = bhi(xv[i][e]) - mean; q = fmaf(a, a, q); q = fmaf(b, b, q); }
    const float var = sum16(q) * (1.0f / D_);
    const float mq = bf16r(mean), rden = 1.0f / bf16r(__builtin_sqrtf(bf16r(bf16r(var) + 1e-5f)));
    const int srcl = 4 * ((lane >> 2) + 16 * (lane & 3));
    const int tb = 4 * mb + (j >> 2), t32 = 8 * (j & 3) + 4 * (wave - 1) + (lane & 3);
#pragma unroll
    for (int i = 0; i < 5; ++i) {
      u32x4 o;
#pragma unroll
      for (int e = 0; e < 4; ++e) {
        const unsigned d = pk2(blo(xv[i][e]) - mq, bhi(xv[i][e]) - mq);
        o[e] = (unsigned)__builtin_amdgcn_ds_bpermute(srcl, (int)pk2(blo(d) * rden, bhi(d) * rden));
      }
      st16_sc1(o, rws, WS_XN + tb * T32_D + ((lane >> 2) + 16 * i) * 512 + t32 * 16, 0);
    }
    VMWAIT(0);
    if (lane == 0) st_u64(ws, flagoff(FL_XN0 + wave - 1, mb, j), flagval(tag));
  } else {
    const rsrc_t rw = mkrsrc(wqkv, D_ * 3 * D_ * 2);
    unsigned vo[15];
#pragma unroll
    for (int p = 0; p < 15; ++p) {
      const int idx = 64 * p + lane, rq = idx / 60, rem = idx - 60 * rq;
      int q, ch;
      if (rem < 48) { q = (rem >> 2) & 3; ch = 4 * (rem >> 4) + (rem & 3); } else { const int r2 = rem - 48; q = r2 / 3; ch = 12 + r2 - 3 * q; }
      const int seg = ch / 5;
      vo[p] = (4 * rq + q) * 3840 + seg * 1280 + 80 * j + (ch - 5 * seg) * 16;
    }
    asm volatile("s_nop 4" ::: "memory");
    for (int st = 0; st < 10; ++st)
#pragma unroll
      for (int p = 0; p < 15; ++p) dma16(rw, vo[p], st * 64 * 3840, L0 + L1_W + (st * 15 + p) * 1024);
    VMWAIT(0);
  }
  STAMP(1);
  wait_flags(ws, flagoff(FL_XN0 + (lane >> 4 & 1), mb, lane & 15), lane < 32, tag);
  u32x4 xf[40];
#pragma unroll
  for (int ks = 0; ks < 40; ++ks) xf[ks] = ld16_sc1(rws, WS_XN + (4 * mb + wave) * T32_D + 16 * lane, ks * 1024);
  MEMFENCE();
  {
    unsigned gv[3];
    const int g0 = 160 * wave + lane;
    for (unsigned spins = 0;;) {
      bool ok = true;
#pragma unroll
      for (int k = 0; k < 3; ++k) { u64 g = (u64)tag << 32; if (k < 2 || lane < 32) g = ld_u64(ws, WS_GRAN + (g0 + 64 * k) * 8); gv[k] = (unsigned)g; ok &= (unsigned)(g >> 32) == tag; }
      if (__all(ok)) break;
      if (spin_fail(spins, ws, tag)) break;
    }
#pragma unroll
    for (int k = 0; k < 3; ++k) if (k < 2 || lane < 32) {
      const int g = g0 + 64 * k;
      f32x2 v = {blo(gv[k]), bhi(gv[k])};
      if (g >= 320) { v.x = bf16r(1.0f + v.x); v.y = bf16r(1.0f + v.y); }
      *(LAS f32x2*)(size_t)(L0 + (g >= 320 ? L1_VS + (g - 320) * 8 : L1_VA + g * 8)) = v;
    }
  }
  BAR();
  STAMP(2);

  f32x16 acc1[4];
#pragma unroll
  for (int b = 0; b < 4; ++b) acc1[b] = (f32x16){};
  {
    const int h = lane >> 5;
    const unsigned wb = L0 + L1_W + 2 * h * 960 + ((lane & 15) >> 2) * 64 + ((lane >> 4) & 1) * 32 + (lane & 3) * 8;
    const unsigned wb3 = L0 + L1_W + 2 * h * 960 + 768 + ((lane & 15) >> 2) * 48 + ((lane >> 4) & 1) * 32 + (lane & 3) * 8;
    const unsigned vb = L0 + L1_VS + h * 32;
    s16x8 w[2][4]; u32x4 hf[2]; f32x4 vs[2][2], va[2][2];
    auto readw = [&](int ks, int b) { const unsigned a = (b < 3 ? wb + b * 256 : wb3) + ks * 3840; return trfrag(a, a + 960); };
    auto readv = [&](int ks, int buf, int part) {
      if (part == 0) { vs[buf][0] = lds_f4(vb + ks * 64); vs[buf][1] = lds_f4(vb + ks * 64 + 16); }
      else { va[buf][0] = lds_f4(vb + 2560 + ks * 64); va[buf][1] = lds_f4(vb + 2560 + ks * 64 + 16); } };
    auto xform = [&](int ks, int buf, int e) {
      const unsigned v = xf[ks][e];
      return pk2(fmaf(blo(v), vs[buf][e >> 1][(e & 1) * 2], va[buf][e >> 1][(e & 1) * 2]), fmaf(bhi(v), vs[buf][e >> 1][(e & 1) * 2 + 1], va[buf][e >> 1][(e & 1) * 2 + 1])); };
#pragma unroll
    for (int b = 0; b < 4; ++b) w[0][b] = readw(0, b);
    readv(0, 0, 0); readv(0, 0, 1); readv(1, 1, 0); readv(1, 1, 1);
#pragma unroll
    for (int e = 0; e < 4; ++e) hf[0][e] = xform(0, 0, e);
    SB();
#pragma unroll
    for (int ks = 0; ks < 40; ++ks) {
      const int c = ks & 1, n = c ^ 1;
#pragma unroll
      for (int b = 0; b < 4; ++b) {
        acc1[b] = mfma32(w[c][b], hf[c], acc1[b]);
        SB();
        if (ks + 1 < 40) {
          w[n][b] = readw(ks + 1, b);
          hf[n][b] = xform(ks + 1, n, b);
        }
        if (ks + 2 < 40 && b >= 2) readv(ks + 2, c, b - 2);
        SB();
      }
    }
  }
  BAR();
  STAMP(3);

  u32x4 qa0, qa1, qb0, qb1;
  {
    const int h = lane >> 5, t = lane & 31;
    u32x2 P[15];
#pragma unroll
    for (int i = 0; i < 15; ++i) { const f32x16& a = acc1[i >> 2]; const int r = 4 * (i & 3); P[i] = (u32x2){pk2(a[r], a[r + 1]), pk2(a[r + 2], a[r + 3])}; }
    const u32x2 one = {0x00003f80u, 0u}, zero = {0u, 0u};
    auto cat = [](u32x2 a, u32x2 b) { return (u32x4){a[0], a[1], b[0], b[1]}; };
    auto sel = [&](u32x2 a1, u32x2 a0) { return (u32x2){h ? a1[0] : a0[0], h ? a1[1] : a0[1]}; };
    qa0 = cat(P[0], P[1]);
    qa1 = cat(sel(zero, P[2]), sel(zero, one));
    qb0 = cat(sel(P[2], P[3]), sel(P[3], P[4]));
    qb1 = cat(sel(P[4], zero), sel(one, zero));
    const unsigned row = L0 + L2_KV + (256 + 32 * wave + t) * 48;
    lds_st4(row + 16 * h, cat(P[5], P[6]));
    lds_st4(row + KV_IMG + 16 * (h ^ 1), cat(sel(P[7], P[8]), sel(P[8], P[9])));
    lds_st4(row + h * KV_IMG + 32, cat(sel(P[9], P[7]), zero));
    lds_st4(row + 2 * KV_IMG + 16 * h, cat(P[10], P[11]));
    lds_st4(row + 3 * KV_IMG + 16 * (h ^ 1), cat(sel(P[12], P[13]), sel(P[13], P[14])));
    lds_st4(row + (2 + h) * KV_IMG + 32, cat(sel(P[14], P[12]), one));
    asm volatile("s_waitcnt lgkmcnt(0)" ::: "memory");
#pragma unroll
    for (int u = 0; u < 6; ++u) {
      const int i = 64 * u + lane, a = i / 96, o = (i - 96 * a) * 16;
      const u32x4 v = lds_u4(L0 + L2_KV + a * KV_IMG + (256 + 32 * wave) * 48 + o);
      st16_sc1(v, rws, WS_KV + ((mb * 16 + j) * 4 + a) * 6144 + wave * 1536 + o, 0);
    }
    VMWAIT(0);
  }
  BAR();
  if (tid == 0) st_u64(ws, flagoff(FL_KV, mb, j), flagval(tag));
  STAMP(4);
  if (wave == 0) {
    wait_flags(ws, flagoff(FL_KV, mb - 1 - (lane & 1) < 0 ? mb : mb - 1 - (lane & 1), j), lane < 2, tag);
  } else if (wave == 1 || (wave == 2 && ii < 8)) {
    mod_group(mkrsrc(cvec, D_ * 2), mkrsrc(wmod, D_ * 6 * D_ * 2), ws, 160 + 40 * xcd + (wave == 1 ? ii : 32 + ii), lane, tag);
  }
  BAR();
  STAMP(5);
  {
    u32x4 hv[12];
#pragma unroll
    for (int u = 0; u < 12; ++u) {
      const int i = 256 * u + tid, blk = i / 1536, r = i - 1536 * blk, a = r / 384, o = (r - 384 * a) * 16;
      const int src = mb - 2 + blk;
      const bool lastc = (o % 48) == 32;
      u32x4 v = {0u, 0u, (lastc && a < 2) ? 0x0000f149u : 0u, 0u};
      if (src >= 0) v = ld16_sc1(rws, WS_KV + ((src * 16 + j) * 4 + a) * 6144 + o, 0);
      hv[u] = v;
    }
#pragma unroll
    for (int u = 0; u < 12; ++u) {
      const int i = 256 * u + tid, blk = i / 1536, r = i - 1536 * blk, a = r / 384, o = (r - 384 * a) * 16;
      lds_st4(L0 + L2_KV + a * KV_IMG + blk * 6144 + o, hv[u]);
    }
  }
  BAR();
  STAMP(6);
  const int th = j & 1, nt = j >> 1;
  const rsrc_t rwo = mkrsrc(wo, D_ * D_ * 2);
  auto wo_issue = [&](int ks, unsigned ldsbase, int piece) {
    auto rowmap = [&](int g, int t, int q) { const int k = 32 * ks + 8 * g + 4 * t + q, hp = k / 40, r = k - 40 * hp; return 40 * hp + (r < 20 ? r : r < 24 ? r + 16 : r - 4); };
    dma16(rwo, w80_voff(piece, lane, nt, rowmap), 0, ldsbase + piece * 1024);
  };
  for (int ks = wave; ks < L3_NPRE; ks += 4)
#pragma unroll
    for (int p = 0; p < 5; ++p) wo_issue(ks, L0 + L3_W0 + ks * 5120, p);

  {
    const int h = lane >> 5, t = lane & 31;
    f32x16 mfirst, mdiag;
#pragma unroll
    for (int r = 0; r < 16; ++r) { const int key = (r & 3) + 8 * (r >> 2) + 4 * h; mfirst[r] = key > t ? 0.f : NEGBIG; mdiag[r] = key > t ? NEGBIG : 0.f; }
    f32x16 oacc[2];
    float linv[2];
#pragma unroll
    for (int hd = 0; hd < 2; ++hd) {
      const unsigned krow = L0 + L2_KV + hd * KV_IMG + (32 * wave + t) * 48;
      const unsigned k0 = krow + 16 * (h ^ hd), k1 = krow + 32;
      const u32x4 q0 = hd ? qb0 : qa0, q1 = hd ? qb1 : qa1;
      f32x16 sc[9];
#pragma unroll
      for (int kb = 0; kb < 9; ++kb) {
        const u32x4 a0 = lds_u4(k0 + kb * 1536), a1 = lds_u4(k1 + kb * 1536);
        const f32x16 c0 = kb == 0 ? mfirst : kb == 8 ? mdiag : (f32x16){};
        sc[kb] = mfma32u(a1, q1, mfma32u(a0, q0, c0));
      }
      float m = sc[0][0];
#pragma unroll
      for (int kb = 0; kb < 9; ++kb)
#pragma unroll
        for (int r = (kb == 0 ? 1 : 0); r < 16; ++r) m = fmaxf(m, sc[kb][r]);
      m = xmax32(m);
      const float mc = -m * SCORE_C;
      const unsigned vrow = L0 + L2_KV + (2 + hd) * KV_IMG + (32 * wave + 4 * h + ((lane & 15) >> 2)) * 48 + ((lane >> 4) & 1) * 32 + (lane & 3) * 8;
      f32x16 o = {};
#pragma unroll
      for (int kb = 0; kb < 9; ++kb) {
#pragma unroll
        for (int s = 0; s < 2; ++s) {
          u32x4 pf;
#pragma unroll
          for (int e = 0; e < 4; ++e) pf[e] = pk2(exp2_(fmaf(sc[kb][8 * s + 2 * e], SCORE_C, mc)), exp2_(fmaf(sc[kb][8 * s + 2 * e + 1], SCORE_C, mc)));
          const unsigned va = vrow + (32 * kb + 16 * s) * 48;
          o = mfma32(trfrag(va, va + 8 * 48), pf, o);
        }
      }
      const auto lr = __builtin_amdgcn_permlane32_swap(__float_as_uint(o[8]), __float_as_uint(o[8]), false, false);
      linv[hd] = 1.0f / __uint_as_float(lr[1]);
      oacc[hd] = o;
    }
    const unsigned ob = WS_O + (4 * mb + wave) * T32_D + 5 * j * 512;
    u32x4 va, vb2, vc;
#pragma unroll
    for (int e = 0; e < 4; ++e) { va[e] = pk2(oacc[0][2 * e] * linv[0], oacc[0][2 * e + 1] * linv[0]); vb2[e] = pk2(oacc[1][2 * e] * linv[1], oacc[1][2 * e + 1] * linv[1]); }
    vc = (u32x4){pk2(oacc[0][8] * linv[0], oacc[0][9] * linv[0]), pk2(oacc[0][10] * linv[0], oacc[0][11] * linv[0]),
                 pk2(oacc[1][8] * linv[1], oacc[1][9] * linv[1]), pk2(oacc[1][10] * linv[1], oacc[1][11] * linv[1])};
    st16_sc1(va, rws, ob + 16 * lane, 0);
    st16_sc1(vb2, rws, ob + 3 * 512 + 16 * lane, 0);
    if (lane < 32) st16_sc1(vc, rws, ob + 2 * 512 + 16 * lane, 0);
    VMWAIT(0);
  }
  BAR();
  if (tid == 0) st_u64(ws, flagoff(FL_O, mb, j), flagval(tag));
  STAMP(7);
  const int t16 = lane & 15, g4 = lane >> 4;
  const int tok3 = 128 * mb + 64 * th + 16 * wave + t16;
  u32x2 xres[5];
#pragma unroll
  for (int p = 0; p < 5; ++p) xres[p] = ld8(rx, tok3 * 1280 + 160 * nt + 32 * ct_of(p, nt & 1) + 8 * g4, 0);
  if (wave == 0) {
    unsigned gv[2];
    const int i0 = lane, i1 = 64 + lane;
    auto goff = [&](int i) { const int kind = i / 40, r = i - 40 * kind; return WS_GRAN + ((kind == 0 ? 640 : kind == 1 ? 1280 : 1600) + 40 * nt + r) * 8; };
    for (unsigned spins = 0;;) {
      const u64 a = ld_u64(ws, goff(i0)); u64 b = (u64)tag << 32; if (lane < 56) b = ld_u64(ws, goff(i1));
      gv[0] = (unsigned)a; gv[1] = (unsigned)b;
      if (__all((unsigned)(a >> 32) == tag && (unsigned)(b >> 32) == tag)) break;
      if (spin_fail(spins, ws, tag)) break;
    }
#pragma unroll
    for (int k = 0; k < 2; ++k) if (k == 0 || lane < 56) {
      const int i = 64 * k + lane, kind = i / 40, r = i - 40 * kind;
      const int ct = r >> 3, p = (nt & 1) ? (ct == 0 ? 4 : ct - 1) : ct;
      f32x2 v = {blo(gv[k]), bhi(gv[k])};
      if (kind == 1) { v.x = bf16r(1.0f + v.x); v.y = bf16r(1.0f + v.y); }
      *(LAS f32x2*)(size_t)(L0 + L3_VEC + kind * 320 + p * 64 + (r & 7) * 8) = v;
    }
    wait_flags(ws, flagoff(FL_O, mb, lane & 15), lane < 16, tag);
  } else if (wave < 3) {
    const int unit = bid;
    if (unit < 216) {
      const rsrc_t rw = mkrsrc(unit < 108 ? wg : wu, D_ * F_ * 2);
      const int cu = unit < 108 ? unit : unit - 108;
      u32x4 w[10];
#pragma unroll
      for (int r = 0; r < 10; ++r) w[r] = ld16(rw, (10 * lane + r) * 3456, 32 * cu + 16 * (wave - 1));
      unsigned sbv[5], cbv[5];
      sweep<5>(ws, WS_GRAN + (960 + 5 * lane) * 8, 8, tag, sbv);
      sweep<5>(ws, WS_GRAN + (1280 + 5 * lane) * 8, 8, tag, cbv);
      float u[8], v[8];
#pragma unroll
      for (int e = 0; e < 8; ++e) { u[e] = 0.f; v[e] = 0.f; }
#pragma unroll
      for (int r = 0; r < 10; ++r) {
        const float s1 = bf16r(1.0f + ((r & 1) ? bhi(cbv[r >> 1]) : blo(cbv[r >> 1]))), s0 = (r & 1) ? bhi(sbv[r >> 1]) : blo(sbv[r >> 1]);
#pragma unroll
        for (int e = 0; e < 4; ++e) {
          const float a = blo(w[r][e]), b = bhi(w[r][e]);
          u[2 * e] = fmaf(s1, a, u[2 * e]); u[2 * e + 1] = fmaf(s1, b, u[2 * e + 1]); v[2 * e] = fmaf(s0, a, v[2 * e]); v[2 * e + 1] = fmaf(s0, b, v[2 * e + 1]);
        }
      }
      float pv = 0.f;
#pragma unroll
      for (int e = 0; e < 8; ++e) { const float a = sum64(u[e]), b = sum64(v[e]); pv = lane == e ? a : lane == 8 + e ? b : pv; }
      if (lane < 16) st_u64(ws, WS_UV + (unit * 32 + (lane >> 3) * 16 + 8 * (wave - 1) + (lane & 7)) * 8, ((u64)tag << 32) | __float_as_uint(pv));
    }
  } else {
    for (int ks = L3_NPRE; ks < 20; ++ks)
#pragma unroll
      for (int p = 0; p < 5; ++p) wo_issue(ks, L0 + L3_W1 + (ks - L3_NPRE) * 5120, p);
    VMWAIT(0);
  }
  BAR();
  STAMP(8);

  const unsigned b80_0 = g4 * 1024 + ((lane & 15) >> 2) * 64 + (g4 & 1) * 32 + (lane & 3) * 8;
  const unsigned b80_1 = b80_0 ^ 32u;
  const unsigned b80_2 = 4096 + 8 * lane;
  auto w80 = [&](unsigned kbase, int p) { const unsigned a = kbase + (p == 4 ? b80_2 : ((p & 1) ? b80_1 : b80_0) + (p >> 1) * 256); return trfrag(a, a + 512); };

  u32x2 x1p[5];
  {
    u32x4 of[20];
    const unsigned ofb = WS_O + (4 * mb + 2 * th + (wave >> 1)) * T32_D + g4 * 512 + ((wave & 1) * 16 + t16) * 16;
#pragma unroll
    for (int s = 0; s < 20; ++s) of[s] = ld16_sc1(rws, ofb, s * 2048);
    MEMFENCE();
    f32x4 acc[5];
#pragma unroll
    for (int p = 0; p < 5; ++p) acc[p] = (f32x4){};
    auto kb3 = [&](int s) { return L0 + (s < L3_NPRE ? L3_W0 + s * 5120 : L3_W1 + (s - L3_NPRE) * 5120); };
    s16x8 w[2][5];
#pragma unroll
    for (int p = 0; p < 5; ++p) w[0][p] = w80(kb3(0), p);
    SB();
#pragma unroll
    for (int s = 0; s < 20; ++s) {
#pragma unroll
      for (int p = 0; p < 5; ++p) {
        acc[p] = mfma16(w[s & 1][p], of[s], acc[p]);
        SB();
        if (s + 1 < 20) w[(s & 1) ^ 1][p] = w80(kb3(s + 1), p);
        SB();
      }
    }
    STAMP(9);
    float xv[5][4]; float s1 = 0.f;
    u32x2 yp[5];
#pragma unroll
    for (int p = 0; p < 5; ++p) {
      const f32x4 ga = lds_f4(L0 + L3_VEC + p * 64 + g4 * 16), sb1 = lds_f4(L0 + L3_VEC + 320 + p * 64 + g4 * 16);
#pragma unroll
      for (int r = 0; r < 4; ++r) {
        const float xr = (r & 1) ? bhi(xres[p][r >> 1]) : blo(xres[p][r >> 1]);
        xv[p][r] = bf16r(xr + bf16r(ga[r] * bf16r(acc[p][r])));
        s1 += xv[p][r];
      }
      x1p[p] = (u32x2){pk2(xv[p][0], xv[p][1]), pk2(xv[p][2], xv[p][3])};
      yp[p] = (u32x2){pk2(xv[p][0] * sb1[0], xv[p][1] * sb1[1]), pk2(xv[p][2] * sb1[2], xv[p][3] * sb1[3])};
    }
    const float mt = xsum32(xsum16(s1)) * (1.0f / 80);
    float m2 = 0.f;
#pragma unroll
    for (int p = 0; p < 5; ++p)
#pragma unroll
      for (int r = 0; r < 4; ++r) { const float d = xv[p][r] - mt; m2 = fmaf(d, d, m2); }
    m2 = xsum32(xsum16(m2));
    if (lane < 16) st_u64(ws, WS_STAT + (((mb * 2 + th) * 8 + nt) * 64 + 16 * wave + t16) * 8, ((u64)__float_as_uint(m2) << 32) | __float_as_uint(mt));
    const unsigned yb = WS_Y + (4 * mb + 2 * th + (wave >> 1)) * T32_D + 10 * nt * 512 + ((wave & 1) * 16 + t16) * 16;
#pragma unroll
    for (int pp = 0; pp < 3; ++pp) {
      const int pa = 2 * pp, pb = pp < 2 ? 2 * pp + 1 : 4;
      const auto r0 = __builtin_amdgcn_permlane16_swap(yp[pa][0], yp[pb][0], false, false), r1 = __builtin_amdgcn_permlane16_swap(yp[pa][1], yp[pb][1], false, false);
      const int ct = (g4 & 1) ? ct_of(pb, nt & 1) : ct_of(pa, nt & 1);
      if (pp < 2 || !(g4 & 1)) st16_sc1((u32x4){r0[0], r1[0], r0[1], r1[1]}, rws, yb + (2 * ct + (g4 >> 1)) * 512, 0);
    }
    VMWAIT(0);
  }
  BAR();
  if (tid == 0) st_u64(ws, flagoff(FL_Y, mb, j), flagval(tag));
  STAMP(10);

  const int nu = j < 12 ? 7 : 6, u0 = j < 12 ? 7 * j : 84 + 6 * (j - 12), uodd = u0 & 1;
  const rsrc_t rgu = mkrsrc(wave < 2 ? wg : wu, D_ * F_ * 2);
  auto gu_voff = [&](int piece  , int isup) {
    const int idx = 64 * piece + lane, rq = idx / 56, rem = idx - 56 * rq;
    int q, p, hf;
    if (rem < 48) { q = (rem >> 2) & 3; const int c = (rem & 3) ^ (isup ? 2 : 0); p = 2 * (rem >> 4) + (c >> 1); hf = c & 1; } else { q = (rem - 48) >> 1; p = 6; hf = rem & 1; }
    return (unsigned)((4 * rq + q) * 3456 + 32 * (u0 + unit_of(p, uodd, nu)) + 16 * hf);
  };
  unsigned guo[7];
#pragma unroll
  for (int i = 0; i < 7; ++i) guo[i] = gu_voff(7 * (wave & 1) + i, wave >> 1);
  auto gu_issue = [&](int st) {
    const unsigned base = L0 + (st % 5) * L4_STAGE + (wave >> 1) * L4_U + (wave & 1) * 7168;
#pragma unroll
    for (int i = 0; i < 7; ++i) dma16(rgu, guo[i], st * 64 * 3456, base + i * 1024);
  };
  float rs4, nm4;
  u32x4 yf[40];
  if (wave == 0) {
    unsigned uv[7];
    for (unsigned spins = 0;;) {
      bool ok = true;
#pragma unroll
      for (int p = 0; p < 7; ++p) { const u64 g = ld_u64(ws, WS_UV + (((lane >> 5) * 108 + u0 + unit_of(p, uodd, nu)) * 32 + (lane & 31)) * 8); uv[p] = (unsigned)g; ok &= (unsigned)(g >> 32) == tag; }
      if (__all(ok)) break;
      if (spin_fail(spins, ws, tag)) break;
    }
#pragma unroll
    for (int p = 0; p < 7; ++p) *(LAS unsigned*)(size_t)(L0 + L4_UV + (p * 64 + lane) * 4) = uv[p];
    wait_flags(ws, flagoff(FL_Y, mb, lane & 15), lane < 16, tag);
  } else {
    for (int st = 0; st < 5; ++st) gu_issue(st);
  }
  BAR();
  STAMP(11);
  {
    const int h = lane >> 5, T = 32 * wave + (lane & 31);
    u32x2 stv[4];
#pragma unroll
    for (int i = 0; i < 4; ++i) stv[i] = ld8_sc1(rws, WS_STAT + (((mb * 2 + (T >> 6)) * 8 + 4 * h + i) * 64 + (T & 63)) * 8, 0);
#pragma unroll
    for (int ks = 0; ks < 40; ++ks) yf[ks] = ld16_sc1(rws, WS_Y + (4 * mb + wave) * T32_D + 16 * lane, ks * 1024);
    MEMFENCE();
    if (wave == 0) for (int st = 0; st < 5; ++st) gu_issue(st);
    float ms = 0.f;
#pragma unroll
    for (int i = 0; i < 4; ++i) ms += __uint_as_float(stv[i][0]);
    const float mean = xsum32(ms) * 0.125f;
    float m2 = 0.f;
#pragma unroll
    for (int i = 0; i < 4; ++i) { const float d = __uint_as_float(stv[i][0]) - mean; m2 += __uint_as_float(stv[i][1]) + 80.0f * d * d; }
    const float var = xsum32(m2) * (1.0f / D_);
    rs4 = 1.0f / bf16r(__builtin_sqrtf(bf16r(bf16r(var) + 1e-5f)));
    nm4 = -rs4 * bf16r(mean);
  }
  f32x16 acc4[7];
#pragma unroll
  for (int b = 0; b < 7; ++b) acc4[b] = (f32x16){};
  {
    const int h = lane >> 5, blk = (lane >> 4) & 1, q = (lane & 15) >> 2;
    const unsigned c0 = L0 + blk * L4_U + 2 * h * 896 + q * 64 + blk * 32 + (lane & 3) * 8, c1 = c0 ^ 32u;
    const unsigned c2 = L0 + blk * L4_U + 2 * h * 896 + 768 + q * 32 + (lane & 3) * 8;
    auto readw = [&](int ks, int p) {
      const unsigned a = (p == 6 ? c2 : ((p & 1) ? c1 : c0) + (p >> 1) * 256) + ((ks >> 2) % 5) * L4_STAGE + (ks & 3) * 4 * 896;
      return trfrag(a, a + 896); };
    s16x8 w[2][7];
    VMWAIT(0);
    BAR();
#pragma unroll
    for (int p = 0; p < 7; ++p) w[0][p] = readw(0, p);
    SB();
#pragma unroll
    for (int ks = 0; ks < 40; ++ks) {
      const int c = ks & 1, n = c ^ 1;
      if ((ks & 3) == 3 && ks + 1 < 40) {
        const int st = ks >> 2;
        if (st == 4 || st == 5) { VMWAIT(21); } else if (st == 6) { VMWAIT(14); } else if (st == 7) { VMWAIT(7); } else if (st == 8) { VMWAIT(0); }
        BAR();
        if (st + 5 < 10) gu_issue(st + 5);
        SB();
      }
#pragma unroll
      for (int p = 0; p < 7; ++p) {
        acc4[p] = mfma32(w[c][p], yf[ks], acc4[p]);
        SB();
        if (ks + 1 < 40) w[n][p] = readw(ks + 1, p);
        SB();
      }
    }
  }
  STAMP(12);
  {
    const int h = lane >> 5;
    const unsigned ab = WS_ACT + (4 * mb + wave) * T32_F + 16 * lane;
#pragma unroll
    for (int p = 0; p < 7; ++p) {
      if (p < 6 || nu == 7) {
        const unsigned tb = L0 + L4_UV + p * 256 + 16 * h;
        float a[8];
#pragma unroll
        for (int half = 0; half < 2; ++half) {
          const f32x4 ug = lds_f4(tb + 32 * half), vg = lds_f4(tb + 64 + 32 * half), uu = lds_f4(tb + 128 + 32 * half), vu = lds_f4(tb + 192 + 32 * half);
#pragma unroll
          for (int r = 0; r < 4; ++r) {
            const float g = fmaf(rs4, acc4[p][4 * half + r], fmaf(nm4, ug[r], vg[r]));
            const float u = fmaf(rs4, acc4[p][8 + 4 * half + r], fmaf(nm4, uu[r], vu[r]));
            a[4 * half + r] = siluf(g) * u;
          }
        }
        st16_sc1((u32x4){pk2(a[0], a[1]), pk2(a[2], a[3]), pk2(a[4], a[5]), pk2(a[6], a[7])}, rws, ab + (u0 + unit_of(p, uodd, nu)) * 1024, 0);
      }
    }
    VMWAIT(0);
  }
  BAR();
  if (tid == 0) st_u64(ws, flagoff(FL_ACT, mb, j), flagval(tag));
  STAMP(13);

  const rsrc_t rwd = mkrsrc(wd, F_ * D_ * 2);
  unsigned wdo[5];
#pragma unroll
  for (int p = 0; p < 5; ++p) wdo[p] = w80_voff(p, lane, nt, [](int g, int t, int q) { return 16 * (g >> 1) + 8 * t + 4 * (g & 1) + q; });
  unsigned wdg[8];
#pragma unroll
  for (int i = 0; i < 8; ++i) {
    const int pc = min(8 * wave + i, 29), kl = pc / 5;
    wdg[i] = w80_voff(pc - 5 * kl, lane, nt, [](int g, int t, int q) { return 16 * (g >> 1) + 8 * t + 4 * (g & 1) + q; }) + kl * 32 * 1280;
  }
  auto wd_issue = [&](int ks) {
#pragma unroll
    for (int p = 0; p < 5; ++p) dma16(rwd, wdo[p], ks * 32 * 1280, L0 + (ks % L5_NRING) * 5120 + p * 1024);
  };
  if (wave == 0) {
    wait_flags(ws, flagoff(FL_ACT, mb, lane & 15), lane < 16, tag);
    if (bid == 0) {
#pragma unroll
      for (int k = 0; k < 4; ++k) wait_flags(ws, flagoff(FL_KV, 4 * k + (lane >> 4), lane & 15), true, tag);
      if (lane == 0) __hip_atomic_store((GAS unsigned*)(ws + WS_EPOCH), tag, __ATOMIC_RELAXED, __HIP_MEMORY_SCOPE_AGENT);
    }
  } else {
    for (int ks = wave - 1; ks < L5_NRING; ks += 3) wd_issue(ks);
  }
  BAR();
  STAMP(14);
  {
    u32x4 af[54];
    const unsigned afb = WS_ACT + (4 * mb + 2 * th + (wave >> 1)) * T32_F + g4 * 512 + ((wave & 1) * 16 + t16) * 16;
#pragma unroll
    for (int s = 0; s < 54; ++s) af[s] = ld16_sc1(rws, afb, s * 2048);
    MEMFENCE();
    f32x4 acc[5];
#pragma unroll
    for (int p = 0; p < 5; ++p) acc[p] = (f32x4){};
    s16x8 w[2][5];
    VMWAIT(54);
    BAR();
#pragma unroll
    for (int p = 0; p < 5; ++p) w[0][p] = w80(L0, p);
    SB();
#pragma unroll
    for (int s = 0; s < 54; ++s) {
      if (s % 6 == 5 && s + 1 < 54) {
        const int G = s / 6;
        if (G + 1 >= 5) { if (G + 1 == 5) { VMWAIT(24); } else if (G + 1 == 6) { VMWAIT(16); } else if (G + 1 == 7) { VMWAIT(8); } else { VMWAIT(0); } }
        BAR();
        if (G < 4) {
#pragma unroll
          for (int i = 0; i < 8; ++i) dma16(rwd, wdg[i], (30 + 6 * G) * 32 * 1280, L0 + 6 * G * 5120 + min(8 * wave + i, 29) * 1024);
        }
        SB();
      }
#pragma unroll
      for (int p = 0; p < 5; ++p) {
        acc[p] = mfma16(w[s & 1][p], af[s], acc[p]);
        SB();
        if (s + 1 < 54) w[(s & 1) ^ 1][p] = w80(L0 + ((s + 1) % L5_NRING) * 5120, p);
        SB();
      }
    }
    STAMP(15);
    u32x2 op[5];
#pragma unroll
    for (int p = 0; p < 5; ++p) {
      const f32x4 gb = lds_f4(L0 + L3_VEC + 640 + p * 64 + g4 * 16);
      float o[4];
#pragma unroll
      for (int r = 0; r < 4; ++r) {
        const float xr = (r & 1) ? bhi(x1p[p][r >> 1]) : blo(x1p[p][r >> 1]);
        o[r] = xr + bf16r(gb[r] * bf16r(acc[p][r]));
      }
      op[p] = (u32x2){pk2(o[0], o[1]), pk2(o[2], o[3])};
    }
    const rsrc_t ro = mkrsrc(out, S_ * D_ * 2);
#pragma unroll
    for (int pp = 0; pp < 3; ++pp) {
      const int pa = 2 * pp, pb = pp < 2 ? 2 * pp + 1 : 4;
      const auto r0 = __builtin_amdgcn_permlane16_swap(op[pa][0], op[pb][0], false, false), r1 = __builtin_amdgcn_permlane16_swap(op[pa][1], op[pb][1], false, false);
      const int ct = (g4 & 1) ? ct_of(pb, nt & 1) : ct_of(pa, nt & 1);
      if (pp < 2 || !(g4 & 1))
        __builtin_amdgcn_raw_buffer_store_b128((u32x4){r0[0], r1[0], r0[1], r1[1]}, ro, tok3 * 1280 + 160 * nt + 32 * ct + 16 * (g4 >> 1), 0, 0);
    }
  }
  STAMP(16);
}

extern "C" void kernel_launch(void* const* d_in, const int* in_sizes, int n_in, void* d_out, int out_size, void* d_ws, size_t ws_size,
                              hipStream_t stream) {
  (void)in_sizes; (void)n_in; (void)out_size; (void)ws_size;
  adaln_block_kernel<<<256, 256, 0, stream>>>((const char*)d_in[0], (const char*)d_in[1], (const char*)d_in[2], (const char*)d_in[4], (const char*)d_in[5],
                                              (const char*)d_in[6], (const char*)d_in[7], (const char*)d_in[8], (char*)d_out, (char*)d_ws);
}
```

```cpp
#include <hip/hip_runtime.h>
#include <stdint.h>

#define LAS __attribute__((address_space(3)))
#define GAS __attribute__((address_space(1)))
#define DI __device__ __forceinline__
typedef float f32x2 __attribute__((ext_vector_type(2)));
typedef float f32x4 __attribute__((ext_vector_type(4)));
typedef float f32x16 __attribute__((ext_vector_type(16)));
typedef unsigned u32x2 __attribute__((ext_vector_type(2)));
typedef unsigned u32x4 __attribute__((ext_vector_type(4)));
typedef short s16x4 __attribute__((ext_vector_type(4)));
typedef short s16x8 __attribute__((ext_vector_type(8)));
typedef __bf16 bf16x2 __attribute__((ext_vector_type(2)));
typedef __bf16 bf16x8 __attribute__((ext_vector_type(8)));
typedef unsigned long long u64;
typedef __amdgpu_buffer_rsrc_t rsrc_t;

constexpr int S_ = 2048, D_ = 640, F_ = 1728;
constexpr int T32_D = 80 * 512;
constexpr int T32_F = 216 * 512;

constexpr int WS_EPOCH = 0;
constexpr int WS_TMO = 256;
constexpr int WS_FLAG = 4096;
constexpr int FL_XN0 = 0, FL_XN1 = 1, FL_KV = 2, FL_O = 3, FL_Y = 4, FL_ACT = 5;
constexpr int WS_GRAN = 256 * 1024;
constexpr int WS_UV = 320 * 1024;
constexpr int WS_STAT = 512 * 1024;
constexpr int WS_STAMP = 768 * 1024;
constexpr int WS_XN = 1 << 20;
constexpr int WS_KV = 4 << 20;
constexpr int WS_O = 12 << 20;
constexpr int WS_Y = 16 << 20;
constexpr int WS_ACT = 20 << 20;
constexpr int WS_USED = 28 << 20;

constexpr int LDS_BYTES = 163840;
constexpr int L1_W = 0;
constexpr int L1_VS = 153600, L1_VA = 156160;
constexpr int L2_KV = 0;
constexpr int KV_IMG = 384 * 48;
constexpr int L3_W0 = 4 * KV_IMG;
constexpr int L3_NPRE = 17;
constexpr int L3_W1 = 0;
constexpr int L3_VEC = 161 * 1000 + 24;
constexpr int L4_STAGE = 28800, L4_U = 14464;
constexpr int L4_UV = 144000;
constexpr int L5_NRING = 30;
static_assert(L3_W0 + L3_NPRE * 5120 <= L3_VEC && L3_VEC + 960 <= LDS_BYTES, "P3 LDS map");
static_assert(L5_NRING * 5120 <= L3_VEC, "P5 LDS map");
static_assert(L4_UV + 1792 <= L3_VEC, "P4 LDS map");

constexpr float LOG2E = 1.4426950408889634f;
constexpr float SCORE_C = 0.22360679774997896f * 1.4426950408889634f;
constexpr float NEGBIG = -1.0e30f;
constexpr unsigned SPIN_CAP = 1u << 15;

#define SB() __builtin_amdgcn_sched_barrier(0)
#define BAR() do { asm volatile("s_waitcnt lgkmcnt(0)" ::: "memory"); __builtin_amdgcn_s_barrier(); asm volatile("" ::: "memory"); } while (0)
#define VMWAIT(n) asm volatile("s_waitcnt vmcnt(" #n ")" ::: "memory")
#define MEMFENCE() asm volatile("" ::: "memory")

DI unsigned pk2(float a, float b) { bf16x2 v = __builtin_convertvector((f32x2){a, b}, bf16x2); return __builtin_bit_cast(unsigned, v); }
DI float blo(unsigned v) { return __uint_as_float(v << 16); }
DI float bhi(unsigned v) { return __uint_as_float(v & 0xffff0000u); }
DI float bf16r(float a) { return blo(pk2(a, a)); }
DI float rcpf_(float x) { return __builtin_amdgcn_rcpf(x); }
DI float exp2_(float x) { return __builtin_amdgcn_exp2f(x); }
DI float siluf(float x) { return x * rcpf_(1.0f + exp2_(-LOG2E * x)); }
template <int CTRL> DI float dpp_add(float x) { return x + __int_as_float(__builtin_amdgcn_update_dpp(0, __float_as_int(x), CTRL, 0xf, 0xf, true)); }
DI float sum16(float x) { x = dpp_add<0xB1>(x); x = dpp_add<0x4E>(x); x = dpp_add<0x141>(x); return dpp_add<0x140>(x); }
DI float xsum16(float x) { auto r = __builtin_amdgcn_permlane16_swap(__float_as_uint(x), __float_as_uint(x), false, false); return __uint_as_float(r[0]) + __uint_as_float(r[1]); }
DI float xsum32(float x) { auto r = __builtin_amdgcn_permlane32_swap(__float_as_uint(x), __float_as_uint(x), false, false); return __uint_as_float(r[0]) + __uint_as_float(r[1]); }
DI float xmax32(float x) { auto r = __builtin_amdgcn_permlane32_swap(__float_as_uint(x), __float_as_uint(x), false, false); return fmaxf(__uint_as_float(r[0]), __uint_as_float(r[1])); }
DI float sum64(float x) { return xsum32(xsum16(sum16(x))); }
DI rsrc_t mkrsrc(const void* p, unsigned bytes) { return __builtin_amdgcn_make_buffer_rsrc((void*)p, (short)0, (int)bytes, 0x00020000); }
DI u32x4 ld16_sc1(rsrc_t r, unsigned voff, unsigned soff) { return __builtin_amdgcn_raw_buffer_load_b128(r, voff, soff, 16); }
DI u32x4 ld16(rsrc_t r, unsigned voff, unsigned soff) { return __builtin_amdgcn_raw_buffer_load_b128(r, voff, soff, 0); }
DI u32x2 ld8(rsrc_t r, unsigned voff, unsigned soff) { return __builtin_amdgcn_raw_buffer_load_b64(r, voff, soff, 0); }
DI u32x2 ld8_sc1(rsrc_t r, unsigned voff, unsigned soff) { return __builtin_amdgcn_raw_buffer_load_b64(r, voff, soff, 16); }
DI void st16_sc1(u32x4 v, rsrc_t r, unsigned voff, unsigned soff) { __builtin_amdgcn_raw_buffer_store_b128(v, r, voff, soff, 16); }
DI void dma16(rsrc_t r, unsigned voff, unsigned soff, unsigned ldsaddr) {
  asm volatile("s_mov_b32 m0, %0\n\ts_nop 0\n\tbuffer_load_dwordx4 %1, %2, %3 offen lds" :: "s"(ldsaddr), "v"(voff), "s"(r), "s"(soff) : "memory");
}
DI u64 ld_u64(const char* ws, unsigned off) { return __hip_atomic_load((GAS u64*)(ws + off), __ATOMIC_RELAXED, __HIP_MEMORY_SCOPE_AGENT); }
DI void st_u64(char* ws, unsigned off, u64 v) { __hip_atomic_store((GAS u64*)(ws + off), v, __ATOMIC_RELAXED, __HIP_MEMORY_SCOPE_AGENT); }
DI u64 flagval(unsigned tag) { return ((u64)tag << 32) | (u64)(~tag); }
DI unsigned flagoff(int kind, int mb, int j) { return WS_FLAG + ((kind * 16 + mb) * 16 + j) * 128; }
DI bool spin_fail(unsigned& spins, char* ws, unsigned tag) {
  __builtin_amdgcn_s_sleep(1);
  if (++spins > SPIN_CAP) { __hip_atomic_store((GAS unsigned*)(ws + WS_TMO), tag, __ATOMIC_RELAXED, __HIP_MEMORY_SCOPE_AGENT); return true; }
  return (spins & 127u) == 0u && (unsigned)__builtin_amdgcn_readfirstlane(__hip_atomic_load((GAS unsigned*)(ws + WS_TMO), __ATOMIC_RELAXED, __HIP_MEMORY_SCOPE_AGENT)) == tag;
}
DI void wait_flags(char* ws, unsigned off, bool on, unsigned tag) {
  const u64 want = flagval(tag);
  for (unsigned spins = 0;;) {
    u64 v = want;
    if (on) v = ld_u64(ws, off);
    if (__all(v == want)) break;
    if (spin_fail(spins, ws, tag)) break;
  }
  MEMFENCE();
}
template <int N> DI void sweep(char* ws, unsigned off0, unsigned stride, unsigned tag, unsigned (&val)[N]) {
  for (unsigned spins = 0;;) {
    bool ok = true;
#pragma unroll
    for (int k = 0; k < N; ++k) { const u64 g = ld_u64(ws, off0 + k * stride); val[k] = (unsigned)g; ok &= (unsigned)(g >> 32) == tag; }
    if (__all(ok)) break;
    if (spin_fail(spins, ws, tag)) break;
  }
  MEMFENCE();
}
DI s16x8 trfrag(unsigned a0, unsigned a1) {
  const s16x4 lo = __builtin_amdgcn_ds_read_tr16_b64_v4i16((LAS s16x4*)(size_t)a0);
  const s16x4 hi = __builtin_amdgcn_ds_read_tr16_b64_v4i16((LAS s16x4*)(size_t)a1);
  return __builtin_shufflevector(lo, hi, 0, 1, 2, 3, 4, 5, 6, 7);
}
DI f32x4 lds_f4(unsigned a) { return *(LAS f32x4*)(size_t)a; }
DI u32x4 lds_u4(unsigned a) { return *(LAS u32x4*)(size_t)a; }
DI void lds_st4(unsigned a, u32x4 v) { *(LAS u32x4*)(size_t)a = v; }
DI f32x16 mfma32(s16x8 a, u32x4 b, f32x16 c) { return __builtin_amdgcn_mfma_f32_32x32x16_bf16(__builtin_bit_cast(bf16x8, a), __builtin_bit_cast(bf16x8, b), c, 0, 0, 0); }
DI f32x16 mfma32u(u32x4 a, u32x4 b, f32x16 c) { return __builtin_amdgcn_mfma_f32_32x32x16_bf16(__builtin_bit_cast(bf16x8, a), __builtin_bit_cast(bf16x8, b), c, 0, 0, 0); }
DI f32x4 mfma16(s16x8 a, u32x4 b, f32x4 c) { return __builtin_amdgcn_mfma_f32_16x16x32_bf16(__builtin_bit_cast(bf16x8, a), __builtin_bit_cast(bf16x8, b), c, 0, 0, 0); }

#define STAMP(i) do { } while (0)

DI void mod_group(rsrc_t rc, rsrc_t rm, char* ws, int grp, int lane, unsigned tag) {
  const u32x4 c4 = ld16(rc, 20 * lane, 0);
  const unsigned c1 = __builtin_amdgcn_raw_buffer_load_b32(rc, 20 * lane + 16, 0, 0);
  u32x4 w[10];
#pragma unroll
  for (int r = 0; r < 10; ++r) w[r] = ld16(rm, (10 * lane + r) * 7680, 16 * grp);
  const unsigned cc[5] = {c4[0], c4[1], c4[2], c4[3], c1};
  float acc[8];
#pragma unroll
  for (int e = 0; e < 8; ++e) acc[e] = 0.f;
#pragma unroll
  for (int r = 0; r < 10; ++r) {
    const float cv = (r & 1) ? bhi(cc[r >> 1]) : blo(cc[r >> 1]);
    const float sc = bf16r(siluf(cv));
#pragma unroll
    for (int e = 0; e < 4; ++e) { acc[2 * e] = fmaf(sc, blo(w[r][e]), acc[2 * e]); acc[2 * e + 1] = fmaf(sc, bhi(w[r][e]), acc[2 * e + 1]); }
  }
#pragma unroll
  for (int e = 0; e < 8; ++e) acc[e] = sum64(acc[e]);
  const unsigned p0 = pk2(acc[0], acc[1]), p1 = pk2(acc[2], acc[3]), p2 = pk2(acc[4], acc[5]), p3 = pk2(acc[6], acc[7]);
  const unsigned pv = lane == 0 ? p0 : lane == 1 ? p1 : lane == 2 ? p2 : p3;
  if (lane < 4) st_u64(ws, WS_GRAN + (4 * grp + lane) * 8, ((u64)tag << 32) | pv);
}

DI int ct_of(int p, int odd) { return odd ? (p < 4 ? p + 1 : 0) : p; }
DI int unit_of(int p, int odd, int nu) { const int u = odd ? (p < 6 ? p + 1 : 0) : p; return u < nu ? u : nu - 1; }

template <class RM> DI unsigned w80_voff(int piece, int lane, int nt, RM rowmap) {
  const int odd = nt & 1;
  int g, t, q, p, hf;
  if (piece < 4) { g = piece; t = lane >> 5; const int su = (lane >> 4) & 1; q = (lane >> 2) & 3; const int c = (lane & 3) ^ (2 * (piece & 1)); p = 2 * su + (c >> 1); hf = c & 1; }
  else { t = lane >> 5; g = (lane >> 3) & 3; q = (lane >> 1) & 3; p = 4; hf = lane & 1; }
  return rowmap(g, t, q) * 1280 + 160 * nt + 32 * ct_of(p, odd) + 16 * hf;
}

__global__ __launch_bounds__(256, 1) void adaln_block_kernel(const char* __restrict__ x, const char* __restrict__ wqkv, const char* __restrict__ wo,
                                                             const char* __restrict__ wg, const char* __restrict__ wu, const char* __restrict__ wd,
                                                             const char* __restrict__ cvec, const char* __restrict__ wmod, char* out, char* ws) {
  __shared__ __attribute__((aligned(16))) char smem[LDS_BYTES];
  const unsigned L0 = (unsigned)(size_t)(LAS char*)smem;
  const int tid = threadIdx.x, lane = tid & 63;
  const int wave = __builtin_amdgcn_readfirstlane(tid >> 6);
  const int bid = blockIdx.x, xcd = bid & 7, ii = bid >> 3, mb = 2 * xcd + (ii >> 4), j = ii & 15;
  const unsigned tag = *(const GAS unsigned*)(ws + WS_EPOCH) + 1u;
  const rsrc_t rx = mkrsrc(x, S_ * D_ * 2), rws = mkrsrc(ws, WS_USED);
  STAMP(0);
  if (wave == 0) {
    const u64 pc = __builtin_amdgcn_s_getpc();
    const unsigned line = ii * 64 + lane;
    unsigned sink = 0;
    if (line < 1380) sink = *(const GAS unsigned*)(pc + (u64)line * 64);
    asm volatile("" :: "v"(sink));
  }

  if (wave == 0) {
    if (ii < 20) mod_group(mkrsrc(cvec, D_ * 2), mkrsrc(wmod, D_ * 6 * D_ * 2), ws, 20 * xcd + ii, lane, tag);
  } else if (wave < 3) {
    const int tok = 128 * mb + 8 * j + 4 * (wave - 1) + (lane >> 4);
    u32x4 xv[5];
#pragma unroll
    for (int i = 0; i < 5; ++i) xv[i] = ld16(rx, tok * 1280 + ((lane & 15) + 16 * i) * 16, 0);
    float s = 0.f;
#pragma unroll
    for (int i = 0; i < 5; ++i)
#pragma unroll
      for (int e = 0; e < 4; ++e) s += blo(xv[i][e]) + bhi(xv[i][e]);
    const float mean = sum16(s) * (1.0f / D_);
    float q = 0.f;
#pragma unroll
    for (int i = 0; i < 5; ++i)
#pragma unroll
      for (int e = 0; e < 4; ++e) { const float a = blo(xv[i][e]) - mean, b = bhi(xv[i][e]) - mean; q = fmaf(a, a, q); q = fmaf(b, b, q); }
    const float var = sum16(q) * (1.0f / D_);
    const float mq = bf16r(mean), rden = 1.0f / bf16r(__builtin_sqrtf(bf16r(bf16r(var) + 1e-5f)));
    const int srcl = 4 * ((lane >> 2) + 16 * (lane & 3));
    const int tb = 4 * mb + (j >> 2), t32 = 8 * (j & 3) + 4 * (wave - 1) + (lane & 3);
#pragma unroll
    for (int i = 0; i < 5; ++i) {
      u32x4 o;
#pragma unroll
      for (int e = 0; e < 4; ++e) {
        const unsigned d = pk2(blo(xv[i][e]) - mq, bhi(xv[i][e]) - mq);
        o[e] = (unsigned)__builtin_amdgcn_ds_bpermute(srcl, (int)pk2(blo(d) * rden, bhi(d) * rden));
      }
      st16_sc1(o, rws, WS_XN + tb * T32_D + ((lane >> 2) + 16 * i) * 512 + t32 * 16, 0);
    }
    VMWAIT(0);
    if (lane == 0) st_u64(ws, flagoff(FL_XN0 + wave - 1, mb, j), flagval(tag));
  } else {
    const rsrc_t rw = mkrsrc(wqkv, D_ * 3 * D_ * 2);
    unsigned vo[15];
#pragma unroll
    for (int p = 0; p < 15; ++p) {
      const int idx = 64 * p + lane, rq = idx / 60, rem = idx - 60 * rq;
      int q, ch;
      if (rem < 48) { q = (rem >> 2) & 3; ch = 4 * (rem >> 4) + (rem & 3); } else { const int r2 = rem - 48; q = r2 / 3; ch = 12 + r2 - 3 * q; }
      const int seg = ch / 5;
      vo[p] = (4 * rq + q) * 3840 + seg * 1280 + 80 * j + (ch - 5 * seg) * 16;
    }
    asm volatile("s_nop 4" ::: "memory");
    for (int st = 0; st < 10; ++st)
#pragma unroll
      for (int p = 0; p < 15; ++p) dma16(rw, vo[p], st * 64 * 3840, L0 + L1_W + (st * 15 + p) * 1024);
    VMWAIT(0);
  }
  STAMP(1);
  wait_flags(ws, flagoff(FL_XN0 + (lane >> 4 & 1), mb, lane & 15), lane < 32, tag);
  u32x4 xf[40];
#pragma unroll
  for (int ks = 0; ks < 40; ++ks) xf[ks] = ld16_sc1(rws, WS_XN + (4 * mb + wave) * T32_D + 16 * lane, ks * 1024);
  MEMFENCE();
  {
    unsigned gv[3];
    const int g0 = 160 * wave + lane;
    for (unsigned spins = 0;;) {
      bool ok = true;
#pragma unroll
      for (int k = 0; k < 3; ++k) { u64 g = (u64)tag << 32; if (k < 2 || lane < 32) g = ld_u64(ws, WS_GRAN + (g0 + 64 * k) * 8); gv[k] = (unsigned)g; ok &= (unsigned)(g >> 32) == tag; }
      if (__all(ok)) break;
      if (spin_fail(spins, ws, tag)) break;
    }
#pragma unroll
    for (int k = 0; k < 3; ++k) if (k < 2 || lane < 32) {
      const int g = g0 + 64 * k;
      f32x2 v = {blo(gv[k]), bhi(gv[k])};
      if (g >= 320) { v.x = bf16r(1.0f + v.x); v.y = bf16r(1.0f + v.y); }
      *(LAS f32x2*)(size_t)(L0 + (g >= 320 ? L1_VS + (g - 320) * 8 : L1_VA + g * 8)) = v;
    }
  }
  BAR();
  STAMP(2);

  f32x16 acc1[4];
#pragma unroll
  for (int b = 0; b < 4; ++b) acc1[b] = (f32x16){};
  {
    const int h = lane >> 5;
    const unsigned wb = L0 + L1_W + 2 * h * 960 + ((lane & 15) >> 2) * 64 + ((lane >> 4) & 1) * 32 + (lane & 3) * 8;
    const unsigned wb3 = L0 + L1_W + 2 * h * 960 + 768 + ((lane & 15) >> 2) * 48 + ((lane >> 4) & 1) * 32 + (lane & 3) * 8;
    const unsigned vb = L0 + L1_VS + h * 32;
    s16x8 w[2][4]; u32x4 hf[2]; f32x4 vs[2][2], va[2][2];
    auto readw = [&](int ks, int b) { const unsigned a = (b < 3 ? wb + b * 256 : wb3) + ks * 3840; return trfrag(a, a + 960); };
    auto readv = [&](int ks, int buf, int part) {
      if (part == 0) { vs[buf][0] = lds_f4(vb + ks * 64); vs[buf][1] = lds_f4(vb + ks * 64 + 16); }
      else { va[buf][0] = lds_f4(vb + 2560 + ks * 64); va[buf][1] = lds_f4(vb + 2560 + ks * 64 + 16); } };
    auto xform = [&](int ks, int buf, int e) {
      const unsigned v = xf[ks][e];
      return pk2(fmaf(blo(v), vs[buf][e >> 1][(e & 1) * 2], va[buf][e >> 1][(e & 1) * 2]), fmaf(bhi(v), vs[buf][e >> 1][(e & 1) * 2 + 1], va[buf][e >> 1][(e & 1) * 2 + 1])); };
#pragma unroll
    for (int b = 0; b < 4; ++b) w[0][b] = readw(0, b);
    readv(0, 0, 0); readv(0, 0, 1); readv(1, 1, 0); readv(1, 1, 1);
#pragma unroll
    for (int e = 0; e < 4; ++e) hf[0][e] = xform(0, 0, e);
    SB();
#pragma unroll
    for (int ks = 0; ks < 40; ++ks) {
      const int c = ks & 1, n = c ^ 1;
#pragma unroll
      for (int b = 0; b < 4; ++b) {
        acc1[b] = mfma32(w[c][b], hf[c], acc1[b]);
        SB();
        if (ks + 1 < 40) {
          w[n][b] = readw(ks + 1, b);
          hf[n][b] = xform(ks + 1, n, b);
        }
        if (ks + 2 < 40 && b >= 2) readv(ks + 2, c, b - 2);
        SB();
      }
    }
  }
  BAR();
  STAMP(3);

  u32x4 qa0, qa1, qb0, qb1;
  {
    const int h = lane >> 5, t = lane & 31;
    u32x2 P[15];
#pragma unroll
    for (int i = 0; i < 15; ++i) { const f32x16& a = acc1[i >> 2]; const int r = 4 * (i & 3); P[i] = (u32x2){pk2(a[r], a[r + 1]), pk2(a[r + 2], a[r + 3])}; }
    const u32x2 one = {0x00003f80u, 0u}, zero = {0u, 0u};
    auto cat = [](u32x2 a, u32x2 b) { return (u32x4){a[0], a[1], b[0], b[1]}; };
    auto sel = [&](u32x2 a1, u32x2 a0) { return (u32x2){h ? a1[0] : a0[0], h ? a1[1] : a0[1]}; };
    qa0 = cat(P[0], P[1]);
    qa1 = cat(sel(zero, P[2]), sel(zero, one));
    qb0 = cat(sel(P[2], P[3]), sel(P[3], P[4]));
    qb1 = cat(sel(P[4], zero), sel(one, zero));
    const unsigned row = L0 + L2_KV + (256 + 32 * wave + t) * 48;
    lds_st4(row + 16 * h, cat(P[5], P[6]));
    lds_st4(row + KV_IMG + 16 * (h ^ 1), cat(sel(P[7], P[8]), sel(P[8], P[9])));
    lds_st4(row + h * KV_IMG + 32, cat(sel(P[9], P[7]), zero));
    lds_st4(row + 2 * KV_IMG + 16 * h, cat(P[10], P[11]));
    lds_st4(row + 3 * KV_IMG + 16 * (h ^ 1), cat(sel(P[12], P[13]), sel(P[13], P[14])));
    lds_st4(row + (2 + h) * KV_IMG + 32, cat(sel(P[14], P[12]), one));
    asm volatile("s_waitcnt lgkmcnt(0)" ::: "memory");
#pragma unroll
    for (int u = 0; u < 6; ++u) {
      const int i = 64 * u + lane, a = i / 96, o = (i - 96 * a) * 16;
      const u32x4 v = lds_u4(L0 + L2_KV + a * KV_IMG + (256 + 32 * wave) * 48 + o);
      st16_sc1(v, rws, WS_KV + ((mb * 16 + j) * 4 + a) * 6144 + wave * 1536 + o, 0);
    }
    VMWAIT(0);
  }
  BAR();
  if (tid == 0) st_u64(ws, flagoff(FL_KV, mb, j), flagval(tag));
  STAMP(4);
  if (wave == 0) {
    wait_flags(ws, flagoff(FL_KV, mb - 1 - (lane & 1) < 0 ? mb : mb - 1 - (lane & 1), j), lane < 2, tag);
  } else if (wave == 1 || (wave == 2 && ii < 8)) {
    mod_group(mkrsrc(cvec, D_ * 2), mkrsrc(wmod, D_ * 6 * D_ * 2), ws, 160 + 40 * xcd + (wave == 1 ? ii : 32 + ii), lane, tag);
  }
  BAR();
  STAMP(5);
  {
    u32x4 hv[12];
#pragma unroll
    for (int u = 0; u < 12; ++u) {
      const int i = 256 * u + tid, blk = i / 1536, r = i - 1536 * blk, a = r / 384, o = (r - 384 * a) * 16;
      const int src = mb - 2 + blk;
      const bool lastc = (o % 48) == 32;
      u32x4 v = {0u, 0u, (lastc && a < 2) ? 0x0000f149u : 0u, 0u};
      if (src >= 0) v = ld16_sc1(rws, WS_KV + ((src * 16 + j) * 4 + a) * 6144 + o, 0);
      hv[u] = v;
    }
#pragma unroll
    for (int u = 0; u < 12; ++u) {
      const int i = 256 * u + tid, blk = i / 1536, r = i - 1536 * blk, a = r / 384, o = (r - 384 * a) * 16;
      lds_st4(L0 + L2_KV + a * KV_IMG + blk * 6144 + o, hv[u]);
    }
  }
  BAR();
  STAMP(6);
  const int th = j & 1, nt = j >> 1;
  const rsrc_t rwo = mkrsrc(wo, D_ * D_ * 2);
  auto wo_issue = [&](int ks, unsigned ldsbase, int piece) {
    auto rowmap = [&](int g, int t, int q) { const int k = 32 * ks + 8 * g + 4 * t + q, hp = k / 40, r = k - 40 * hp; return 40 * hp + (r < 20 ? r : r < 24 ? r + 16 : r - 4); };
    dma16(rwo, w80_voff(piece, lane, nt, rowmap), 0, ldsbase + piece * 1024);
  };
  for (int ks = wave; ks < L3_NPRE; ks += 4)
#pragma unroll
    for (int p = 0; p < 5; ++p) wo_issue(ks, L0 + L3_W0 + ks * 5120, p);

  {
    const int h = lane >> 5, t = lane & 31;
    f32x16 mfirst, mdiag;
#pragma unroll
    for (int r = 0; r < 16; ++r) { const int key = (r & 3) + 8 * (r >> 2) + 4 * h; mfirst[r] = key > t ? 0.f : NEGBIG; mdiag[r] = key > t ? NEGBIG : 0.f; }
    f32x16 oacc[2];
    float linv[2];
#pragma unroll
    for (int hd = 0; hd < 2; ++hd) {
      const unsigned krow = L0 + L2_KV + hd * KV_IMG + (32 * wave + t) * 48;
      const unsigned k0 = krow + 16 * (h ^ hd), k1 = krow + 32;
      const u32x4 q0 = hd ? qb0 : qa0, q1 = hd ? qb1 : qa1;
      f32x16 sc[9];
#pragma unroll
      for (int kb = 0; kb < 9; ++kb) {
        const u32x4 a0 = lds_u4(k0 + kb * 1536), a1 = lds_u4(k1 + kb * 1536);
        const f32x16 c0 = kb == 0 ? mfirst : kb == 8 ? mdiag : (f32x16){};
        sc[kb] = mfma32u(a1, q1, mfma32u(a0, q0, c0));
      }
      float m = sc[0][0];
#pragma unroll
      for (int kb = 0; kb < 9; ++kb)
#pragma unroll
        for (int r = (kb == 0 ? 1 : 0); r < 16; ++r) m = fmaxf(m, sc[kb][r]);
      m = xmax32(m);
      const float mc = -m * SCORE_C;
      const unsigned vrow = L0 + L2_KV + (2 + hd) * KV_IMG + (32 * wave + 4 * h + ((lane & 15) >> 2)) * 48 + ((lane >> 4) & 1) * 32 + (lane & 3) * 8;
      f32x16 o = {};
#pragma unroll
      for (int kb = 0; kb < 9; ++kb) {
#pragma unroll
        for (int s = 0; s < 2; ++s) {
          u32x4 pf;
#pragma unroll
          for (int e = 0; e < 4; ++e) pf[e] = pk2(exp2_(fmaf(sc[kb][8 * s + 2 * e], SCORE_C, mc)), exp2_(fmaf(sc[kb][8 * s + 2 * e + 1], SCORE_C, mc)));
          const unsigned va = vrow + (32 * kb + 16 * s) * 48;
          o = mfma32(trfrag(va, va + 8 * 48), pf, o);
        }
      }
      const auto lr = __builtin_amdgcn_permlane32_swap(__float_as_uint(o[8]), __float_as_uint(o[8]), false, false);
      linv[hd] = 1.0f / __uint_as_float(lr[1]);
      oacc[hd] = o;
    }
    const unsigned ob = WS_O + (4 * mb + wave) * T32_D + 5 * j * 512;
    u32x4 va, vb2, vc;
#pragma unroll
    for (int e = 0; e < 4; ++e) { va[e] = pk2(oacc[0][2 * e] * linv[0], oacc[0][2 * e + 1] * linv[0]); vb2[e] = pk2(oacc[1][2 * e] * linv[1], oacc[1][2 * e + 1] * linv[1]); }
    vc = (u32x4){pk2(oacc[0][8] * linv[0], oacc[0][9] * linv[0]), pk2(oacc[0][10] * linv[0], oacc[0][11] * linv[0]),
                 pk2(oacc[1][8] * linv[1], oacc[1][9] * linv[1]), pk2(oacc[1][10] * linv[1], oacc[1][11] * linv[1])};
    st16_sc1(va, rws, ob + 16 * lane, 0);
    st16_sc1(vb2, rws, ob + 3 * 512 + 16 * lane, 0);
    if (lane < 32) st16_sc1(vc, rws, ob + 2 * 512 + 16 * lane, 0);
    VMWAIT(0);
  }
  BAR();
  if (tid == 0) st_u64(ws, flagoff(FL_O, mb, j), flagval(tag));
  STAMP(7);
  const int t16 = lane & 15, g4 = lane >> 4;
  const int tok3 = 128 * mb + 64 * th + 16 * wave + t16;
  u32x2 xres[5];
#pragma unroll
  for (int p = 0; p < 5; ++p) xres[p] = ld8(rx, tok3 * 1280 + 160 * nt + 32 * ct_of(p, nt & 1) + 8 * g4, 0);
  if (wave == 0) {
    unsigned gv[2];
    const int i0 = lane, i1 = 64 + lane;
    auto goff = [&](int i) { const int kind = i / 40, r = i - 40 * kind; return WS_GRAN + ((kind == 0 ? 640 : kind == 1 ? 1280 : 1600) + 40 * nt + r) * 8; };
    for (unsigned spins = 0;;) {
      const u64 a = ld_u64(ws, goff(i0)); u64 b = (u64)tag << 32; if (lane < 56) b = ld_u64(ws, goff(i1));
      gv[0] = (unsigned)a; gv[1] = (unsigned)b;
      if (__all((unsigned)(a >> 32) == tag && (unsigned)(b >> 32) == tag)) break;
      if (spin_fail(spins, ws, tag)) break;
    }
#pragma unroll
    for (int k = 0; k < 2; ++k) if (k == 0 || lane < 56) {
      const int i = 64 * k + lane, kind = i / 40, r = i - 40 * kind;
      const int ct = r >> 3, p = (nt & 1) ? (ct == 0 ? 4 : ct - 1) : ct;
      f32x2 v = {blo(gv[k]), bhi(gv[k])};
      if (kind == 1) { v.x = bf16r(1.0f + v.x); v.y = bf16r(1.0f + v.y); }
      *(LAS f32x2*)(size_t)(L0 + L3_VEC + kind * 320 + p * 64 + (r & 7) * 8) = v;
    }
    wait_flags(ws, flagoff(FL_O, mb, lane & 15), lane < 16, tag);
  } else if (wave < 3) {
    const int unit = bid;
    if (unit < 216) {
      const rsrc_t rw = mkrsrc(unit < 108 ? wg : wu, D_ * F_ * 2);
      const int cu = unit < 108 ? unit : unit - 108;
      u32x4 w[10];
#pragma unroll
      for (int r = 0; r < 10; ++r) w[r] = ld16(rw, (10 * lane + r) * 3456, 32 * cu + 16 * (wave - 1));
      unsigned sbv[5], cbv[5];
      sweep<5>(ws, WS_GRAN + (960 + 5 * lane) * 8, 8, tag, sbv);
      sweep<5>(ws, WS_GRAN + (1280 + 5 * lane) * 8, 8, tag, cbv);
      float u[8], v[8];
#pragma unroll
      for (int e = 0; e < 8; ++e) { u[e] = 0.f; v[e] = 0.f; }
#pragma unroll
      for (int r = 0; r < 10; ++r) {
        const float s1 = bf16r(1.0f + ((r & 1) ? bhi(cbv[r >> 1]) : blo(cbv[r >> 1]))), s0 = (r & 1) ? bhi(sbv[r >> 1]) : blo(sbv[r >> 1]);
#pragma unroll
        for (int e = 0; e < 4; ++e) {
          const float a = blo(w[r][e]), b = bhi(w[r][e]);
          u[2 * e] = fmaf(s1, a, u[2 * e]); u[2 * e + 1] = fmaf(s1, b, u[2 * e + 1]); v[2 * e] = fmaf(s0, a, v[2 * e]); v[2 * e + 1] = fmaf(s0, b, v[2 * e + 1]);
        }
      }
      float pv = 0.f;
#pragma unroll
      for (int e = 0; e < 8; ++e) { const float a = sum64(u[e]), b = sum64(v[e]); pv = lane == e ? a : lane == 8 + e ? b : pv; }
      if (lane < 16) st_u64(ws, WS_UV + (unit * 32 + (lane >> 3) * 16 + 8 * (wave - 1) + (lane & 7)) * 8, ((u64)tag << 32) | __float_as_uint(pv));
    }
  } else {
    for (int ks = L3_NPRE; ks < 20; ++ks)
#pragma unroll
      for (int p = 0; p < 5; ++p) wo_issue(ks, L0 + L3_W1 + (ks - L3_NPRE) * 5120, p);
    VMWAIT(0);
  }
  BAR();
  STAMP(8);

  const unsigned b80_0 = g4 * 1024 + ((lane & 15) >> 2) * 64 + (g4 & 1) * 32 + (lane & 3) * 8;
  const unsigned b80_1 = b80_0 ^ 32u;
  const unsigned b80_2 = 4096 + 8 * lane;
  auto w80 = [&](unsigned kbase, int p) { const unsigned a = kbase + (p == 4 ? b80_2 : ((p & 1) ? b80_1 : b80_0) + (p >> 1) * 256); return trfrag(a, a + 512); };

  u32x2 x1p[5];
  {
    u32x4 of[20];
    const unsigned ofb = WS_O + (4 * mb + 2 * th + (wave >> 1)) * T32_D + g4 * 512 + ((wave & 1) * 16 + t16) * 16;
#pragma unroll
    for (int s = 0; s < 20; ++s) of[s] = ld16_sc1(rws, ofb, s * 2048);
    MEMFENCE();
    f32x4 acc[5];
#pragma unroll
    for (int p = 0; p < 5; ++p) acc[p] = (f32x4){};
    auto kb3 = [&](int s) { return L0 + (s < L3_NPRE ? L3_W0 + s * 5120 : L3_W1 + (s - L3_NPRE) * 5120); };
    s16x8 w[2][5];
#pragma unroll
    for (int p = 0; p < 5; ++p) w[0][p] = w80(kb3(0), p);
    SB();
#pragma unroll
    for (int s = 0; s < 20; ++s) {
#pragma unroll
      for (int p = 0; p < 5; ++p) {
        acc[p] = mfma16(w[s & 1][p], of[s], acc[p]);
        SB();
        if (s + 1 < 20) w[(s & 1) ^ 1][p] = w80(kb3(s + 1), p);
        SB();
      }
    }
    STAMP(9);
    float xv[5][4]; float s1 = 0.f;
    u32x2 yp[5];
#pragma unroll
    for (int p = 0; p < 5; ++p) {
      const f32x4 ga = lds_f4(L0 + L3_VEC + p * 64 + g4 * 16), sb1 = lds_f4(L0 + L3_VEC + 320 + p * 64 + g4 * 16);
#pragma unroll
      for (int r = 0; r < 4; ++r) {
        const float xr = (r & 1) ? bhi(xres[p][r >> 1]) : blo(xres[p][r >> 1]);
        xv[p][r] = bf16r(xr + bf16r(ga[r] * bf16r(acc[p][r])));
        s1 += xv[p][r];
      }
      x1p[p] = (u32x2){pk2(xv[p][0], xv[p][1]), pk2(xv[p][2], xv[p][3])};
      yp[p] = (u32x2){pk2(xv[p][0] * sb1[0], xv[p][1] * sb1[1]), pk2(xv[p][2] * sb1[2], xv[p][3] * sb1[3])};
    }
    const float mt = xsum32(xsum16(s1)) * (1.0f / 80);
    float m2 = 0.f;
#pragma unroll
    for (int p = 0; p < 5; ++p)
#pragma unroll
      for (int r = 0; r < 4; ++r) { const float d = xv[p][r] - mt; m2 = fmaf(d, d, m2); }
    m2 = xsum32(xsum16(m2));
    if (lane < 16) st_u64(ws, WS_STAT + (((mb * 2 + th) * 8 + nt) * 64 + 16 * wave + t16) * 8, ((u64)__float_as_uint(m2) << 32) | __float_as_uint(mt));
    const unsigned yb = WS_Y + (4 * mb + 2 * th + (wave >> 1)) * T32_D + 10 * nt * 512 + ((wave & 1) * 16 + t16) * 16;
#pragma unroll
    for (int pp = 0; pp < 3; ++pp) {
      const int pa = 2 * pp, pb = pp < 2 ? 2 * pp + 1 : 4;
      const auto r0 = __builtin_amdgcn_permlane16_swap(yp[pa][0], yp[pb][0], false, false), r1 = __builtin_amdgcn_permlane16_swap(yp[pa][1], yp[pb][1], false, false);
      const int ct = (g4 & 1) ? ct_of(pb, nt & 1) : ct_of(pa, nt & 1);
      if (pp < 2 || !(g4 & 1)) st16_sc1((u32x4){r0[0], r1[0], r0[1], r1[1]}, rws, yb + (2 * ct + (g4 >> 1)) * 512, 0);
    }
    VMWAIT(0);
  }
  BAR();
  if (tid == 0) st_u64(ws, flagoff(FL_Y, mb, j), flagval(tag));
  STAMP(10);

  const int nu = j < 12 ? 7 : 6, u0 = j < 12 ? 7 * j : 84 + 6 * (j - 12), uodd = u0 & 1;
  const rsrc_t rgu = mkrsrc(wave < 2 ? wg : wu, D_ * F_ * 2);
  auto gu_voff = [&](int piece  , int isup) {
    const int idx = 64 * piece + lane, rq = idx / 56, rem = idx - 56 * rq;
    int q, p, hf;
    if (rem < 48) { q = (rem >> 2) & 3; const int c = (rem & 3) ^ (isup ? 2 : 0); p = 2 * (rem >> 4) + (c >> 1); hf = c & 1; } else { q = (rem - 48) >> 1; p = 6; hf = rem & 1; }
    return (unsigned)((4 * rq + q) * 3456 + 32 * (u0 + unit_of(p, uodd, nu)) + 16 * hf);
  };
  unsigned guo[7];
#pragma unroll
  for (int i = 0; i < 7; ++i) guo[i] = gu_voff(7 * (wave & 1) + i, wave >> 1);
  auto gu_issue = [&](int st) {
    const unsigned base = L0 + (st % 5) * L4_STAGE + (wave >> 1) * L4_U + (wave & 1) * 7168;
#pragma unroll
    for (int i = 0; i < 7; ++i) dma16(rgu, guo[i], st * 64 * 3456, base + i * 1024);
  };
  float rs4, nm4;
  u32x4 yf[40];
  if (wave == 0) {
    unsigned uv[7];
    for (unsigned spins = 0;;) {
      bool ok = true;
#pragma unroll
      for (int p = 0; p < 7; ++p) { const u64 g = ld_u64(ws, WS_UV + (((lane >> 5) * 108 + u0 + unit_of(p, uodd, nu)) * 32 + (lane & 31)) * 8); uv[p] = (unsigned)g; ok &= (unsigned)(g >> 32) == tag; }
      if (__all(ok)) break;
      if (spin_fail(spins, ws, tag)) break;
    }
#pragma unroll
    for (int p = 0; p < 7; ++p) *(LAS unsigned*)(size_t)(L0 + L4_UV + (p * 64 + lane) * 4) = uv[p];
    wait_flags(ws, flagoff(FL_Y, mb, lane & 15), lane < 16, tag);
  } else {
    for (int st = 0; st < 5; ++st) gu_issue(st);
  }
  BAR();
  STAMP(11);
  {
    const int h = lane >> 5, T = 32 * wave + (lane & 31);
    u32x2 stv[4];
#pragma unroll
    for (int i = 0; i < 4; ++i) stv[i] = ld8_sc1(rws, WS_STAT + (((mb * 2 + (T >> 6)) * 8 + 4 * h + i) * 64 + (T & 63)) * 8, 0);
#pragma unroll
    for (int ks = 0; ks < 40; ++ks) yf[ks] = ld16_sc1(rws, WS_Y + (4 * mb + wave) * T32_D + 16 * lane, ks * 1024);
    MEMFENCE();
    if (wave == 0) for (int st = 0; st < 5; ++st) gu_issue(st);
    float ms = 0.f;
#pragma unroll
    for (int i = 0; i < 4; ++i) ms += __uint_as_float(stv[i][0]);
    const float mean = xsum32(ms) * 0.125f;
    float m2 = 0.f;
#pragma unroll
    for (int i = 0; i < 4; ++i) { const float d = __uint_as_float(stv[i][0]) - mean; m2 += __uint_as_float(stv[i][1]) + 80.0f * d * d; }
    const float var = xsum32(m2) * (1.0f / D_);
    rs4 = 1.0f / bf16r(__builtin_sqrtf(bf16r(bf16r(var) + 1e-5f)));
    nm4 = -rs4 * bf16r(mean);
  }
  f32x16 acc4[7];
#pragma unroll
  for (int b = 0; b < 7; ++b) acc4[b] = (f32x16){};
  {
    const int h = lane >> 5, blk = (lane >> 4) & 1, q = (lane & 15) >> 2;
    const unsigned c0 = L0 + blk * L4_U + 2 * h * 896 + q * 64 + blk * 32 + (lane & 3) * 8, c1 = c0 ^ 32u;
    const unsigned c2 = L0 + blk * L4_U + 2 * h * 896 + 768 + q * 32 + (lane & 3) * 8;
    auto readw = [&](int ks, int p) {
      const unsigned a = (p == 6 ? c2 : ((p & 1) ? c1 : c0) + (p >> 1) * 256) + ((ks >> 2) % 5) * L4_STAGE + (ks & 3) * 4 * 896;
      return trfrag(a, a + 896); };
    s16x8 w[2][7];
    VMWAIT(0);
    BAR();
#pragma unroll
    for (int p = 0; p < 7; ++p) w[0][p] = readw(0, p);
    SB();
#pragma unroll
    for (int ks = 0; ks < 40; ++ks) {
      const int c = ks & 1, n = c ^ 1;
      if ((ks & 3) == 3 && ks + 1 < 40) {
        const int st = ks >> 2;
        if (st == 4 || st == 5) { VMWAIT(21); } else if (st == 6) { VMWAIT(14); } else if (st == 7) { VMWAIT(7); } else if (st == 8) { VMWAIT(0); }
        BAR();
        if (st + 5 < 10) gu_issue(st + 5);
        SB();
      }
#pragma unroll
      for (int p = 0; p < 7; ++p) {
        acc4[p] = mfma32(w[c][p], yf[ks], acc4[p]);
        SB();
        if (ks + 1 < 40) w[n][p] = readw(ks + 1, p);
        SB();
      }
    }
  }
  STAMP(12);
  {
    const int h = lane >> 5;
    const unsigned ab = WS_ACT + (4 * mb + wave) * T32_F + 16 * lane;
#pragma unroll
    for (int p = 0; p < 7; ++p) {
      if (p < 6 || nu == 7) {
        const unsigned tb = L0 + L4_UV + p * 256 + 16 * h;
        float a[8];
#pragma unroll
        for (int half = 0; half < 2; ++half) {
          const f32x4 ug = lds_f4(tb + 32 * half), vg = lds_f4(tb + 64 + 32 * half), uu = lds_f4(tb + 128 + 32 * half), vu = lds_f4(tb + 192 + 32 * half);
#pragma unroll
          for (int r = 0; r < 4; ++r) {
            const float g = fmaf(rs4, acc4[p][4 * half + r], fmaf(nm4, ug[r], vg[r]));
            const float u = fmaf(rs4, acc4[p][8 + 4 * half + r], fmaf(nm4, uu[r], vu[r]));
            a[4 * half + r] = siluf(g) * u;
          }
        }
        st16_sc1((u32x4){pk2(a[0], a[1]), pk2(a[2], a[3]), pk2(a[4], a[5]), pk2(a[6], a[7])}, rws, ab + (u0 + unit_of(p, uodd, nu)) * 1024, 0);
      }
    }
    VMWAIT(0);
  }
  BAR();
  if (tid == 0) st_u64(ws, flagoff(FL_ACT, mb, j), flagval(tag));
  STAMP(13);

  const rsrc_t rwd = mkrsrc(wd, F_ * D_ * 2);
  unsigned wdo[5];
#pragma unroll
  for (int p = 0; p < 5; ++p) wdo[p] = w80_voff(p, lane, nt, [](int g, int t, int q) { return 16 * (g >> 1) + 8 * t + 4 * (g & 1) + q; });
  unsigned wdg[8];
#pragma unroll
  for (int i = 0; i < 8; ++i) {
    const int pc = min(8 * wave + i, 29), kl = pc / 5;
    wdg[i] = w80_voff(pc - 5 * kl, lane, nt, [](int g, int t, int q) { return 16 * (g >> 1) + 8 * t + 4 * (g & 1) + q; }) + kl * 32 * 1280;
  }
  auto wd_issue = [&](int ks) {
#pragma unroll
    for (int p = 0; p < 5; ++p) dma16(rwd, wdo[p], ks * 32 * 1280, L0 + (ks % L5_NRING) * 5120 + p * 1024);
  };
  if (wave == 0) {
    wait_flags(ws, flagoff(FL_ACT, mb, lane & 15), lane < 16, tag);
    if (bid == 0) {
#pragma unroll
      for (int k = 0; k < 4; ++k) wait_flags(ws, flagoff(FL_KV, 4 * k + (lane >> 4), lane & 15), true, tag);
      if (lane == 0) __hip_atomic_store((GAS unsigned*)(ws + WS_EPOCH), tag, __ATOMIC_RELAXED, __HIP_MEMORY_SCOPE_AGENT);
    }
  } else {
    for (int ks = wave - 1; ks < L5_NRING; ks += 3) wd_issue(ks);
  }
  BAR();
  STAMP(14);
  {
    u32x4 af[54];
    const unsigned afb = WS_ACT + (4 * mb + 2 * th + (wave >> 1)) * T32_F + g4 * 512 + ((wave & 1) * 16 + t16) * 16;
#pragma unroll
    for (int s = 0; s < 54; ++s) af[s] = ld16_sc1(rws, afb, s * 2048);
    MEMFENCE();
    f32x4 acc[5];
#pragma unroll
    for (int p = 0; p < 5; ++p) acc[p] = (f32x4){};
    s16x8 w[2][5];
    VMWAIT(54);
    BAR();
#pragma unroll
    for (int p = 0; p < 5; ++p) w[0][p] = w80(L0, p);
    SB();
#pragma unroll
    for (int s = 0; s < 54; ++s) {
      if (s % 6 == 5 && s + 1 < 54) {
        const int G = s / 6;
        if (G + 1 >= 5) { if (G + 1 == 5) { VMWAIT(24); } else if (G + 1 == 6) { VMWAIT(16); } else if (G + 1 == 7) { VMWAIT(8); } else { VMWAIT(0); } }
        BAR();
        if (G < 4) {
#pragma unroll
          for (int i = 0; i < 8; ++i) dma16(rwd, wdg[i], (30 + 6 * G) * 32 * 1280, L0 + 6 * G * 5120 + min(8 * wave + i, 29) * 1024);
        }
        SB();
      }
#pragma unroll
      for (int p = 0; p < 5; ++p) {
        acc[p] = mfma16(w[s & 1][p], af[s], acc[p]);
        SB();
        if (s + 1 < 54) w[(s & 1) ^ 1][p] = w80(L0 + ((s + 1) % L5_NRING) * 5120, p);
        SB();
      }
    }
    STAMP(15);
    u32x2 op[5];
#pragma unroll
    for (int p = 0; p < 5; ++p) {
      const f32x4 gb = lds_f4(L0 + L3_VEC + 640 + p * 64 + g4 * 16);
      float o[4];
#pragma unroll
      for (int r = 0; r < 4; ++r) {
        const float xr = (r & 1) ? bhi(x1p[p][r >> 1]) : blo(x1p[p][r >> 1]);
        o[r] = xr + bf16r(gb[r] * bf16r(acc[p][r]));
      }
      op[p] = (u32x2){pk2(o[0], o[1]), pk2(o[2], o[3])};
    }
    const rsrc_t ro = mkrsrc(out, S_ * D_ * 2);
#pragma unroll
    for (int pp = 0; pp < 3; ++pp) {
      const int pa = 2 * pp, pb = pp < 2 ? 2 * pp + 1 : 4;
      const auto r0 = __builtin_amdgcn_permlane16_swap(op[pa][0], op[pb][0], false, false), r1 = __builtin_amdgcn_permlane16_swap(op[pa][1], op[pb][1], false, false);
      const int ct = (g4 & 1) ? ct_of(pb, nt & 1) : ct_of(pa, nt & 1);
      if (pp < 2 || !(g4 & 1))
        __builtin_amdgcn_raw_buffer_store_b128((u32x4){r0[0], r1[0], r0[1], r1[1]}, ro, tok3 * 1280 + 160 * nt + 32 * ct + 16 * (g4 >> 1), 0, 0);
    }
  }
  STAMP(16);
}

extern "C" void kernel_launch(void* const* d_in, const int* in_sizes, int n_in, void* d_out, int out_size, void* d_ws, size_t ws_size,
                              hipStream_t stream) {
  (void)in_sizes; (void)n_in; (void)out_size; (void)ws_size;
  adaln_block_kernel<<<256, 256, 0, stream>>>((const char*)d_in[0], (const char*)d_in[1], (const char*)d_in[2], (const char*)d_in[4], (const char*)d_in[5],
                                              (const char*)d_in[6], (const char*)d_in[7], (const char*)d_in[8], (char*)d_out, (char*)d_ws);
}
```

```cpp
#include <hip/hip_runtime.h>
#include <stdint.h>
#include <type_traits>

#define LAS __attribute__((address_space(3)))
#define GAS __attribute__((address_space(1)))
#define DI __device__ __forceinline__
typedef float f32x2 __attribute__((ext_vector_type(2)));
typedef float f32x4 __attribute__((ext_vector_type(4)));
typedef float f32x16 __attribute__((ext_vector_type(16)));
typedef unsigned u32x2 __attribute__((ext_vector_type(2)));
typedef unsigned u32x4 __attribute__((ext_vector_type(4)));
typedef short s16x4 __attribute__((ext_vector_type(4)));
typedef short s16x8 __attribute__((ext_vector_type(8)));
typedef __bf16 bf16x2 __attribute__((ext_vector_type(2)));
typedef __bf16 bf16x8 __attribute__((ext_vector_type(8)));
typedef unsigned long long u64;
typedef __amdgpu_buffer_rsrc_t rsrc_t;

constexpr int S_ = 2048, D_ = 640, F_ = 1728;
constexpr int T32_D = 80 * 512;
constexpr int T32_F = 216 * 512;

constexpr int WS_EPOCH = 0;
constexpr int WS_TMO = 256;
constexpr int WS_FLAG = 29 << 20;
constexpr int FL_XN = 0, FL_KV = 1, FL_O = 2, FL_Y = 3, FL_ACT = 4;
constexpr int WS_GRAN = 256 * 1024;
constexpr int WS_UV = 320 * 1024;
constexpr int WS_STAT = 512 * 1024;
constexpr int WS_STAMP = 28 << 20;
constexpr int WS_XN = 1 << 20;
constexpr int WS_KV = 4 << 20;
constexpr int WS_O = 12 << 20;
constexpr int WS_Y = 16 << 20;
constexpr int WS_ACT = 20 << 20;
constexpr int WS_USED = 30 << 20;

constexpr int LDS_BYTES = 163840;
constexpr int L1_W = 0;
constexpr int L0_TB = 122880;
constexpr int L0_JW = 133120;
constexpr int L1_VS = 153600, L1_VA = 154880;
constexpr int L0_SC = 158720;
constexpr int L2_KV = 0;
constexpr int KV_IMG = 384 * 48;
constexpr int L3_W0 = 4 * KV_IMG;
constexpr int L3_NPRE = 15;
constexpr int L3_W1 = 0;
constexpr int L2_JW = 32768, L2_JV = 53248;
constexpr int L_DUMMY = 162816;
constexpr int L3_VEC = 161024;
constexpr int L4_STAGE = 30720, L4_U = 14464;
constexpr int L4_UV = 153600;
static_assert(L3_W0 + L3_NPRE * 5120 <= L3_VEC && L3_VEC + 960 <= LDS_BYTES, "P3 LDS map");
static_assert(30 * 5120 <= L3_VEC, "P5 LDS map");
static_assert(L4_UV + 1792 <= L3_VEC, "P4 LDS map");

constexpr float LOG2E = 1.4426950408889634f;
constexpr float SCORE_C = 0.22360679774997896f * 1.4426950408889634f;
constexpr float NEGBIG = -1.0e30f;
constexpr unsigned SPIN_CAP = 1u << 15;

#define SB() __builtin_amdgcn_sched_barrier(0)
#define BAR() do { asm volatile("s_waitcnt lgkmcnt(0)" ::: "memory"); __builtin_amdgcn_s_barrier(); asm volatile("" ::: "memory"); } while (0)
#define BAR_RAW() do { asm volatile("" ::: "memory"); __builtin_amdgcn_s_barrier(); asm volatile("" ::: "memory"); } while (0)
#define MEMFENCE() asm volatile("" ::: "memory")
template <int N> DI void vmwait() { asm volatile("s_waitcnt vmcnt(%0)" :: "n"(N) : "memory"); }
template <int N> DI void vmwait(u32x4& d) { asm volatile("s_waitcnt vmcnt(%1)" : "+v"(d) : "n"(N) : "memory"); }

DI unsigned pk2(float a, float b) { bf16x2 v = __builtin_convertvector((f32x2){a, b}, bf16x2); return __builtin_bit_cast(unsigned, v); }
DI float blo(unsigned v) { return __uint_as_float(v << 16); }
DI float bhi(unsigned v) { return __uint_as_float(v & 0xffff0000u); }
DI float bf16r(float a) { return blo(pk2(a, a)); }
DI f32x2 bf16r2(f32x2 a) { const unsigned p = pk2(a.x, a.y); return (f32x2){blo(p), bhi(p)}; }
DI f32x2 unpk(unsigned p) { return (f32x2){blo(p), bhi(p)}; }
DI float rcpf_(float x) { return __builtin_amdgcn_rcpf(x); }
DI float exp2_(float x) { return __builtin_amdgcn_exp2f(x); }
DI float siluf(float x) { return x * rcpf_(1.0f + exp2_(-LOG2E * x)); }
DI float rden_of(float var) { return rcpf_(bf16r(__builtin_amdgcn_sqrtf(bf16r(bf16r(var) + 1e-5f)))); }
template <int CTRL> DI float dpp_add(float x) { return x + __int_as_float(__builtin_amdgcn_update_dpp(0, __float_as_int(x), CTRL, 0xf, 0xf, true)); }
DI float sum16(float x) { x = dpp_add<0xB1>(x); x = dpp_add<0x4E>(x); x = dpp_add<0x141>(x); return dpp_add<0x140>(x); }
DI float xsum16(float x) { auto r = __builtin_amdgcn_permlane16_swap(__float_as_uint(x), __float_as_uint(x), false, false); return __uint_as_float(r[0]) + __uint_as_float(r[1]); }
DI float xsum32(float x) { auto r = __builtin_amdgcn_permlane32_swap(__float_as_uint(x), __float_as_uint(x), false, false); return __uint_as_float(r[0]) + __uint_as_float(r[1]); }
DI float xmax32(float x) { auto r = __builtin_amdgcn_permlane32_swap(__float_as_uint(x), __float_as_uint(x), false, false); return fmaxf(__uint_as_float(r[0]), __uint_as_float(r[1])); }
DI rsrc_t mkrsrc(const void* p, unsigned bytes) { return __builtin_amdgcn_make_buffer_rsrc((void*)p, (short)0, (int)bytes, 0x00020000); }
DI u32x4 ld16_sc1(rsrc_t r, unsigned voff, unsigned soff) { return __builtin_amdgcn_raw_buffer_load_b128(r, voff, soff, 16); }
DI u32x4 ld16(rsrc_t r, unsigned voff, unsigned soff) { return __builtin_amdgcn_raw_buffer_load_b128(r, voff, soff, 0); }
DI u32x2 ld8(rsrc_t r, unsigned voff, unsigned soff) { return __builtin_amdgcn_raw_buffer_load_b64(r, voff, soff, 0); }
DI u32x2 ld8_sc1(rsrc_t r, unsigned voff, unsigned soff) { return __builtin_amdgcn_raw_buffer_load_b64(r, voff, soff, 16); }
DI void st16_sc1(u32x4 v, rsrc_t r, unsigned voff, unsigned soff) { __builtin_amdgcn_raw_buffer_store_b128(v, r, voff, soff, 16); }
DI void ald16_sc1(u32x4& d, rsrc_t r, unsigned voff, unsigned soff) {
  asm volatile("buffer_load_dwordx4 %0, %1, %2, %3 offen sc1" : "=v"(d) : "v"(voff), "s"(r), "s"(soff) : "memory");
}
DI void ald16(u32x4& d, rsrc_t r, unsigned voff, unsigned soff) {
  asm volatile("buffer_load_dwordx4 %0, %1, %2, %3 offen" : "=v"(d) : "v"(voff), "s"(r), "s"(soff) : "memory");
}
DI void ald4(unsigned& d, rsrc_t r, unsigned voff, unsigned soff) {
  asm volatile("buffer_load_dword %0, %1, %2, %3 offen" : "=v"(d) : "v"(voff), "s"(r), "s"(soff) : "memory");
}
DI void ald8_sc1(u32x2& d, rsrc_t r, unsigned voff, unsigned soff) {
  asm volatile("buffer_load_dwordx2 %0, %1, %2, %3 offen sc1" : "=v"(d) : "v"(voff), "s"(r), "s"(soff) : "memory");
}
DI void touch(rsrc_t r, unsigned voff, unsigned soff, unsigned ldsaddr) {
  asm volatile("s_mov_b32 m0, %0\n\ts_nop 0\n\tbuffer_load_dword %1, %2, %3 offen lds" :: "s"(ldsaddr), "v"(voff), "s"(r), "s"(soff) : "memory");
}
DI void dma16_sc1(rsrc_t r, unsigned voff, unsigned soff, unsigned ldsaddr) {
  asm volatile("s_mov_b32 m0, %0\n\ts_nop 0\n\tbuffer_load_dwordx4 %1, %2, %3 offen sc1 lds" :: "s"(ldsaddr), "v"(voff), "s"(r), "s"(soff) : "memory");
}
DI void dma16(rsrc_t r, unsigned voff, unsigned soff, unsigned ldsaddr) {
  asm volatile("s_mov_b32 m0, %0\n\ts_nop 0\n\tbuffer_load_dwordx4 %1, %2, %3 offen lds" :: "s"(ldsaddr), "v"(voff), "s"(r), "s"(soff) : "memory");
}
DI u64 ld_u64(const char* ws, unsigned off) { return __hip_atomic_load((GAS u64*)(ws + off), __ATOMIC_RELAXED, __HIP_MEMORY_SCOPE_AGENT); }
DI void st_u64(char* ws, unsigned off, u64 v) { __hip_atomic_store((GAS u64*)(ws + off), v, __ATOMIC_RELAXED, __HIP_MEMORY_SCOPE_AGENT); }
DI u64 flagval(unsigned tag) { return ((u64)tag << 32) | (u64)(~tag); }
DI unsigned flagoff(int kind, int mb, int jw) { return WS_FLAG + ((kind * 16 + mb) * 64 + jw) * 128; }
DI void publish(char* ws, int kind, int mb, int j, int wave, int lane, unsigned tag) {
  vmwait<0>();
  if (lane == 0) st_u64(ws, flagoff(kind, mb, 4 * j + wave), flagval(tag));
}
DI bool spin_fail(unsigned& spins, char* ws, unsigned tag) {
  __builtin_amdgcn_s_sleep(1);
  if (++spins > SPIN_CAP) { __hip_atomic_store((GAS unsigned*)(ws + WS_TMO), tag, __ATOMIC_RELAXED, __HIP_MEMORY_SCOPE_AGENT); return true; }
  return (spins & 127u) == 0u && (unsigned)__builtin_amdgcn_readfirstlane(__hip_atomic_load((GAS unsigned*)(ws + WS_TMO), __ATOMIC_RELAXED, __HIP_MEMORY_SCOPE_AGENT)) == tag;
}
DI void wait_flags(char* ws, unsigned off, bool on, unsigned tag) {
  const u64 want = flagval(tag);
#pragma clang loop unroll(disable)
  for (unsigned spins = 0;;) {
    u64 v = want;
    if (on) v = ld_u64(ws, off);
    if (__all(v == want)) break;
    if (spin_fail(spins, ws, tag)) break;
  }
  MEMFENCE();
}
template <int N, class OFF, class ON> DI void sweep(char* ws, OFF off, ON on, unsigned tag, unsigned (&val)[N]) {
#pragma clang loop unroll(disable)
  for (unsigned spins = 0;;) {
    bool ok = true;
#pragma unroll
    for (int k = 0; k < N; ++k) { u64 g = (u64)tag << 32; if (on(k)) g = ld_u64(ws, off(k)); val[k] = (unsigned)g; ok &= (unsigned)(g >> 32) == tag; }
    if (__all(ok)) break;
    if (spin_fail(spins, ws, tag)) break;
  }
  MEMFENCE();
}
DI s16x8 trfrag(unsigned a0, unsigned a1) {
  const s16x4 lo = __builtin_amdgcn_ds_read_tr16_b64_v4i16((LAS s16x4*)(size_t)a0);
  const s16x4 hi = __builtin_amdgcn_ds_read_tr16_b64_v4i16((LAS s16x4*)(size_t)a1);
  return __builtin_shufflevector(lo, hi, 0, 1, 2, 3, 4, 5, 6, 7);
}
DI f32x4 lds_f4(unsigned a) { return *(LAS f32x4*)(size_t)a; }
DI u32x4 lds_u4(unsigned a) { return *(LAS u32x4*)(size_t)a; }
DI void lds_st4(unsigned a, u32x4 v) { *(LAS u32x4*)(size_t)a = v; }
DI void lds_st1(unsigned a, unsigned v) { *(LAS unsigned*)(size_t)a = v; }
DI void lds_st2f(unsigned a, f32x2 v) { *(LAS f32x2*)(size_t)a = v; }
DI unsigned opaque(unsigned v) { asm volatile("" : "+v"(v)); return v; }
DI unsigned sopaque(unsigned v) { asm volatile("" : "+s"(v)); return v; }
struct Rot { unsigned b[4]; };
DI Rot mkrot(int r, int shift, int N, int step) {
  const unsigned a = shift * step, w = N * step;
  return Rot{{sopaque(a), sopaque(a - (r >= 3 ? w : 0)), sopaque(a - (r >= 2 ? w : 0)), sopaque(a - (r >= 1 ? w : 0))}};
}
DI f32x16 mfma32(s16x8 a, u32x4 b, f32x16 c) { return __builtin_amdgcn_mfma_f32_32x32x16_bf16(__builtin_bit_cast(bf16x8, a), __builtin_bit_cast(bf16x8, b), c, 0, 0, 0); }
DI f32x4 mfma16(s16x8 a, u32x4 b, f32x4 c) { return __builtin_amdgcn_mfma_f32_16x16x32_bf16(__builtin_bit_cast(bf16x8, a), __builtin_bit_cast(bf16x8, b), c, 0, 0, 0); }
DI void smfma_c(f32x16& d, u32x4 a, u32x4 b, const f32x16& c) { asm("v_mfma_f32_32x32x16_bf16 %0, %1, %2, %3" : "=&v"(d) : "v"(a), "v"(b), "v"(c)); }
DI void smfma_0(f32x16& d, u32x4 a, u32x4 b) { asm("v_mfma_f32_32x32x16_bf16 %0, %1, %2, 0" : "=&v"(d) : "v"(a), "v"(b)); }
DI void smfma_a(f32x16& d, u32x4 a, u32x4 b) { asm("v_mfma_f32_32x32x16_bf16 %0, %1, %2, %0" : "+v"(d) : "v"(a), "v"(b)); }
DI void sfence(f32x16& d) { asm volatile("s_nop 11" : "+v"(d)); }
DI float fmix_lo(unsigned x, unsigned s, unsigned a) { float r; asm("v_fma_mix_f32 %0, %1, %2, %3 op_sel_hi:[1,1,1]" : "=v"(r) : "v"(x), "v"(s), "v"(a)); return r; }
DI float fmix_hi(unsigned x, unsigned s, unsigned a) { float r; asm("v_fma_mix_f32 %0, %1, %2, %3 op_sel:[1,1,1] op_sel_hi:[1,1,1]" : "=v"(r) : "v"(x), "v"(s), "v"(a)); return r; }
DI unsigned pkh(float a, float b) { return __builtin_bit_cast(unsigned, __builtin_amdgcn_cvt_pkrtz(a, b)); }

#define STAMP(i) do { } while (0)
#define STAMPN(i) do { } while (0)
#define STAMP_INIT() do { } while (0)
#define STAMP_FLUSH() do { } while (0)

template <int NS> DI void gemv16_issue(rsrc_t rw, unsigned rowstride, unsigned srcbyte, unsigned ldsw, int lane) {
  const unsigned vo = (8 * ((lane >> 3) & 3) + 4 * (lane >> 5) + ((lane >> 1) & 3)) * rowstride + 16 * (lane & 1);
#pragma unroll 5
  for (int s = 0; s < NS; ++s) dma16(rw, vo, s * 32 * rowstride + srcbyte, ldsw + s * 1024);
}
template <int NS> DI f32x4 gemv16_run(unsigned ldsw, unsigned ldsv, int vmask, int lane) {
  f32x4 acc0 = {}, acc1 = {};
  const unsigned wa = opaque(ldsw + 8 * lane), va = opaque(ldsv + (lane & vmask) * 1280 + (lane >> 4) * 16);
  s16x8 w[3]; u32x4 v[3];
#pragma unroll
  for (int s = 0; s < 2; ++s) { w[s] = trfrag(wa + s * 1024, wa + s * 1024 + 512); v[s] = lds_u4(va + s * 64); }
  SB();
#pragma unroll
  for (int s = 0; s < NS; ++s) {
    if (s + 2 < NS) { w[(s + 2) % 3] = trfrag(wa + (s + 2) * 1024, wa + (s + 2) * 1024 + 512); v[(s + 2) % 3] = lds_u4(va + (s + 2) * 64); }
    SB();
    if (s & 1) acc1 = mfma16(w[s % 3], v[s % 3], acc1); else acc0 = mfma16(w[s % 3], v[s % 3], acc0);
    SB();
  }
  return acc0 + acc1;
}

DI int ct_of(int p, int odd) { return odd ? (p < 4 ? p + 1 : 0) : p; }
DI int unit_of(int p, int odd, int nu) { const int u = odd ? (p < 6 ? p + 1 : 0) : p; return u < nu ? u : nu - 1; }

template <class RM> DI unsigned w80_voff(int piece, int lane, int nt, RM rowmap) {
  const int odd = nt & 1;
  int g, t, q, p, hf;
  if (piece < 4) { g = piece; t = lane >> 5; const int su = (lane >> 4) & 1; q = (lane >> 2) & 3; const int c = (lane & 3) ^ (2 * (piece & 1)); p = 2 * su + (c >> 1); hf = c & 1; }
  else { t = lane >> 5; g = (lane >> 3) & 3; q = (lane >> 1) & 3; p = 4; hf = lane & 1; }
  return rowmap(g, t, q) * 1280 + 160 * nt + 32 * ct_of(p, odd) + 16 * hf;
}

template <class OPS> constexpr int vm_sum(OPS ops, int lo, int hi) { int n = 0; for (int k = lo; k <= hi; ++k) if (k >= 0) n += ops(k); return n; }
constexpr int P1F0 = 10;
constexpr int p1_pieces(int k) { return k < 24 ? 1 : 0; }
constexpr int p1_ops(int k) { return (k < 40 - P1F0 ? 1 : 0) + p1_pieces(k); }
constexpr int p1_wait_frag(int f) {
  return f < P1F0 ? 8 + vm_sum(p1_ops, 0, f - 3) : p1_pieces(f - P1F0) + vm_sum(p1_ops, f - P1F0 + 1, f - 3);
}
constexpr int p1_wait_stage(int st) { return st == 2 ? 4 + vm_sum(p1_ops, 0, 5) : st == 3 ? vm_sum(p1_ops, 0, 9) : vm_sum(p1_ops, 4 * st - 12, 4 * st - 3); }
constexpr int P4F0 = 8;
constexpr int p4_pieces(int k) { return k >= 2 && k <= 25 ? ((k + 14) % 4 == 3 ? 1 : 2) : 0; }
constexpr int p4_wd_before(int k) { return k <= 26 ? 0 : k == 27 ? 2 : k == 28 ? 3 : k == 29 ? 5 : k == 30 ? 6 : k == 31 ? 8 : k == 32 ? 9 : k == 33 ? 11 : k == 34 ? 12 : k == 35 ? 14 : k <= 40 ? 16 + (k - 36) : 20; }
constexpr int p4_touch(int k) { return p4_wd_before(k + 1) - p4_wd_before(k); }
constexpr int p4_ops(int k) { return (k < 40 - P4F0 ? 1 : 0) + p4_pieces(k) + p4_touch(k); }
constexpr int p4_wait_frag(int ks) { return ks < P4F0 ? (P4F0 - 1 - ks) + vm_sum(p4_ops, 0, ks - 1) : p4_pieces(ks - P4F0) + p4_touch(ks - P4F0) + vm_sum(p4_ops, ks - P4F0 + 1, ks - 1); }
constexpr int p4_wait_stage(int st) { return vm_sum(p4_ops, 4 * st - 10, 4 * st - 3); }
constexpr int P5F0 = 12;
constexpr int p5_pieces(int k) { return k >= 10 && k <= 33 ? ((k + 20) % 6 < 2 ? 2 : 1) : 0; }
constexpr int p5_ops(int k) { return (k < 54 - P5F0 ? 1 : 0) + p5_pieces(k); }
constexpr int p5_wait_frag(int s) { return s < P5F0 ? (P5F0 - 1 - s) + vm_sum(p5_ops, 0, s - 1) : p5_pieces(s - P5F0) + vm_sum(p5_ops, s - P5F0 + 1, s - 1); }
constexpr int p5_wait_group(int g) { return vm_sum(p5_ops, 6 * g - 14, 6 * g - 3); }
constexpr int P3F0 = 6;
constexpr int p3_wait_frag(int s) { return s < P3F0 ? (P3F0 - 1 - s) + s : (s < 20 - P3F0 ? P3F0 - 1 : 19 - s); }

template <int I, int N, class Fn> DI void static_for(Fn&& f) { if constexpr (I < N) { f(std::integral_constant<int, I>{}); static_for<I + 1, N>(f); } }

__global__ __launch_bounds__(256, 1) void adaln_block_kernel(const char* __restrict__ x, const char* __restrict__ wqkv, const char* __restrict__ wo,
                                                             const char* __restrict__ wg, const char* __restrict__ wu, const char* __restrict__ wd,
                                                             const char* __restrict__ cvec, const char* __restrict__ wmod, char* out, char* ws) {
  __shared__ __attribute__((aligned(16))) char smem[LDS_BYTES];
  const unsigned L0 = (unsigned)(size_t)(LAS char*)smem;
  const int tid = threadIdx.x, lane = tid & 63;
  const int wave = __builtin_amdgcn_readfirstlane(tid >> 6);
  const int bid = blockIdx.x, xcd = bid & 7, ii = bid >> 3;
  const int mbL = 2 * xcd + (ii >> 4), jL = ii & 15;
  const int mb = 4 * (xcd >> 1) + (ii >> 3), j = 8 * (xcd & 1) + (ii & 7);
  const int rW = mb & 3, rL = 0, rD = 2 * (mbL & 1) + (jL & 1);
  const int soW = (5 * rW) >> 1;
  const int goL = rD == 3 ? 7 : 2 * rD;
  const unsigned tag = *(const GAS unsigned*)(ws + WS_EPOCH) + 1u;
  const rsrc_t rx = mkrsrc(x, S_ * D_ * 2), rws = mkrsrc(ws, WS_USED);
  STAMP_INIT();
  STAMP(0);

  const rsrc_t rwq = mkrsrc(wqkv, D_ * 3 * D_ * 2);
  unsigned wqo[4];
  auto rot10 = [&](int so, int st) { const int g = st + so; return g >= 10 ? g - 10 : g; };
  const int so1 = (int)sopaque(soW);
  auto wq_piece = [&](int st, int i) { dma16(rwq, wqo[i], rot10(so1, st) * 64 * 3840, L0 + L1_W + (st * 15 + min(4 * wave + i, 14)) * 1024); };
  auto fo40 = [&](const Rot& R, int ks) { return R.b[ks < 12 ? 0 : ks < 20 ? 1 : ks < 32 ? 2 : 3] + ks * 1024; };
  auto wq_issue = [&](int st) {
#pragma unroll
    for (int i = 0; i < 4; ++i) wq_piece(st, i);
  };
  {
    const int c32 = lane & 31, tok = 128 * mbL + 8 * jL + 2 * wave + (lane >> 5);
    u32x4 xv[3], c4 = {}; unsigned c1 = 0;
    ald16(xv[0], rx, tok * 1280 + c32 * 16, 0);
    ald16(xv[1], rx, tok * 1280 + c32 * 16 + 512, 0);
    ald16(xv[2], rx, c32 < 16 ? tok * 1280 + c32 * 16 + 1024 : 0x40000000u, 0);
    const int munit = ii < 10 ? 10 * xcd + ii : 80 + 20 * xcd + (ii - 10);
    const bool mjob = wave == 0 && ii < 30;
    if (mjob) {
      const rsrc_t rc = mkrsrc(cvec, D_ * 2);
      ald16(c4, rc, 20 * lane, 0); ald4(c1, rc, 20 * lane + 16, 0);
    }
#pragma unroll
    for (int i = 0; i < 4; ++i) {
      const int idx = 64 * min(4 * wave + i, 14) + lane, rq = idx / 60, rem = idx - 60 * rq;
      int q, ch;
      if (rem < 48) { q = (rem >> 2) & 3; ch = 4 * (rem >> 4) + (rem & 3); } else { const int r2 = rem - 48; q = r2 / 3; ch = 12 + r2 - 3 * q; }
      const int seg = ch / 5;
      wqo[i] = (4 * rq + q) * 3840 + seg * 1280 + 80 * j + (ch - 5 * seg) * 16;
    }
    if (mjob) {
      gemv16_issue<20>(mkrsrc(wmod, D_ * 6 * D_ * 2), 7680, 32 * munit, L0 + L0_JW, lane);
      vmwait<20>();
    } else {
      vmwait<0>();
    }
    asm volatile("" : "+v"(xv[0]), "+v"(xv[1]), "+v"(xv[2]), "+v"(c4), "+v"(c1));
    STAMPN(48);
    f32x2 s2 = {0.f, 0.f};
#pragma unroll
    for (int i = 0; i < 3; ++i)
#pragma unroll
      for (int e = 0; e < 4; ++e) s2 += unpk(xv[i][e]);
    const float mean = xsum16(sum16(s2.x + s2.y)) * (1.0f / D_);
    f32x2 q2 = {0.f, 0.f}, q3 = {0.f, 0.f};
#pragma unroll
    for (int e = 0; e < 4; ++e) {
      const f32x2 d0 = unpk(xv[0][e]) - mean, d1 = unpk(xv[1][e]) - mean, d2 = unpk(xv[2][e]) - mean;
      q2 += d0 * d0; q2 += d1 * d1; q3 += d2 * d2;
    }
    const float var = xsum16(sum16(q2.x + q2.y + (c32 < 16 ? q3.x + q3.y : 0.f))) * (1.0f / D_);
    const float mq = bf16r(mean), rden = rden_of(var);
#pragma unroll
    for (int i = 0; i < 3; ++i) {
      u32x4 o;
#pragma unroll
      for (int e = 0; e < 4; ++e) { const f32x2 d = bf16r2(bf16r2(unpk(xv[i][e]) - mq) * rden); o[e] = pkh(d.x, d.y); }
      const int slot = c32 + 32 * i;
      if (i < 2 || c32 < 16) lds_st4(L0 + L0_TB + slot * 128 + (((2 * wave + (lane >> 5)) ^ (slot & 7)) * 16), o);
    }
    STAMPN(49);
    BAR();
    STAMPN(50);
#pragma unroll
    for (int u = 0; u < 3; ++u) {
      const int i = 256 * u + tid, slot = i >> 3, tk = (i & 7) ^ (slot & 7);
      const unsigned voff = (u < 2 || tid < 128) ? WS_XN + (4 * mbL + (jL >> 2)) * T32_D + slot * 512 + (8 * (jL & 3) + tk) * 16 : 0x40000000u;
      st16_sc1(lds_u4(L0 + L0_TB + min(i, 639) * 16), rws, voff, 0);
    }
    STAMPN(51);
    wq_issue(0);
    vmwait<4>();
    if (lane == 0) st_u64(ws, flagoff(FL_XN, mbL, 4 * jL + wave), flagval(tag));
    STAMP(1);
    if (mjob) {
      const unsigned cc[5] = {c4[0], c4[1], c4[2], c4[3], c1};
#pragma unroll
      for (int r = 0; r < 5; ++r) lds_st1(L0 + L0_SC + 20 * lane + 4 * r, pk2(siluf(blo(cc[r])), siluf(bhi(cc[r]))));
      asm volatile("s_waitcnt lgkmcnt(0)" ::: "memory");
      const f32x4 d = gemv16_run<20>(L0 + L0_JW, L0 + L0_SC, 0, lane);
      const unsigned pv = (lane & 1) ? pk2(d[2], d[3]) : pk2(d[0], d[1]);
      if ((lane & 15) < 2) st_u64(ws, WS_GRAN + (8 * munit + 2 * (lane >> 4) + (lane & 1)) * 8, ((u64)tag << 32) | pv);
    }
    STAMPN(52);
    wq_issue(1);
    STAMPN(53);
  }
  if (wave == 0) wait_flags(ws, flagoff(FL_XN, mb, lane), true, tag);
  BAR_RAW();
  STAMP(17);
  u32x4 xf[40];
  const unsigned xfo = WS_XN + (4 * mb + wave) * T32_D + 16 * lane;
  const Rot R1 = mkrot(rW, 4 * soW, 40, 1024);
  {
    const int g0 = 160 * wave + lane;
    u32x2 ga[3];
#pragma unroll
    for (int ks = 0; ks < P1F0; ++ks) ald16_sc1(xf[ks], rws, xfo, fo40(R1, ks));
#pragma unroll
    for (int k = 0; k < 3; ++k) ald8_sc1(ga[k], rws, WS_GRAN + (k < 2 || lane < 32 ? g0 + 64 * k : g0) * 8, 0);
    wq_issue(2); wq_issue(3);
    vmwait<8>();
    asm volatile("" : "+v"(ga[0]), "+v"(ga[1]), "+v"(ga[2]));
    unsigned gv[3] = {ga[0][0], ga[1][0], ga[2][0]};
    if (!__all(ga[0][1] == tag && ga[1][1] == tag && ga[2][1] == tag))
      sweep<3>(ws, [&](int k) { return WS_GRAN + (g0 + 64 * k) * 8; }, [&](int k) { return k < 2 || lane < 32; }, tag, gv);
    STAMP(18);
#pragma unroll
    for (int k = 0; k < 3; ++k) if (k < 2 || lane < 32) {
      const int g = g0 + 64 * k;
      f32x2 v = unpk(gv[k]);
      if (g >= 320) v = bf16r2(v + 1.0f);
      const int gp = g >= 320 ? g - 320 : g;
      int kp = (gp >> 3) - 4 * soW; kp += kp < 0 ? 40 : 0;
      lds_st1(L0 + (g >= 320 ? L1_VS : L1_VA) + kp * 32 + (gp & 7) * 4, pkh(v.x, v.y));
    }
  }
  BAR();
  STAMP(2);

  f32x16 acc1[4];
#pragma unroll
  for (int b = 0; b < 4; ++b) acc1[b] = (f32x16){};
  {
    const int h = lane >> 5;
    const unsigned wb = opaque(L0 + L1_W + 2 * h * 960 + ((lane & 15) >> 2) * 64 + ((lane >> 4) & 1) * 32 + (lane & 3) * 8);
    const unsigned wb3 = opaque(L0 + L1_W + 2 * h * 960 + 768 + ((lane & 15) >> 2) * 48 + ((lane >> 4) & 1) * 32 + (lane & 3) * 8);
    const unsigned wbh = opaque(wb + 61440), wb3h = opaque(wb3 + 61440), wbg = opaque(wb + 122880), wb3g = opaque(wb3 + 122880);
    const unsigned vb = opaque(L0 + L1_VS + h * 16);
    s16x8 w[3][4]; u32x4 hf[2], vs[2], va[2]; float ft[2][8];
    auto readw = [&](int ks, int b) {
      const int kl = ks % 16; const unsigned base = ks < 16 ? (b < 3 ? wb : wb3) : ks < 32 ? (b < 3 ? wbh : wb3h) : (b < 3 ? wbg : wb3g);
      const unsigned a = base + (b < 3 ? b * 256 : 0) + kl * 3840; return trfrag(a, a + 960); };
    auto reads = [&](int ks) { vs[ks & 1] = lds_u4(vb + ks * 32); };
    auto reada = [&](int ks) { va[ks & 1] = lds_u4(vb + 1280 + ks * 32); };
    auto fstage = [&](int ks, int e) {
      ft[ks & 1][2 * e] = fmix_lo(xf[ks][e], vs[ks & 1][e], va[ks & 1][e]);
      ft[ks & 1][2 * e + 1] = fmix_hi(xf[ks][e], vs[ks & 1][e], va[ks & 1][e]); };
    auto cstage = [&](int ks, int e) { hf[ks & 1][e] = pk2(ft[ks & 1][2 * e], ft[ks & 1][2 * e + 1]); };
#pragma unroll
    for (int b = 0; b < 4; ++b) { w[0][b] = readw(0, b); w[1][b] = readw(1, b); }
    reads(0); reada(0); reads(1); reada(1);
    vmwait<p1_wait_frag(0)>(xf[0]);
#pragma unroll
    for (int e = 0; e < 4; ++e) fstage(0, e);
#pragma unroll
    for (int e = 0; e < 4; ++e) cstage(0, e);
    vmwait<p1_wait_frag(1)>(xf[1]);
#pragma unroll
    for (int e = 0; e < 4; ++e) fstage(1, e);
    reads(2); reada(2);
    SB();
    static_for<0, 40>([&](auto KS) {
      constexpr int ks = KS, c = ks & 1, wc = ks % 3, wn = (ks + 2) % 3;
      constexpr bool needf = ks + 2 < 40, needs = (ks & 3) == 2 && (ks + 2) / 4 >= 2 && ks + 2 < 40;
      constexpr int nf = needf ? p1_wait_frag(ks + 2) : 63, ns = needs ? p1_wait_stage((ks + 2) / 4) : 63;
      if constexpr (needs) { vmwait<(nf < ns ? nf : ns)>(); BAR_RAW(); if constexpr (ks >= 14) STAMPN(24 + (ks - 14) / 4); }
      if constexpr (needf) vmwait<nf>(xf[ks + 2]);
      SB();
      acc1[0] = mfma32(w[wc][0], hf[c], acc1[0]); SB();
      if constexpr (ks + 2 < 40) w[wn][0] = readw(ks + 2, 0);
      if constexpr (ks + 1 < 40) { cstage(ks + 1, 0); cstage(ks + 1, 1); }
      if constexpr (ks + 3 < 40) reads(ks + 3);
      SB();
      acc1[1] = mfma32(w[wc][1], hf[c], acc1[1]); SB();
      if constexpr (ks + P1F0 < 40) ald16_sc1(xf[ks + P1F0], rws, xfo, fo40(R1, ks + P1F0));
      if constexpr (ks + 2 < 40) w[wn][1] = readw(ks + 2, 1);
      if constexpr (ks + 1 < 40) { cstage(ks + 1, 2); cstage(ks + 1, 3); }
      if constexpr (ks + 3 < 40) reada(ks + 3);
      SB();
      acc1[2] = mfma32(w[wc][2], hf[c], acc1[2]); SB();
      if constexpr (ks + 2 < 40) w[wn][2] = readw(ks + 2, 2);
      if constexpr (ks + 2 < 40) { fstage(ks + 2, 0); fstage(ks + 2, 1); }
      SB();
      acc1[3] = mfma32(w[wc][3], hf[c], acc1[3]); SB();
      if constexpr (ks < 24) wq_piece(ks / 4 + 4, ks & 3);
      if constexpr (ks + 2 < 40) w[wn][3] = readw(ks + 2, 3);
      if constexpr (ks + 2 < 40) { fstage(ks + 2, 2); fstage(ks + 2, 3); }
      SB();
    });
  }
  STAMP(3);

  u32x4 qa0, qa1, qb0, qb1;
  {
    const int h = lane >> 5, t = lane & 31;
    u32x2 P[15];
#pragma unroll
    for (int i = 0; i < 15; ++i) { const f32x16& a = acc1[i >> 2]; const int r = 4 * (i & 3); P[i] = (u32x2){pk2(a[r], a[r + 1]), pk2(a[r + 2], a[r + 3])}; }
    const u32x2 one = {0x00003f80u, 0u}, zero = {0u, 0u};
    auto cat = [](u32x2 a, u32x2 b) { return (u32x4){a[0], a[1], b[0], b[1]}; };
    auto sel = [&](u32x2 a1, u32x2 a0) { return (u32x2){h ? a1[0] : a0[0], h ? a1[1] : a0[1]}; };
    const unsigned row = L0 + L2_KV + (256 + 32 * wave + t) * 48;
    lds_st4(row + 16 * h, cat(P[5], P[6]));
    lds_st4(row + KV_IMG + 16 * (h ^ 1), cat(sel(P[7], P[8]), sel(P[8], P[9])));
    lds_st4(row + h * KV_IMG + 32, cat(sel(P[9], P[7]), zero));
    lds_st4(row + 2 * KV_IMG + 16 * h, cat(P[10], P[11]));
    lds_st4(row + 3 * KV_IMG + 16 * (h ^ 1), cat(sel(P[12], P[13]), sel(P[13], P[14])));
    lds_st4(row + (2 + h) * KV_IMG + 32, cat(sel(P[14], P[12]), one));
    asm volatile("s_waitcnt lgkmcnt(0)" ::: "memory");
#pragma unroll
    for (int u = 0; u < 6; ++u) {
      const int i = 64 * u + lane, a = i / 96, o = (i - 96 * a) * 16;
      const u32x4 v = lds_u4(L0 + L2_KV + a * KV_IMG + (256 + 32 * wave) * 48 + o);
      st16_sc1(v, rws, WS_KV + ((mb * 16 + j) * 4 + a) * 6144 + wave * 1536 + o, 0);
    }
    qa0 = cat(P[0], P[1]);
    qa1 = cat(sel(zero, P[2]), sel(zero, one));
    qb0 = cat(sel(P[2], P[3]), sel(P[3], P[4]));
    qb1 = cat(sel(P[4], zero), sel(one, zero));
    publish(ws, FL_KV, mb, j, wave, lane, tag);
  }
  STAMP(4);
  const int th = jL & 1, nt = jL >> 1;
  const rsrc_t rwo = mkrsrc(wo, D_ * D_ * 2);
  unsigned woo[7], wol[7];
#pragma unroll
  for (int i = 0; i < 7; ++i) {
    const int pi = min(wave + 4 * i, 24), kc = pi / 5, pc = pi - 5 * kc;
    auto rowmap = [&](int g, int t, int q) { const int k = 32 * kc + 8 * g + 4 * t + q, hp = k / 40, r = k - 40 * hp; return 40 * hp + (r < 20 ? r : r < 24 ? r + 16 : r - 4); };
    woo[i] = w80_voff(pc, lane, nt, rowmap);
    wol[i] = pi * 1024;
  }
  auto wo_piece = [&](int gam, int i) { dma16(rwo, woo[i], ((gam + rL) & 3) * 160 * 1280, L0 + (gam < 3 ? L3_W0 + gam * 25600 : L3_W1) + wol[i]); };
  {
    const int blk = wave >> 1, src = mb - 2 + blk;
    if (src >= 0) {
      wait_flags(ws, flagoff(FL_KV, src, 4 * j + (lane & 3)), lane < 4, tag);
      STAMP(5);
#pragma unroll
      for (int i = 0; i < 12; ++i) {
        const int a = 2 * (wave & 1) + i / 6, sub = i % 6;
        dma16_sc1(rws, 16 * lane, WS_KV + ((src * 16 + j) * 4 + a) * 6144 + sub * 1024, L0 + L2_KV + a * KV_IMG + blk * 6144 + sub * 1024);
      }
    } else {
      STAMP(5);
#pragma unroll
      for (int i = 0; i < 12; ++i) {
        const int a = 2 * (wave & 1) + i / 6, o = (i % 6) * 1024 + 16 * lane;
        lds_st4(L0 + L2_KV + a * KV_IMG + blk * 6144 + o, (u32x4){0u, 0u, ((o % 48) == 32 && a < 2) ? 0x0000f149u : 0u, 0u});
      }
    }
  }
  vmwait<0>();
  BAR();
  STAMP(6);

  {
    const rsrc_t rgt = mkrsrc(wave < 2 ? wg : wu, D_ * F_ * 2);
    const unsigned tgo = (20 * ii + 10 * (wave & 1) + (lane >> 5)) * 3456 + (xcd & 1) * 1792 + 64 * min(lane & 31, (xcd & 1) ? 25 : 27);
    const int h = lane >> 5, t = lane & 31;
    f32x16 mfirst, mdiag;
#pragma unroll
    for (int r = 0; r < 16; ++r) { const int key = (r & 3) + 8 * (r >> 2) + 4 * h; mfirst[r] = key > t ? 0.f : NEGBIG; mdiag[r] = key > t ? NEGBIG : 0.f; }
    const unsigned krow = L0 + L2_KV + (32 * wave + t) * 48;
    const unsigned ka0 = opaque(krow + 16 * h), ka1 = opaque(krow + 32), kb0 = opaque(krow + KV_IMG + 16 * (h ^ 1)), kb1 = opaque(krow + KV_IMG + 32);
    const unsigned vrow = L0 + L2_KV + 2 * KV_IMG + (32 * wave + 4 * h + ((lane & 15) >> 2)) * 48 + ((lane >> 4) & 1) * 32 + (lane & 3) * 8;
    const unsigned vra = opaque(vrow), vrb = opaque(vrow + KV_IMG);
    f32x16 sa[9], sb[9];
    auto qk = [&](f32x16& s, int kb, u32x4 a0, u32x4 a1, u32x4 q0, u32x4 q1) {
      if (kb == 0) smfma_c(s, a0, q0, mfirst); else if (kb == 8) smfma_c(s, a0, q0, mdiag); else smfma_0(s, a0, q0);
      smfma_a(s, a1, q1); };
    float e[8];
    auto pfma = [&](const f32x16& s, int half, float mc) {
#pragma unroll
      for (int i = 0; i < 8; ++i) e[i] = fmaf(s[8 * half + i], SCORE_C, mc); };
    auto pexp = [&]() {
#pragma unroll
      for (int i = 0; i < 8; ++i) e[i] = exp2_(e[i]); };
    auto pcvt = [&]() { return (u32x4){pk2(e[0], e[1]), pk2(e[2], e[3]), pk2(e[4], e[5]), pk2(e[6], e[7])}; };
    auto qk0 = [&](f32x16& s, int kb, u32x4 a0, u32x4 q0) { if (kb == 0) smfma_c(s, a0, q0, mfirst); else if (kb == 8) smfma_c(s, a0, q0, mdiag); else smfma_0(s, a0, q0); };
    auto maxhalf = [&](float (&m)[4], const f32x16& s, int half) {
#pragma unroll
      for (int r = 8 * half; r < 8 * half + 8; r += 2) m[(r >> 1) & 3] = fmaxf(fmaxf(m[(r >> 1) & 3], s[r]), s[r + 1]); };
    auto vfrag = [&](unsigned base, int kb, int half) { const unsigned a = base + (32 * kb + 16 * half) * 48; return trfrag(a, a + 8 * 48); };
    auto maxblk = [&](float (&m)[4], const f32x16& s) {
#pragma unroll
      for (int r = 0; r < 16; r += 2) m[(r >> 1) & 3] = fmaxf(fmaxf(m[(r >> 1) & 3], s[r]), s[r + 1]); };
    u32x4 k0 = lds_u4(ka0), k1 = lds_u4(ka1);
    float mx[4] = {NEGBIG, NEGBIG, NEGBIG, NEGBIG};
#pragma unroll
    for (int kb = 0; kb < 9; ++kb) {
      u32x4 n0, n1;
      SB();
      qk0(sa[kb], kb, k0, qa0);
      SB();
      n0 = kb < 8 ? lds_u4(ka0 + (kb + 1) * 1536) : lds_u4(kb0);
      if (kb == 2) asm volatile("s_nop 3" : "+v"(sa[0]));
      if (kb >= 2) maxhalf(mx, sa[kb - 2], 0);
      SB();
      smfma_a(sa[kb], k1, qa1);
      SB();
      n1 = kb < 8 ? lds_u4(ka1 + (kb + 1) * 1536) : lds_u4(kb1);
      if (kb >= 2) { maxhalf(mx, sa[kb - 2], 1); wo_piece(2, kb - 2); }
      k0 = n0; k1 = n1;
    }
    SB();
    asm volatile("s_nop 7" : "+v"(sa[7]));
    maxblk(mx, sa[7]);
    sfence(sa[8]);
    maxblk(mx, sa[8]);
    const float mca = -SCORE_C * xmax32(fmaxf(fmaxf(mx[0], mx[1]), fmaxf(mx[2], mx[3])));
    f32x16 oa = {}, ob = {};
    s16x8 v0 = vfrag(vra, 0, 0), v1 = vfrag(vra, 0, 1);
#pragma unroll
    for (int r = 0; r < 4; ++r) mx[r] = NEGBIG;
    SB();
#pragma unroll
    for (int kb = 0; kb < 9; ++kb) {
      qk0(sb[kb], kb, k0, qb0);
      SB();
      if (kb < 8) k0 = lds_u4(kb0 + (kb + 1) * 1536);
      pfma(sa[kb], 0, mca); pexp();
      SB();
      smfma_a(sb[kb], k1, qb1);
      SB();
      if (kb < 8) k1 = lds_u4(kb1 + (kb + 1) * 1536);
      const u32x4 p0 = pcvt();
      pfma(sa[kb], 1, mca);
      SB();
      oa = mfma32(v0, p0, oa);
      SB();
      v0 = kb < 8 ? vfrag(vra, kb + 1, 0) : vfrag(vrb, 0, 0);
      pexp();
      const u32x4 p1 = pcvt();
      if (kb >= 1) maxhalf(mx, sb[kb - 1], 0);
      SB();
      oa = mfma32(v1, p1, oa);
      SB();
      v1 = kb < 8 ? vfrag(vra, kb + 1, 1) : vfrag(vrb, 0, 1);
      if (kb >= 1) maxhalf(mx, sb[kb - 1], 1);
      if (kb < 7) wo_piece(0, kb);
      if (kb < 5) touch(rgt, tgo, kb * 2 * 3456, L0 + L_DUMMY);
      SB();
    }
    sfence(sb[8]);
    maxblk(mx, sb[8]);
    const float mcb = -SCORE_C * xmax32(fmaxf(fmaxf(mx[0], mx[1]), fmaxf(mx[2], mx[3])));
    SB();
    pfma(sb[0], 0, mcb); pexp();
    SB();
#pragma unroll
    for (int kb = 0; kb < 9; ++kb) {
      const u32x4 p0 = pcvt();
      pfma(sb[kb], 1, mcb);
      SB();
      ob = mfma32(v0, p0, ob);
      SB();
      if (kb < 8) v0 = vfrag(vrb, kb + 1, 0);
      pexp();
      const u32x4 p1 = pcvt();
      if (kb < 8) pfma(sb[kb + 1], 0, mcb);
      SB();
      ob = mfma32(v1, p1, ob);
      SB();
      if (kb < 8) { v1 = vfrag(vrb, kb + 1, 1); pexp(); }
      if (kb < 7) wo_piece(1, kb);
      SB();
    }
    const auto la = __builtin_amdgcn_permlane32_swap(__float_as_uint(oa[8]), __float_as_uint(oa[8]), false, false);
    const auto lb = __builtin_amdgcn_permlane32_swap(__float_as_uint(ob[8]), __float_as_uint(ob[8]), false, false);
    const float ia = rcpf_(__uint_as_float(la[1])), ib = rcpf_(__uint_as_float(lb[1]));
    const unsigned obase = WS_O + (4 * mb + wave) * T32_D + 5 * j * 512;
    u32x4 va, vb2, vc;
#pragma unroll
    for (int e = 0; e < 4; ++e) { va[e] = pk2(oa[2 * e] * ia, oa[2 * e + 1] * ia); vb2[e] = pk2(ob[2 * e] * ib, ob[2 * e + 1] * ib); }
    vc = (u32x4){pk2(oa[8] * ia, oa[9] * ia), pk2(oa[10] * ia, oa[11] * ia), pk2(ob[8] * ib, ob[9] * ib), pk2(ob[10] * ib, ob[11] * ib)};
    STAMP(23);
    st16_sc1(va, rws, obase + 16 * lane, 0);
    st16_sc1(vb2, rws, obase + 3 * 512 + 16 * lane, 0);
    st16_sc1(vc, rws, lane < 32 ? obase + 2 * 512 + 16 * lane : 0x40000000u, 0);
    publish(ws, FL_O, mb, j, wave, lane, tag);
  }
  BAR();
  STAMP(7);
  const int t16 = lane & 15, g4 = lane >> 4;
  const int tok3 = 128 * mbL + 64 * th + 16 * wave + t16;
  u32x2 xres[5];
  const int jidx = 32 * (xcd >> 1) + ii, jper = (xcd & 1) ? 52 : 56;
  const bool ujob = jidx < 2 * jper;
  const int jmat = jidx >= jper, junit = ((xcd & 1) ? 56 : 0) + jidx - jmat * jper;
  auto jgi = [&](int k) { const int i = 64 * k + lane; return i < 80 ? 1280 + 80 * wave + i : i < 160 ? 960 + 80 * wave + i - 80 : 1280; };
  auto goff = [&](int k) { const int i = min(64 * k + lane, 119), kind = i / 40, r = i - 40 * kind; return WS_GRAN + ((kind == 0 ? 640 : kind == 1 ? 1280 : 1600) + 40 * nt + r) * 8; };
  u32x2 jg[3], vg2[2];
  gemv16_issue<5>(mkrsrc(jmat ? wu : wg, D_ * F_ * 2), 3456, 32 * (ujob ? junit : 0) + wave * 160 * 3456, L0 + L2_JW + wave * 5120, lane);
#pragma unroll
  for (int k = 0; k < 3; ++k) ald8_sc1(jg[k], rws, WS_GRAN + jgi(k) * 8, 0);
#pragma unroll
  for (int k = 0; k < 2; ++k) ald8_sc1(vg2[k], rws, goff(k), 0);
#pragma unroll
  for (int i = 0; i < 7; ++i) wo_piece(3, i);
#pragma unroll
  for (int p = 0; p < 5; ++p) xres[p] = ld8(rx, tok3 * 1280 + 160 * nt + 32 * ct_of(p, nt & 1) + 8 * g4, 0);
  MEMFENCE();
  const int nu = j < 12 ? 7 : 6, u0 = j < 12 ? 7 * j : 84 + 6 * (j - 12), uodd = u0 & 1;
  const rsrc_t rgu = mkrsrc(wave < 2 ? wg : wu, D_ * F_ * 2);
  unsigned guo[7];
#pragma unroll
  for (int i = 0; i < 7; ++i) {
    const int isup = wave >> 1, idx = 64 * (7 * (wave & 1) + i) + lane, rq = idx / 56, rem = idx - 56 * rq;
    int q, p, hf;
    if (rem < 48) { q = (rem >> 2) & 3; const int c = (rem & 3) ^ (isup ? 2 : 0); p = 2 * (rem >> 4) + (c >> 1); hf = c & 1; } else { q = (rem - 48) >> 1; p = 6; hf = rem & 1; }
    guo[i] = opaque((4 * rq + q) * 3456 + 32 * (u0 + unit_of(p, uodd, nu)) + 16 * hf);
  }
  vmwait<12>();
  asm volatile("" : "+v"(jg[0]), "+v"(jg[1]), "+v"(jg[2]), "+v"(vg2[0]), "+v"(vg2[1]));
  if (wave == 2) {
    unsigned gv[2] = {vg2[0][0], vg2[1][0]};
    if (!__all(vg2[0][1] == tag && vg2[1][1] == tag)) sweep<2>(ws, goff, [&](int) { return true; }, tag, gv);
#pragma unroll
    for (int k = 0; k < 2; ++k) if (k == 0 || lane < 56) {
      const int i = 64 * k + lane, kind = i / 40, r = i - 40 * kind;
      const int ct = r >> 3, p = (nt & 1) ? (ct == 0 ? 4 : ct - 1) : ct;
      f32x2 v = unpk(gv[k]);
      if (kind == 1) v = bf16r2(v + 1.0f);
      lds_st2f(L0 + L3_VEC + kind * 320 + p * 64 + (r & 7) * 8, v);
    }
  }
  {
    unsigned gv[3] = {jg[0][0], jg[1][0], jg[2][0]};
    if (!__all(jg[0][1] == tag && jg[1][1] == tag && jg[2][1] == tag))
      sweep<3>(ws, [&](int k) { return WS_GRAN + jgi(k) * 8; }, [&](int) { return true; }, tag, gv);
#pragma unroll
    for (int k = 0; k < 3; ++k) if (k < 2 || lane < 32) {
      const int i = 64 * k + lane;
      unsigned v = gv[k];
      if (i < 80) { const f32x2 t = unpk(v) + 1.0f; v = pk2(t.x, t.y); }
      lds_st1(L0 + L2_JV + (i < 80 ? 320 * wave + 4 * i : 1280 + 320 * wave + 4 * (i - 80)), v);
    }
    asm volatile("s_waitcnt lgkmcnt(0)" ::: "memory");
    const f32x4 d = gemv16_run<5>(L0 + L2_JW + wave * 5120, L0 + L2_JV + wave * 320, 1, lane);
    lds_st4(L0 + L2_JV + 2560 + wave * 1024 + lane * 16, __builtin_bit_cast(u32x4, d));
  }
  if (wave == 3 && bid == 0) {
    unsigned fv[16];
    sweep<16>(ws, [&](int k) { return flagoff(FL_KV, k, lane); }, [&](int) { return true; }, tag, fv);
    if (lane == 0) __hip_atomic_store((GAS unsigned*)(ws + WS_EPOCH), tag, __ATOMIC_RELAXED, __HIP_MEMORY_SCOPE_AGENT);
  }
  if (wave == 0) wait_flags(ws, flagoff(FL_O, mbL, lane), true, tag);
  vmwait<0>();
  STAMP(20);
  BAR();
  STAMP(8);

  const unsigned b80_0 = g4 * 1024 + ((lane & 15) >> 2) * 64 + (g4 & 1) * 32 + (lane & 3) * 8;
  const unsigned b80_1 = b80_0 ^ 32u;
  const unsigned b80_2 = 4096 + 8 * lane;
  struct B80 { unsigned a, b, c; };
  auto b80 = [&](unsigned kbase) { return B80{opaque(kbase + b80_0), opaque(kbase + b80_1), opaque(kbase + b80_2)}; };
  auto w80 = [&](const B80& B, int koff, int p) { const unsigned a = (p == 4 ? B.c : ((p & 1) ? B.b : B.a) + (p >> 1) * 256) + koff; return trfrag(a, a + 512); };

  u32x2 x1p[5];
  {
    u32x4 of[20];
    const unsigned ofb = WS_O + (4 * mbL + 2 * th + (wave >> 1)) * T32_D + g4 * 512 + ((wave & 1) * 16 + t16) * 16;
    const Rot R3 = mkrot(rL, 5 * rL, 20, 2048);
    auto fo20 = [&](int s) { return R3.b[s < 5 ? 0 : s < 10 ? 1 : s < 15 ? 2 : 3] + s * 2048; };
#pragma unroll
    for (int s = 0; s < P3F0; ++s) ald16_sc1(of[s], rws, ofb, fo20(s));
    f32x4 acc[5];
#pragma unroll
    for (int p = 0; p < 5; ++p) acc[p] = (f32x4){};
    const B80 B0 = b80(L0 + L3_W0), B1 = b80(L0 + L3_W0 + 10 * 5120), B2 = b80(L0 + L3_W1);
    auto rd3 = [&](int s, int p) { return s < 10 ? w80(B0, s * 5120, p) : s < L3_NPRE ? w80(B1, (s - 10) * 5120, p) : w80(B2, (s - L3_NPRE) * 5120, p); };
    s16x8 w[3][5];
#pragma unroll
    for (int p = 0; p < 5; ++p) { w[0][p] = rd3(0, p); w[1][p] = rd3(1, p); }
    SB();
    static_for<0, 20>([&](auto SS) {
      constexpr int s = SS;
      vmwait<p3_wait_frag(s)>(of[s]);
      SB();
#pragma unroll
      for (int p = 0; p < 5; ++p) {
        acc[p] = mfma16(w[s % 3][p], of[s], acc[p]);
        SB();
        if (p == 0 && s + P3F0 < 20) ald16_sc1(of[s + P3F0 < 20 ? s + P3F0 : 0], rws, ofb, fo20(s + P3F0));
        if (s + 2 < 20) w[(s + 2) % 3][p] = rd3(s + 2, p);
        SB();
      }
    });
    BAR();
    STAMP(9);
    if (ujob && wave == 1) {
      f32x4 d = lds_f4(L0 + L2_JV + 2560 + lane * 16);
#pragma unroll
      for (int w = 1; w < 4; ++w) d += lds_f4(L0 + L2_JV + 2560 + w * 1024 + lane * 16);
      if ((lane & 15) < 2) {
#pragma unroll
        for (int r = 0; r < 4; ++r) st_u64(ws, WS_UV + ((jmat * 108 + junit) * 32 + (lane & 15) * 16 + 4 * (lane >> 4) + r) * 8, ((u64)tag << 32) | __float_as_uint(d[r]));
      }
    }

    f32x2 xv[5][2]; f32x2 s2 = {0.f, 0.f};
    u32x2 yp[5];
    const unsigned vecb = opaque(L0 + L3_VEC + g4 * 16);
#pragma unroll
    for (int p = 0; p < 5; ++p) {
      const f32x4 ga = lds_f4(vecb + p * 64), sb1 = lds_f4(vecb + 320 + p * 64);
#pragma unroll
      for (int r = 0; r < 2; ++r) {
        const f32x2 at = bf16r2((f32x2){acc[p][2 * r], acc[p][2 * r + 1]});
        const f32x2 xs = unpk(xres[p][r]) + bf16r2(at * (f32x2){ga[2 * r], ga[2 * r + 1]});
        x1p[p][r] = pk2(xs.x, xs.y);
        xv[p][r] = unpk(x1p[p][r]);
        s2 += xv[p][r];
        const f32x2 y = xv[p][r] * (f32x2){sb1[2 * r], sb1[2 * r + 1]};
        yp[p][r] = pk2(y.x, y.y);
      }
    }
    const unsigned yb = WS_Y + (4 * mbL + 2 * th + (wave >> 1)) * T32_D + 10 * nt * 512 + ((wave & 1) * 16 + t16) * 16;
#pragma unroll
    for (int pp = 0; pp < 3; ++pp) {
      const int pa = 2 * pp, pb = pp < 2 ? 2 * pp + 1 : 4;
      const auto r0 = __builtin_amdgcn_permlane16_swap(yp[pa][0], yp[pb][0], false, false), r1 = __builtin_amdgcn_permlane16_swap(yp[pa][1], yp[pb][1], false, false);
      const int ct = (g4 & 1) ? ct_of(pb, nt & 1) : ct_of(pa, nt & 1);
      st16_sc1((u32x4){r0[0], r1[0], r0[1], r1[1]}, rws, (pp < 2 || !(g4 & 1)) ? yb + (2 * ct + (g4 >> 1)) * 512 : 0x40000000u, 0);
    }
    const float mt = xsum32(xsum16(s2.x + s2.y)) * (1.0f / 80);
    f32x2 m2 = {0.f, 0.f};
#pragma unroll
    for (int p = 0; p < 5; ++p)
#pragma unroll
      for (int r = 0; r < 2; ++r) { const f32x2 d = xv[p][r] - mt; m2 += d * d; }
    const float m2s = xsum32(xsum16(m2.x + m2.y));
    if (lane < 16) st_u64(ws, WS_STAT + (((mbL * 2 + th) * 8 + nt) * 64 + 16 * wave + t16) * 8, ((u64)__float_as_uint(m2s) << 32) | __float_as_uint(mt));
    publish(ws, FL_Y, mbL, jL, wave, lane, tag);
  }
  STAMP(10);

  const int so4 = 0;
  auto gu_piece = [&](int st, int i) { dma16(rgu, guo[i], rot10(so4, st) * 64 * 3456, L0 + (st % 5) * L4_STAGE + (wave >> 1) * L4_U + (wave & 1) * 7168 + i * 1024); };
  for (int st = 0; st < 4; ++st)
#pragma unroll
    for (int i = 0; i < 7; ++i) gu_piece(st, i);
  const rsrc_t rwd = mkrsrc(wd, F_ * D_ * 2);
  unsigned wdg[8];
#pragma unroll
  for (int i = 0; i < 8; ++i) {
    const int pc = min(8 * wave + i, 29), kl = pc / 5;
    wdg[i] = opaque(w80_voff(pc - 5 * kl, lane, nt, [](int g, int t, int q) { return 16 * (g >> 1) + 8 * t + 4 * (g & 1) + q; }) + kl * 32 * 1280);
  }
  auto wd_piece = [&](int G, int i) { const int g = G + goL; dma16(rwd, wdg[i], (g >= 9 ? g - 9 : g) * 6 * 32 * 1280, L0 + (G % 5) * 30720 + min(8 * wave + i, 29) * 1024); };
  const Rot R5 = mkrot(rD, 6 * goL, 54, 2048);
  auto fo54 = [&](int s) { return R5.b[s < 12 ? 0 : s < 30 ? 1 : s < 42 ? 2 : 3] + s * 2048; };
  STAMP(19);
  if (wave == 0) wait_flags(ws, flagoff(FL_Y, mb, lane), true, tag);
  if (wave == 1) {
    unsigned uv[7];
    sweep<7>(ws, [&](int p) { return WS_UV + (((lane >> 5) * 108 + u0 + unit_of(p, uodd, nu)) * 32 + (lane & 31)) * 8; }, [&](int) { return true; }, tag, uv);
#pragma unroll
    for (int p = 0; p < 7; ++p) lds_st1(L0 + L4_UV + (p * 64 + lane) * 4, uv[p]);
  }
  STAMP(21);
  vmwait<0>();
  BAR();
  STAMP(11);
  u32x2 stv[4];
#pragma unroll
  for (int i = 0; i < 4; ++i) { const int T = 32 * wave + (lane & 31); stv[i] = ld8_sc1(rws, WS_STAT + (((mb * 2 + (T >> 6)) * 8 + 4 * (lane >> 5) + i) * 64 + (T & 63)) * 8, 0); }
  MEMFENCE();
  u32x4 yf[40];
  const unsigned yfo = WS_Y + (4 * mb + wave) * T32_D + 16 * lane;
  const Rot R4 = mkrot(0, 0, 40, 1024);
#pragma unroll
  for (int ks = 0; ks < P4F0; ++ks) ald16_sc1(yf[ks], rws, yfo, fo40(R4, ks));
  f32x16 acc4[7];
#pragma unroll
  for (int b = 0; b < 7; ++b) acc4[b] = (f32x16){};
  {
    const int h = lane >> 5, blk = (lane >> 4) & 1, q = (lane & 15) >> 2;
    const unsigned c0 = L0 + blk * L4_U + 2 * h * 896 + q * 64 + blk * 32 + (lane & 3) * 8;
    const unsigned c2 = L0 + blk * L4_U + 2 * h * 896 + 768 + q * 32 + (lane & 3) * 8;
    unsigned cb0[5], cb1[5], cb2[5];
#pragma unroll
    for (int sl = 0; sl < 5; ++sl) { cb0[sl] = opaque(c0 + sl * L4_STAGE); cb1[sl] = opaque((c0 ^ 32u) + sl * L4_STAGE); cb2[sl] = opaque(c2 + sl * L4_STAGE); }
    auto readw = [&](int ks, int p) {
      const int sl = (ks >> 2) % 5;
      const unsigned a = (p == 6 ? cb2[sl] : ((p & 1) ? cb1[sl] : cb0[sl]) + (p >> 1) * 256) + (ks & 3) * 4 * 896;
      return trfrag(a, a + 896); };
    s16x8 w[3][7];
#pragma unroll
    for (int p = 0; p < 7; ++p) { w[0][p] = readw(0, p); w[1][p] = readw(1, p); }
    SB();
    static_for<0, 40>([&](auto KS) {
      constexpr int ks = KS, c = ks % 3, n = (ks + 2) % 3;
      if constexpr ((ks & 3) == 2) {
        constexpr int st = (ks + 2) / 4;
        if constexpr (st <= 9) STAMPN(30 + st - 1);
        if constexpr (st >= 4 && st <= 9) vmwait<p4_wait_stage(st)>();
        BAR_RAW();
      }
      vmwait<p4_wait_frag(ks)>(yf[ks]);
      SB();
#pragma unroll
      for (int p = 0; p < 7; ++p) {
        acc4[p] = mfma32(w[c][p], yf[ks], acc4[p]);
        SB();
        if (p == 1 && ks + P4F0 < 40) ald16_sc1(yf[ks + P4F0 < 40 ? ks + P4F0 : 0], rws, yfo, fo40(R4, ks + P4F0));
        if (p == 3 && p4_pieces(ks) > 0) gu_piece((ks + 14) / 4, 2 * ((ks + 14) % 4));
        if (p == 5 && p4_pieces(ks) > 1) gu_piece((ks + 14) / 4, 2 * ((ks + 14) % 4) + 1);
        if (p == 3 && p4_touch(ks) > 0) wd_piece(p4_wd_before(ks) / 8, p4_wd_before(ks) % 8);
        if (p == 5 && p4_touch(ks) > 1) wd_piece((p4_wd_before(ks) + 1) / 8, (p4_wd_before(ks) + 1) % 8);
        if (ks + 2 < 40) w[n][p] = readw(ks + 2, p);
        SB();
      }
    });
  }
  STAMP(12);
  {
    const int h = lane >> 5;
    float ms = 0.f;
#pragma unroll
    for (int i = 0; i < 4; ++i) ms += __uint_as_float(stv[i][0]);
    const float mean = xsum32(ms) * 0.125f;
    float m2 = 0.f;
#pragma unroll
    for (int i = 0; i < 4; ++i) { const float d = __uint_as_float(stv[i][0]) - mean; m2 += __uint_as_float(stv[i][1]) + 80.0f * d * d; }
    const float rs = rden_of(xsum32(m2) * (1.0f / D_)), nm = -rs * bf16r(mean);
    const unsigned ab = WS_ACT + (4 * mb + wave) * T32_F + 16 * lane, uvb = opaque(L0 + L4_UV + 16 * h);
#pragma unroll
    for (int p = 0; p < 7; ++p) {
      if (p < 6 || nu == 7) {
        const unsigned tb = uvb + p * 256;
        u32x4 ov;
#pragma unroll
        for (int half = 0; half < 2; ++half) {
          const f32x4 ug = lds_f4(tb + 32 * half), vg = lds_f4(tb + 64 + 32 * half), uu = lds_f4(tb + 128 + 32 * half), vu = lds_f4(tb + 192 + 32 * half);
#pragma unroll
          for (int r = 0; r < 2; ++r) {
            const f32x2 ag = {acc4[p][4 * half + 2 * r], acc4[p][4 * half + 2 * r + 1]}, au = {acc4[p][8 + 4 * half + 2 * r], acc4[p][8 + 4 * half + 2 * r + 1]};
            const f32x2 g = rs * ag + (nm * (f32x2){ug[2 * r], ug[2 * r + 1]} + (f32x2){vg[2 * r], vg[2 * r + 1]});
            const f32x2 u = rs * au + (nm * (f32x2){uu[2 * r], uu[2 * r + 1]} + (f32x2){vu[2 * r], vu[2 * r + 1]});
            const f32x2 e = g * (-LOG2E);
            const f32x2 den = (f32x2){exp2_(e.x), exp2_(e.y)} + 1.0f;
            const f32x2 a = (g * u) * (f32x2){rcpf_(den.x), rcpf_(den.y)};
            ov[2 * half + r] = pk2(a.x, a.y);
          }
        }
        st16_sc1(ov, rws, ab + (u0 + unit_of(p, uodd, nu)) * 1024, 0);
      }
    }
    publish(ws, FL_ACT, mb, j, wave, lane, tag);
  }
  BAR();
  STAMP(13);

#pragma unroll
  for (int i = 4; i < 8; ++i) wd_piece(2, i);
#pragma unroll
  for (int i = 0; i < 8; ++i) wd_piece(3, i);
#pragma unroll
  for (int i = 0; i < 8; ++i) wd_piece(4, i);
  STAMP(22);
  if (wave == 0) wait_flags(ws, flagoff(FL_ACT, mbL, lane), true, tag);
  vmwait<0>();
  BAR();
  STAMP(14);
  {
    u32x4 af[54];
    const unsigned afb = WS_ACT + (4 * mbL + 2 * th + (wave >> 1)) * T32_F + g4 * 512 + ((wave & 1) * 16 + t16) * 16;
#pragma unroll
    for (int s = 0; s < P5F0; ++s) ald16_sc1(af[s], rws, afb, fo54(s));
    f32x4 acc[5];
#pragma unroll
    for (int p = 0; p < 5; ++p) acc[p] = (f32x4){};
    const B80 B5[3] = {b80(L0), b80(L0 + 61440), b80(L0 + 122880)};
    auto rd5 = [&](int s, int p) { const int sl = s % 30; return w80(B5[sl / 12], (sl % 12) * 5120, p); };
    s16x8 w[3][5];
#pragma unroll
    for (int p = 0; p < 5; ++p) { w[0][p] = rd5(0, p); w[1][p] = rd5(1, p); }
    SB();
    static_for<0, 54>([&](auto SS) {
      constexpr int s = SS;
      if constexpr (s % 6 == 4 && s + 2 < 54) {
        constexpr int g = (s + 2) / 6;
        STAMPN(40 + g - 1);
        if constexpr (g >= 5) vmwait<p5_wait_group(g)>();
        BAR_RAW();
      }
      vmwait<p5_wait_frag(s)>(af[s]);
      SB();
#pragma unroll
      for (int p = 0; p < 5; ++p) {
        acc[p] = mfma16(w[s % 3][p], af[s], acc[p]);
        SB();
        constexpr int r = (s + 20) % 6, i0 = r < 2 ? 2 * r : r + 2;
        if (p == 0 && s + P5F0 < 54) ald16_sc1(af[s + P5F0 < 54 ? s + P5F0 : 0], rws, afb, fo54(s + P5F0));
        if (p == 2 && p5_pieces(s) > 0) wd_piece((s + 20) / 6, i0);
        if (p == 4 && p5_pieces(s) > 1) wd_piece((s + 20) / 6, i0 + 1);
        if (s + 2 < 54) w[(s + 2) % 3][p] = rd5(s + 2, p);
        SB();
      }
    });
    STAMP(15);
    u32x2 op[5];
    const unsigned gbb = opaque(L0 + L3_VEC + 640 + g4 * 16);
#pragma unroll
    for (int p = 0; p < 5; ++p) {
      const f32x4 gb = lds_f4(gbb + p * 64);
#pragma unroll
      for (int r = 0; r < 2; ++r) {
        const f32x2 dn = bf16r2((f32x2){acc[p][2 * r], acc[p][2 * r + 1]});
        const f32x2 o = unpk(x1p[p][r]) + bf16r2(dn * (f32x2){gb[2 * r], gb[2 * r + 1]});
        op[p][r] = pk2(o.x, o.y);
      }
    }
    const rsrc_t ro = mkrsrc(out, S_ * D_ * 2);
#pragma unroll
    for (int pp = 0; pp < 3; ++pp) {
      const int pa = 2 * pp, pb = pp < 2 ? 2 * pp + 1 : 4;
      const auto r0 = __builtin_amdgcn_permlane16_swap(op[pa][0], op[pb][0], false, false), r1 = __builtin_amdgcn_permlane16_swap(op[pa][1], op[pb][1], false, false);
      const int ct = (g4 & 1) ? ct_of(pb, nt & 1) : ct_of(pa, nt & 1);
      __builtin_amdgcn_raw_buffer_store_b128((u32x4){r0[0], r1[0], r0[1], r1[1]}, ro, (pp < 2 || !(g4 & 1)) ? tok3 * 1280 + 160 * nt + 32 * ct + 16 * (g4 >> 1) : 0x40000000u, 0, 0);
    }
  }
  STAMP(16);
  STAMP_FLUSH();
}

extern "C" void kernel_launch(void* const* d_in, const int* in_sizes, int n_in, void* d_out, int out_size, void* d_ws, size_t ws_size,
                              hipStream_t stream) {
  (void)in_sizes; (void)n_in; (void)out_size; (void)ws_size;
  adaln_block_kernel<<<256, 256, 0, stream>>>((const char*)d_in[0], (const char*)d_in[1], (const char*)d_in[2], (const char*)d_in[4], (const char*)d_in[5],
                                              (const char*)d_in[6], (const char*)d_in[7], (const char*)d_in[8], (char*)d_out, (char*)d_ws);
}
```

```cpp
#define PF_CODE 66000
#include <hip/hip_runtime.h>
#include <stdint.h>
#include <type_traits>

#define LAS __attribute__((address_space(3)))
#define GAS __attribute__((address_space(1)))
#define DI __device__ __forceinline__
typedef float f32x2 __attribute__((ext_vector_type(2)));
typedef float f32x4 __attribute__((ext_vector_type(4)));
typedef float f32x16 __attribute__((ext_vector_type(16)));
typedef unsigned u32x2 __attribute__((ext_vector_type(2)));
typedef unsigned u32x4 __attribute__((ext_vector_type(4)));
typedef short s16x4 __attribute__((ext_vector_type(4)));
typedef short s16x8 __attribute__((ext_vector_type(8)));
typedef __bf16 bf16x2 __attribute__((ext_vector_type(2)));
typedef __bf16 bf16x8 __attribute__((ext_vector_type(8)));
typedef unsigned long long u64;
typedef __amdgpu_buffer_rsrc_t rsrc_t;

constexpr int S_ = 2048, D_ = 640, F_ = 1728;
constexpr int T32_D = 80 * 512;
constexpr int T32_F = 216 * 512;

constexpr int WS_EPOCH = 0;
constexpr int WS_TMO = 256;
constexpr int WS_FLAG = 29 << 20;
constexpr int FL_XN = 0, FL_KV = 1, FL_O = 2, FL_Y = 3, FL_ACT = 4;
constexpr int WS_GRAN = 256 * 1024;
constexpr int WS_UV = 320 * 1024;
constexpr int WS_STAT = 512 * 1024;
constexpr int WS_STAMP = 28 << 20;
constexpr int WS_XN = 1 << 20;
constexpr int WS_KV = 4 << 20;
constexpr int WS_O = 12 << 20;
constexpr int WS_Y = 16 << 20;
constexpr int WS_ACT = 20 << 20;
constexpr int WS_USED = 30 << 20;

constexpr int LDS_BYTES = 163840;
constexpr int L1_W = 0;
constexpr int L0_TB = 122880;
constexpr int L0_JW = 133120;
constexpr int L1_VS = 153600, L1_VA = 154880;
constexpr int L0_SC = 158720;
constexpr int L2_KV = 0;
constexpr int KV_IMG = 384 * 48;
constexpr int L3_W0 = 4 * KV_IMG;
constexpr int L3_NPRE = 15;
constexpr int L3_W1 = 0;
constexpr int L2_JW = 32768, L2_JV = 53248;
constexpr int L_DUMMY = 162816;
constexpr int L3_VEC = 161024;
constexpr int L4_STAGE = 30720, L4_U = 14464;
constexpr int L4_UV = 153600;
static_assert(L3_W0 + L3_NPRE * 5120 <= L3_VEC && L3_VEC + 960 <= LDS_BYTES, "P3 LDS map");
static_assert(30 * 5120 <= L3_VEC, "P5 LDS map");
static_assert(L4_UV + 1792 <= L3_VEC, "P4 LDS map");

constexpr float LOG2E = 1.4426950408889634f;
constexpr float SCORE_C = 0.22360679774997896f * 1.4426950408889634f;
constexpr float NEGBIG = -1.0e30f;
constexpr unsigned SPIN_CAP = 1u << 15;

#define SB() __builtin_amdgcn_sched_barrier(0)
#define BAR() do { asm volatile("s_waitcnt lgkmcnt(0)" ::: "memory"); __builtin_amdgcn_s_barrier(); asm volatile("" ::: "memory"); } while (0)
#define BAR_RAW() do { asm volatile("" ::: "memory"); __builtin_amdgcn_s_barrier(); asm volatile("" ::: "memory"); } while (0)
#define MEMFENCE() asm volatile("" ::: "memory")
template <int N> DI void vmwait() { asm volatile("s_waitcnt vmcnt(%0)" :: "n"(N) : "memory"); }
template <int N> DI void vmwait(u32x4& d) { asm volatile("s_waitcnt vmcnt(%1)" : "+v"(d) : "n"(N) : "memory"); }

DI unsigned pk2(float a, float b) { bf16x2 v = __builtin_convertvector((f32x2){a, b}, bf16x2); return __builtin_bit_cast(unsigned, v); }
DI float blo(unsigned v) { return __uint_as_float(v << 16); }
DI float bhi(unsigned v) { return __uint_as_float(v & 0xffff0000u); }
DI float bf16r(float a) { return blo(pk2(a, a)); }
DI f32x2 bf16r2(f32x2 a) { const unsigned p = pk2(a.x, a.y); return (f32x2){blo(p), bhi(p)}; }
DI f32x2 unpk(unsigned p) { return (f32x2){blo(p), bhi(p)}; }
DI float rcpf_(float x) { return __builtin_amdgcn_rcpf(x); }
DI float exp2_(float x) { return __builtin_amdgcn_exp2f(x); }
DI float siluf(float x) { return x * rcpf_(1.0f + exp2_(-LOG2E * x)); }
DI float rden_of(float var) { return rcpf_(bf16r(__builtin_amdgcn_sqrtf(bf16r(bf16r(var) + 1e-5f)))); }
template <int CTRL> DI float dpp_add(float x) { return x + __int_as_float(__builtin_amdgcn_update_dpp(0, __float_as_int(x), CTRL, 0xf, 0xf, true)); }
DI float sum16(float x) { x = dpp_add<0xB1>(x); x = dpp_add<0x4E>(x); x = dpp_add<0x141>(x); return dpp_add<0x140>(x); }
DI float xsum16(float x) { auto r = __builtin_amdgcn_permlane16_swap(__float_as_uint(x), __float_as_uint(x), false, false); return __uint_as_float(r[0]) + __uint_as_float(r[1]); }
DI float xsum32(float x) { auto r = __builtin_amdgcn_permlane32_swap(__float_as_uint(x), __float_as_uint(x), false, false); return __uint_as_float(r[0]) + __uint_as_float(r[1]); }
DI float xmax32(float x) { auto r = __builtin_amdgcn_permlane32_swap(__float_as_uint(x), __float_as_uint(x), false, false); return fmaxf(__uint_as_float(r[0]), __uint_as_float(r[1])); }
DI rsrc_t mkrsrc(const void* p, unsigned bytes) { return __builtin_amdgcn_make_buffer_rsrc((void*)p, (short)0, (int)bytes, 0x00020000); }
DI u32x4 ld16_sc1(rsrc_t r, unsigned voff, unsigned soff) { return __builtin_amdgcn_raw_buffer_load_b128(r, voff, soff, 16); }
DI u32x4 ld16(rsrc_t r, unsigned voff, unsigned soff) { return __builtin_amdgcn_raw_buffer_load_b128(r, voff, soff, 0); }
DI u32x2 ld8(rsrc_t r, unsigned voff, unsigned soff) { return __builtin_amdgcn_raw_buffer_load_b64(r, voff, soff, 0); }
DI u32x2 ld8_sc1(rsrc_t r, unsigned voff, unsigned soff) { return __builtin_amdgcn_raw_buffer_load_b64(r, voff, soff, 16); }
DI void st16_sc1(u32x4 v, rsrc_t r, unsigned voff, unsigned soff) { __builtin_amdgcn_raw_buffer_store_b128(v, r, voff, soff, 16); }
DI void ald16_sc1(u32x4& d, rsrc_t r, unsigned voff, unsigned soff) {
  asm volatile("buffer_load_dwordx4 %0, %1, %2, %3 offen sc1" : "=v"(d) : "v"(voff), "s"(r), "s"(soff) : "memory");
}
DI void ald16(u32x4& d, rsrc_t r, unsigned voff, unsigned soff) {
  asm volatile("buffer_load_dwordx4 %0, %1, %2, %3 offen" : "=v"(d) : "v"(voff), "s"(r), "s"(soff) : "memory");
}
DI void ald4(unsigned& d, rsrc_t r, unsigned voff, unsigned soff) {
  asm volatile("buffer_load_dword %0, %1, %2, %3 offen" : "=v"(d) : "v"(voff), "s"(r), "s"(soff) : "memory");
}
DI void ald8_sc1(u32x2& d, rsrc_t r, unsigned voff, unsigned soff) {
  asm volatile("buffer_load_dwordx2 %0, %1, %2, %3 offen sc1" : "=v"(d) : "v"(voff), "s"(r), "s"(soff) : "memory");
}
DI void touch(rsrc_t r, unsigned voff, unsigned soff, unsigned ldsaddr) {
  asm volatile("s_mov_b32 m0, %0\n\ts_nop 0\n\tbuffer_load_dword %1, %2, %3 offen lds" :: "s"(ldsaddr), "v"(voff), "s"(r), "s"(soff) : "memory");
}
DI void dma16_sc1(rsrc_t r, unsigned voff, unsigned soff, unsigned ldsaddr) {
  asm volatile("s_mov_b32 m0, %0\n\ts_nop 0\n\tbuffer_load_dwordx4 %1, %2, %3 offen sc1 lds" :: "s"(ldsaddr), "v"(voff), "s"(r), "s"(soff) : "memory");
}
DI void dma16(rsrc_t r, unsigned voff, unsigned soff, unsigned ldsaddr) {
  asm volatile("s_mov_b32 m0, %0\n\ts_nop 0\n\tbuffer_load_dwordx4 %1, %2, %3 offen lds" :: "s"(ldsaddr), "v"(voff), "s"(r), "s"(soff) : "memory");
}
DI u64 ld_u64(const char* ws, unsigned off) { return __hip_atomic_load((GAS u64*)(ws + off), __ATOMIC_RELAXED, __HIP_MEMORY_SCOPE_AGENT); }
DI void st_u64(char* ws, unsigned off, u64 v) { __hip_atomic_store((GAS u64*)(ws + off), v, __ATOMIC_RELAXED, __HIP_MEMORY_SCOPE_AGENT); }
DI u64 flagval(unsigned tag) { return ((u64)tag << 32) | (u64)(~tag); }
DI unsigned flagoff(int kind, int mb, int jw) { return WS_FLAG + ((kind * 16 + mb) * 64 + jw) * 128; }
DI void publish(char* ws, int kind, int mb, int j, int wave, int lane, unsigned tag) {
  vmwait<0>();
  if (lane == 0) st_u64(ws, flagoff(kind, mb, 4 * j + wave), flagval(tag));
}
DI bool spin_fail(unsigned& spins, char* ws, unsigned tag) {
  __builtin_amdgcn_s_sleep(1);
  if (++spins > SPIN_CAP) { __hip_atomic_store((GAS unsigned*)(ws + WS_TMO), tag, __ATOMIC_RELAXED, __HIP_MEMORY_SCOPE_AGENT); return true; }
  return (spins & 127u) == 0u && (unsigned)__builtin_amdgcn_readfirstlane(__hip_atomic_load((GAS unsigned*)(ws + WS_TMO), __ATOMIC_RELAXED, __HIP_MEMORY_SCOPE_AGENT)) == tag;
}
DI void wait_flags(char* ws, unsigned off, bool on, unsigned tag) {
  const u64 want = flagval(tag);
#pragma clang loop unroll(disable)
  for (unsigned spins = 0;;) {
    u64 v = want;
    if (on) v = ld_u64(ws, off);
    if (__all(v == want)) break;
    if (spin_fail(spins, ws, tag)) break;
  }
  MEMFENCE();
}
template <int N, class OFF, class ON> DI void sweep(char* ws, OFF off, ON on, unsigned tag, unsigned (&val)[N]) {
#pragma clang loop unroll(disable)
  for (unsigned spins = 0;;) {
    bool ok = true;
#pragma unroll
    for (int k = 0; k < N; ++k) { u64 g = (u64)tag << 32; if (on(k)) g = ld_u64(ws, off(k)); val[k] = (unsigned)g; ok &= (unsigned)(g >> 32) == tag; }
    if (__all(ok)) break;
    if (spin_fail(spins, ws, tag)) break;
  }
  MEMFENCE();
}
DI s16x8 trfrag(unsigned a0, unsigned a1) {
  const s16x4 lo = __builtin_amdgcn_ds_read_tr16_b64_v4i16((LAS s16x4*)(size_t)a0);
  const s16x4 hi = __builtin_amdgcn_ds_read_tr16_b64_v4i16((LAS s16x4*)(size_t)a1);
  return __builtin_shufflevector(lo, hi, 0, 1, 2, 3, 4, 5, 6, 7);
}
DI f32x4 lds_f4(unsigned a) { return *(LAS f32x4*)(size_t)a; }
DI u32x4 lds_u4(unsigned a) { return *(LAS u32x4*)(size_t)a; }
DI void lds_st4(unsigned a, u32x4 v) { *(LAS u32x4*)(size_t)a = v; }
DI void lds_st1(unsigned a, unsigned v) { *(LAS unsigned*)(size_t)a = v; }
DI void lds_st2f(unsigned a, f32x2 v) { *(LAS f32x2*)(size_t)a = v; }
DI unsigned opaque(unsigned v) { asm volatile("" : "+v"(v)); return v; }
DI unsigned sopaque(unsigned v) { asm volatile("" : "+s"(v)); return v; }
struct Rot { unsigned b[4]; };
DI Rot mkrot(int r, int shift, int N, int step) {
  const unsigned a = shift * step, w = N * step;
  return Rot{{sopaque(a), sopaque(a - (r >= 3 ? w : 0)), sopaque(a - (r >= 2 ? w : 0)), sopaque(a - (r >= 1 ? w : 0))}};
}
DI f32x16 mfma32(s16x8 a, u32x4 b, f32x16 c) { return __builtin_amdgcn_mfma_f32_32x32x16_bf16(__builtin_bit_cast(bf16x8, a), __builtin_bit_cast(bf16x8, b), c, 0, 0, 0); }
DI f32x4 mfma16(s16x8 a, u32x4 b, f32x4 c) { return __builtin_amdgcn_mfma_f32_16x16x32_bf16(__builtin_bit_cast(bf16x8, a), __builtin_bit_cast(bf16x8, b), c, 0, 0, 0); }
DI void smfma_c(f32x16& d, u32x4 a, u32x4 b, const f32x16& c) { asm("v_mfma_f32_32x32x16_bf16 %0, %1, %2, %3" : "=&v"(d) : "v"(a), "v"(b), "v"(c)); }
DI void smfma_0(f32x16& d, u32x4 a, u32x4 b) { asm("v_mfma_f32_32x32x16_bf16 %0, %1, %2, 0" : "=&v"(d) : "v"(a), "v"(b)); }
DI void smfma_a(f32x16& d, u32x4 a, u32x4 b) { asm("v_mfma_f32_32x32x16_bf16 %0, %1, %2, %0" : "+v"(d) : "v"(a), "v"(b)); }
DI void sfence(f32x16& d) { asm volatile("s_nop 11" : "+v"(d)); }
DI float fmix_lo(unsigned x, unsigned s, unsigned a) { float r; asm("v_fma_mix_f32 %0, %1, %2, %3 op_sel_hi:[1,1,1]" : "=v"(r) : "v"(x), "v"(s), "v"(a)); return r; }
DI float fmix_hi(unsigned x, unsigned s, unsigned a) { float r; asm("v_fma_mix_f32 %0, %1, %2, %3 op_sel:[1,1,1] op_sel_hi:[1,1,1]" : "=v"(r) : "v"(x), "v"(s), "v"(a)); return r; }
DI unsigned pkh(float a, float b) { return __builtin_bit_cast(unsigned, __builtin_amdgcn_cvt_pkrtz(a, b)); }

#define STAMP(i) do { } while (0)
#define STAMPN(i) do { } while (0)
#define STAMP_INIT() do { } while (0)
#define STAMP_FLUSH() do { } while (0)

template <int NS> DI void gemv16_issue(rsrc_t rw, unsigned rowstride, unsigned srcbyte, unsigned ldsw, int lane) {
  const unsigned vo = (8 * ((lane >> 3) & 3) + 4 * (lane >> 5) + ((lane >> 1) & 3)) * rowstride + 16 * (lane & 1);
#pragma unroll 5
  for (int s = 0; s < NS; ++s) dma16(rw, vo, s * 32 * rowstride + srcbyte, ldsw + s * 1024);
}
template <int NS> DI f32x4 gemv16_run(unsigned ldsw, unsigned ldsv, int vmask, int lane) {
  f32x4 acc0 = {}, acc1 = {};
  const unsigned wa = opaque(ldsw + 8 * lane), va = opaque(ldsv + (lane & vmask) * 1280 + (lane >> 4) * 16);
  s16x8 w[3]; u32x4 v[3];
#pragma unroll
  for (int s = 0; s < 2; ++s) { w[s] = trfrag(wa + s * 1024, wa + s * 1024 + 512); v[s] = lds_u4(va + s * 64); }
  SB();
#pragma unroll
  for (int s = 0; s < NS; ++s) {
    if (s + 2 < NS) { w[(s + 2) % 3] = trfrag(wa + (s + 2) * 1024, wa + (s + 2) * 1024 + 512); v[(s + 2) % 3] = lds_u4(va + (s + 2) * 64); }
    SB();
    if (s & 1) acc1 = mfma16(w[s % 3], v[s % 3], acc1); else acc0 = mfma16(w[s % 3], v[s % 3], acc0);
    SB();
  }
  return acc0 + acc1;
}

DI int ct_of(int p, int odd) { return odd ? (p < 4 ? p + 1 : 0) : p; }
DI int unit_of(int p, int odd, int nu) { const int u = odd ? (p < 6 ? p + 1 : 0) : p; return u < nu ? u : nu - 1; }

template <class RM> DI unsigned w80_voff(int piece, int lane, int nt, RM rowmap) {
  const int odd = nt & 1;
  int g, t, q, p, hf;
  if (piece < 4) { g = piece; t = lane >> 5; const int su = (lane >> 4) & 1; q = (lane >> 2) & 3; const int c = (lane & 3) ^ (2 * (piece & 1)); p = 2 * su + (c >> 1); hf = c & 1; }
  else { t = lane >> 5; g = (lane >> 3) & 3; q = (lane >> 1) & 3; p = 4; hf = lane & 1; }
  return rowmap(g, t, q) * 1280 + 160 * nt + 32 * ct_of(p, odd) + 16 * hf;
}

template <class OPS> constexpr int vm_sum(OPS ops, int lo, int hi) { int n = 0; for (int k = lo; k <= hi; ++k) if (k >= 0) n += ops(k); return n; }
constexpr int P1F0 = 10;
constexpr int p1_pieces(int k) { return k < 24 ? 1 : 0; }
constexpr int p1_ops(int k) { return (k < 40 - P1F0 ? 1 : 0) + p1_pieces(k); }
constexpr int p1_wait_frag(int f) {
  return f < P1F0 ? 8 + vm_sum(p1_ops, 0, f - 3) : p1_pieces(f - P1F0) + vm_sum(p1_ops, f - P1F0 + 1, f - 3);
}
constexpr int p1_wait_stage(int st) { return st == 2 ? 4 + vm_sum(p1_ops, 0, 5) : st == 3 ? vm_sum(p1_ops, 0, 9) : vm_sum(p1_ops, 4 * st - 12, 4 * st - 3); }
constexpr int P4F0 = 8;
constexpr int p4_pieces(int k) { return k >= 2 && k <= 25 ? ((k + 14) % 4 == 3 ? 1 : 2) : 0; }
constexpr int p4_wd_before(int k) { return k <= 26 ? 0 : k == 27 ? 2 : k == 28 ? 3 : k == 29 ? 5 : k == 30 ? 6 : k == 31 ? 8 : k == 32 ? 9 : k == 33 ? 11 : k == 34 ? 12 : k == 35 ? 14 : k <= 40 ? 16 + (k - 36) : 20; }
constexpr int p4_touch(int k) { return p4_wd_before(k + 1) - p4_wd_before(k); }
constexpr int p4_ops(int k) { return (k < 40 - P4F0 ? 1 : 0) + p4_pieces(k) + p4_touch(k); }
constexpr int p4_wait_frag(int ks) { return ks < P4F0 ? (P4F0 - 1 - ks) + vm_sum(p4_ops, 0, ks - 1) : p4_pieces(ks - P4F0) + p4_touch(ks - P4F0) + vm_sum(p4_ops, ks - P4F0 + 1, ks - 1); }
constexpr int p4_wait_stage(int st) { return vm_sum(p4_ops, 4 * st - 10, 4 * st - 3); }
constexpr int P5F0 = 12;
constexpr int p5_pieces(int k) { return k >= 10 && k <= 33 ? ((k + 20) % 6 < 2 ? 2 : 1) : 0; }
constexpr int p5_ops(int k) { return (k < 54 - P5F0 ? 1 : 0) + p5_pieces(k); }
constexpr int p5_wait_frag(int s) { return s < P5F0 ? (P5F0 - 1 - s) + vm_sum(p5_ops, 0, s - 1) : p5_pieces(s - P5F0) + vm_sum(p5_ops, s - P5F0 + 1, s - 1); }
constexpr int p5_wait_group(int g) { return vm_sum(p5_ops, 6 * g - 14, 6 * g - 3); }
constexpr int P3F0 = 6;
constexpr int p3_wait_frag(int s) { return s < P3F0 ? (P3F0 - 1 - s) + s : (s < 20 - P3F0 ? P3F0 - 1 : 19 - s); }

template <int I, int N, class Fn> DI void static_for(Fn&& f) { if constexpr (I < N) { f(std::integral_constant<int, I>{}); static_for<I + 1, N>(f); } }

__global__ __launch_bounds__(256, 1) void adaln_block_kernel(const char* __restrict__ x, const char* __restrict__ wqkv, const char* __restrict__ wo,
                                                             const char* __restrict__ wg, const char* __restrict__ wu, const char* __restrict__ wd,
                                                             const char* __restrict__ cvec, const char* __restrict__ wmod, char* out, char* ws) {
  __shared__ __attribute__((aligned(16))) char smem[LDS_BYTES];
  const unsigned L0 = (unsigned)(size_t)(LAS char*)smem;
  const int tid = threadIdx.x, lane = tid & 63;
  const int wave = __builtin_amdgcn_readfirstlane(tid >> 6);
  const int bid = blockIdx.x, xcd = bid & 7, ii = bid >> 3;
  const int mbL = 2 * xcd + (ii >> 4), jL = ii & 15;
  const int mb = 4 * (xcd >> 1) + (ii >> 3), j = 8 * (xcd & 1) + (ii & 7);
  const int rW = mb & 3, rL = 0, rD = 2 * (mbL & 1) + (jL & 1);
  const int soW = (5 * rW) >> 1;
  const int goL = rD == 3 ? 7 : 2 * rD;
  const unsigned tag = *(const GAS unsigned*)(ws + WS_EPOCH) + 1u;
  const rsrc_t rx = mkrsrc(x, S_ * D_ * 2), rws = mkrsrc(ws, WS_USED);
  STAMP_INIT();
  STAMP(0);

  const rsrc_t rwq = mkrsrc(wqkv, D_ * 3 * D_ * 2);
  unsigned wqo[4];
  auto rot10 = [&](int so, int st) { const int g = st + so; return g >= 10 ? g - 10 : g; };
  const int so1 = (int)sopaque(soW);
  auto wq_piece = [&](int st, int i) { dma16(rwq, wqo[i], rot10(so1, st) * 64 * 3840, L0 + L1_W + (st * 15 + min(4 * wave + i, 14)) * 1024); };
  auto fo40 = [&](const Rot& R, int ks) { return R.b[ks < 12 ? 0 : ks < 20 ? 1 : ks < 32 ? 2 : 3] + ks * 1024; };
  auto wq_issue = [&](int st) {
#pragma unroll
    for (int i = 0; i < 4; ++i) wq_piece(st, i);
  };
  {
    const int c32 = lane & 31, tok = 128 * mbL + 8 * jL + 2 * wave + (lane >> 5);
    u32x4 xv[3], c4 = {}; unsigned c1 = 0;
    ald16(xv[0], rx, tok * 1280 + c32 * 16, 0);
    ald16(xv[1], rx, tok * 1280 + c32 * 16 + 512, 0);
    ald16(xv[2], rx, c32 < 16 ? tok * 1280 + c32 * 16 + 1024 : 0x40000000u, 0);
    if (wave == 3) {
      const u64 pc = __builtin_amdgcn_s_getpc();
      touch(mkrsrc((const void*)pc, PF_CODE), (ii * 33 + min(lane, 32)) * 64, 0, L0 + L_DUMMY);
    }
    const int munit = ii < 10 ? 10 * xcd + ii : 80 + 20 * xcd + (ii - 10);
    const bool mjob = wave == 0 && ii < 30;
    if (mjob) {
      const rsrc_t rc = mkrsrc(cvec, D_ * 2);
      ald16(c4, rc, 20 * lane, 0); ald4(c1, rc, 20 * lane + 16, 0);
    }
#pragma unroll
    for (int i = 0; i < 4; ++i) {
      const int idx = 64 * min(4 * wave + i, 14) + lane, rq = idx / 60, rem = idx - 60 * rq;
      int q, ch;
      if (rem < 48) { q = (rem >> 2) & 3; ch = 4 * (rem >> 4) + (rem & 3); } else { const int r2 = rem - 48; q = r2 / 3; ch = 12 + r2 - 3 * q; }
      const int seg = ch / 5;
      wqo[i] = (4 * rq + q) * 3840 + seg * 1280 + 80 * j + (ch - 5 * seg) * 16;
    }
    if (mjob) {
      gemv16_issue<20>(mkrsrc(wmod, D_ * 6 * D_ * 2), 7680, 32 * munit, L0 + L0_JW, lane);
      vmwait<20>();
    } else {
      vmwait<0>();
    }
    asm volatile("" : "+v"(xv[0]), "+v"(xv[1]), "+v"(xv[2]), "+v"(c4), "+v"(c1));
    STAMPN(48);
    f32x2 s2 = {0.f, 0.f};
#pragma unroll
    for (int i = 0; i < 3; ++i)
#pragma unroll
      for (int e = 0; e < 4; ++e) s2 += unpk(xv[i][e]);
    const float mean = xsum16(sum16(s2.x + s2.y)) * (1.0f / D_);
    f32x2 q2 = {0.f, 0.f}, q3 = {0.f, 0.f};
#pragma unroll
    for (int e = 0; e < 4; ++e) {
      const f32x2 d0 = unpk(xv[0][e]) - mean, d1 = unpk(xv[1][e]) - mean, d2 = unpk(xv[2][e]) - mean;
      q2 += d0 * d0; q2 += d1 * d1; q3 += d2 * d2;
    }
    const float var = xsum16(sum16(q2.x + q2.y + (c32 < 16 ? q3.x + q3.y : 0.f))) * (1.0f / D_);
    const float mq = bf16r(mean), rden = rden_of(var);
#pragma unroll
    for (int i = 0; i < 3; ++i) {
      u32x4 o;
#pragma unroll
      for (int e = 0; e < 4; ++e) { const f32x2 d = bf16r2(bf16r2(unpk(xv[i][e]) - mq) * rden); o[e] = pkh(d.x, d.y); }
      const int slot = c32 + 32 * i;
      if (i < 2 || c32 < 16) lds_st4(L0 + L0_TB + slot * 128 + (((2 * wave + (lane >> 5)) ^ (slot & 7)) * 16), o);
    }
    STAMPN(49);
    BAR();
    STAMPN(50);
#pragma unroll
    for (int u = 0; u < 3; ++u) {
      const int i = 256 * u + tid, slot = i >> 3, tk = (i & 7) ^ (slot & 7);
      const unsigned voff = (u < 2 || tid < 128) ? WS_XN + (4 * mbL + (jL >> 2)) * T32_D + slot * 512 + (8 * (jL & 3) + tk) * 16 : 0x40000000u;
      st16_sc1(lds_u4(L0 + L0_TB + min(i, 639) * 16), rws, voff, 0);
    }
    STAMPN(51);
    wq_issue(0);
    vmwait<4>();
    if (lane == 0) st_u64(ws, flagoff(FL_XN, mbL, 4 * jL + wave), flagval(tag));
    STAMP(1);
    if (mjob) {
      const unsigned cc[5] = {c4[0], c4[1], c4[2], c4[3], c1};
#pragma unroll
      for (int r = 0; r < 5; ++r) lds_st1(L0 + L0_SC + 20 * lane + 4 * r, pk2(siluf(blo(cc[r])), siluf(bhi(cc[r]))));
      asm volatile("s_waitcnt lgkmcnt(0)" ::: "memory");
      const f32x4 d = gemv16_run<20>(L0 + L0_JW, L0 + L0_SC, 0, lane);
      const unsigned pv = (lane & 1) ? pk2(d[2], d[3]) : pk2(d[0], d[1]);
      if ((lane & 15) < 2) st_u64(ws, WS_GRAN + (8 * munit + 2 * (lane >> 4) + (lane & 1)) * 8, ((u64)tag << 32) | pv);
    }
    STAMPN(52);
    wq_issue(1);
    STAMPN(53);
  }
  if (wave == 0) wait_flags(ws, flagoff(FL_XN, mb, lane), true, tag);
  BAR_RAW();
  STAMP(17);
  u32x4 xf[40];
  const unsigned xfo = WS_XN + (4 * mb + wave) * T32_D + 16 * lane;
  const Rot R1 = mkrot(rW, 4 * soW, 40, 1024);
  {
    const int g0 = 160 * wave + lane;
    u32x2 ga[3];
#pragma unroll
    for (int ks = 0; ks < P1F0; ++ks) ald16_sc1(xf[ks], rws, xfo, fo40(R1, ks));
#pragma unroll
    for (int k = 0; k < 3; ++k) ald8_sc1(ga[k], rws, WS_GRAN + (k < 2 || lane < 32 ? g0 + 64 * k : g0) * 8, 0);
    wq_issue(2); wq_issue(3);
    vmwait<8>();
    asm volatile("" : "+v"(ga[0]), "+v"(ga[1]), "+v"(ga[2]));
    unsigned gv[3] = {ga[0][0], ga[1][0], ga[2][0]};
    if (!__all(ga[0][1] == tag && ga[1][1] == tag && ga[2][1] == tag))
      sweep<3>(ws, [&](int k) { return WS_GRAN + (g0 + 64 * k) * 8; }, [&](int k) { return k < 2 || lane < 32; }, tag, gv);
    STAMP(18);
#pragma unroll
    for (int k = 0; k < 3; ++k) if (k < 2 || lane < 32) {
      const int g = g0 + 64 * k;
      f32x2 v = unpk(gv[k]);
      if (g >= 320) v = bf16r2(v + 1.0f);
      const int gp = g >= 320 ? g - 320 : g;
      int kp = (gp >> 3) - 4 * soW; kp += kp < 0 ? 40 : 0;
      lds_st1(L0 + (g >= 320 ? L1_VS : L1_VA) + kp * 32 + (gp & 7) * 4, pkh(v.x, v.y));
    }
  }
  BAR();
  STAMP(2);

  f32x16 acc1[4];
#pragma unroll
  for (int b = 0; b < 4; ++b) acc1[b] = (f32x16){};
  {
    const int h = lane >> 5;
    const unsigned wb = opaque(L0 + L1_W + 2 * h * 960 + ((lane & 15) >> 2) * 64 + ((lane >> 4) & 1) * 32 + (lane & 3) * 8);
    const unsigned wb3 = opaque(L0 + L1_W + 2 * h * 960 + 768 + ((lane & 15) >> 2) * 48 + ((lane >> 4) & 1) * 32 + (lane & 3) * 8);
    const unsigned wbh = opaque(wb + 61440), wb3h = opaque(wb3 + 61440), wbg = opaque(wb + 122880), wb3g = opaque(wb3 + 122880);
    const unsigned vb = opaque(L0 + L1_VS + h * 16);
    s16x8 w[3][4]; u32x4 hf[2], vs[2], va[2]; float ft[2][8];
    auto readw = [&](int ks, int b) {
      const int kl = ks % 16; const unsigned base = ks < 16 ? (b < 3 ? wb : wb3) : ks < 32 ? (b < 3 ? wbh : wb3h) : (b < 3 ? wbg : wb3g);
      const unsigned a = base + (b < 3 ? b * 256 : 0) + kl * 3840; return trfrag(a, a + 960); };
    auto reads = [&](int ks) { vs[ks & 1] = lds_u4(vb + ks * 32); };
    auto reada = [&](int ks) { va[ks & 1] = lds_u4(vb + 1280 + ks * 32); };
    auto fstage = [&](int ks, int e) {
      ft[ks & 1][2 * e] = fmix_lo(xf[ks][e], vs[ks & 1][e], va[ks & 1][e]);
      ft[ks & 1][2 * e + 1] = fmix_hi(xf[ks][e], vs[ks & 1][e], va[ks & 1][e]); };
    auto cstage = [&](int ks, int e) { hf[ks & 1][e] = pk2(ft[ks & 1][2 * e], ft[ks & 1][2 * e + 1]); };
#pragma unroll
    for (int b = 0; b < 4; ++b) { w[0][b] = readw(0, b); w[1][b] = readw(1, b); }
    reads(0); reada(0); reads(1); reada(1);
    vmwait<p1_wait_frag(0)>(xf[0]);
#pragma unroll
    for (int e = 0; e < 4; ++e) fstage(0, e);
#pragma unroll
    for (int e = 0; e < 4; ++e) cstage(0, e);
    vmwait<p1_wait_frag(1)>(xf[1]);
#pragma unroll
    for (int e = 0; e < 4; ++e) fstage(1, e);
    reads(2); reada(2);
    SB();
    static_for<0, 40>([&](auto KS) {
      constexpr int ks = KS, c = ks & 1, wc = ks % 3, wn = (ks + 2) % 3;
      constexpr bool needf = ks + 2 < 40, needs = (ks & 3) == 2 && (ks + 2) / 4 >= 2 && ks + 2 < 40;
      constexpr int nf = needf ? p1_wait_frag(ks + 2) : 63, ns = needs ? p1_wait_stage((ks + 2) / 4) : 63;
      if constexpr (needs) { vmwait<(nf < ns ? nf : ns)>(); BAR_RAW(); if constexpr (ks >= 14) STAMPN(24 + (ks - 14) / 4); }
      if constexpr (needf) vmwait<nf>(xf[ks + 2]);
      SB();
      acc1[0] = mfma32(w[wc][0], hf[c], acc1[0]); SB();
      if constexpr (ks + 2 < 40) w[wn][0] = readw(ks + 2, 0);
      if constexpr (ks + 1 < 40) { cstage(ks + 1, 0); cstage(ks + 1, 1); }
      if constexpr (ks + 3 < 40) reads(ks + 3);
      SB();
      acc1[1] = mfma32(w[wc][1], hf[c], acc1[1]); SB();
      if constexpr (ks + P1F0 < 40) ald16_sc1(xf[ks + P1F0], rws, xfo, fo40(R1, ks + P1F0));
      if constexpr (ks + 2 < 40) w[wn][1] = readw(ks + 2, 1);
      if constexpr (ks + 1 < 40) { cstage(ks + 1, 2); cstage(ks + 1, 3); }
      if constexpr (ks + 3 < 40) reada(ks + 3);
      SB();
      acc1[2] = mfma32(w[wc][2], hf[c], acc1[2]); SB();
      if constexpr (ks + 2 < 40) w[wn][2] = readw(ks + 2, 2);
      if constexpr (ks + 2 < 40) { fstage(ks + 2, 0); fstage(ks + 2, 1); }
      SB();
      acc1[3] = mfma32(w[wc][3], hf[c], acc1[3]); SB();
      if constexpr (ks < 24) wq_piece(ks / 4 + 4, ks & 3);
      if constexpr (ks + 2 < 40) w[wn][3] = readw(ks + 2, 3);
      if constexpr (ks + 2 < 40) { fstage(ks + 2, 2); fstage(ks + 2, 3); }
      SB();
    });
  }
  STAMP(3);

  u32x4 qa0, qa1, qb0, qb1;
  {
    const int h = lane >> 5, t = lane & 31;
    u32x2 P[15];
#pragma unroll
    for (int i = 0; i < 15; ++i) { const f32x16& a = acc1[i >> 2]; const int r = 4 * (i & 3); P[i] = (u32x2){pk2(a[r], a[r + 1]), pk2(a[r + 2], a[r + 3])}; }
    const u32x2 one = {0x00003f80u, 0u}, zero = {0u, 0u};
    auto cat = [](u32x2 a, u32x2 b) { return (u32x4){a[0], a[1], b[0], b[1]}; };
    auto sel = [&](u32x2 a1, u32x2 a0) { return (u32x2){h ? a1[0] : a0[0], h ? a1[1] : a0[1]}; };
    const unsigned row = L0 + L2_KV + (256 + 32 * wave + t) * 48;
    lds_st4(row + 16 * h, cat(P[5], P[6]));
    lds_st4(row + KV_IMG + 16 * (h ^ 1), cat(sel(P[7], P[8]), sel(P[8], P[9])));
    lds_st4(row + h * KV_IMG + 32, cat(sel(P[9], P[7]), zero));
    lds_st4(row + 2 * KV_IMG + 16 * h, cat(P[10], P[11]));
    lds_st4(row + 3 * KV_IMG + 16 * (h ^ 1), cat(sel(P[12], P[13]), sel(P[13], P[14])));
    lds_st4(row + (2 + h) * KV_IMG + 32, cat(sel(P[14], P[12]), one));
    asm volatile("s_waitcnt lgkmcnt(0)" ::: "memory");
#pragma unroll
    for (int u = 0; u < 6; ++u) {
      const int i = 64 * u + lane, a = i / 96, o = (i - 96 * a) * 16;
      const u32x4 v = lds_u4(L0 + L2_KV + a * KV_IMG + (256 + 32 * wave) * 48 + o);
      st16_sc1(v, rws, WS_KV + ((mb * 16 + j) * 4 + a) * 6144 + wave * 1536 + o, 0);
    }
    qa0 = cat(P[0], P[1]);
    qa1 = cat(sel(zero, P[2]), sel(zero, one));
    qb0 = cat(sel(P[2], P[3]), sel(P[3], P[4]));
    qb1 = cat(sel(P[4], zero), sel(one, zero));
    publish(ws, FL_KV, mb, j, wave, lane, tag);
  }
  STAMP(4);
  const int th = jL & 1, nt = jL >> 1;
  const rsrc_t rwo = mkrsrc(wo, D_ * D_ * 2);
  unsigned woo[7], wol[7];
#pragma unroll
  for (int i = 0; i < 7; ++i) {
    const int pi = min(wave + 4 * i, 24), kc = pi / 5, pc = pi - 5 * kc;
    auto rowmap = [&](int g, int t, int q) { const int k = 32 * kc + 8 * g + 4 * t + q, hp = k / 40, r = k - 40 * hp; return 40 * hp + (r < 20 ? r : r < 24 ? r + 16 : r - 4); };
    woo[i] = w80_voff(pc, lane, nt, rowmap);
    wol[i] = pi * 1024;
  }
  auto wo_piece = [&](int gam, int i) { dma16(rwo, woo[i], ((gam + rL) & 3) * 160 * 1280, L0 + (gam < 3 ? L3_W0 + gam * 25600 : L3_W1) + wol[i]); };
  {
    const int blk = wave >> 1, src = mb - 2 + blk;
    if (src >= 0) {
      wait_flags(ws, flagoff(FL_KV, src, 4 * j + (lane & 3)), lane < 4, tag);
      STAMP(5);
#pragma unroll
      for (int i = 0; i < 12; ++i) {
        const int a = 2 * (wave & 1) + i / 6, sub = i % 6;
        dma16_sc1(rws, 16 * lane, WS_KV + ((src * 16 + j) * 4 + a) * 6144 + sub * 1024, L0 + L2_KV + a * KV_IMG + blk * 6144 + sub * 1024);
      }
    } else {
      STAMP(5);
#pragma unroll
      for (int i = 0; i < 12; ++i) {
        const int a = 2 * (wave & 1) + i / 6, o = (i % 6) * 1024 + 16 * lane;
        lds_st4(L0 + L2_KV + a * KV_IMG + blk * 6144 + o, (u32x4){0u, 0u, ((o % 48) == 32 && a < 2) ? 0x0000f149u : 0u, 0u});
      }
    }
  }
  vmwait<0>();
  BAR();
  STAMP(6);

  {
    const rsrc_t rgt = mkrsrc(wave < 2 ? wg : wu, D_ * F_ * 2);
    const unsigned tgo = (20 * ii + 10 * (wave & 1) + (lane >> 5)) * 3456 + (xcd & 1) * 1792 + 64 * min(lane & 31, (xcd & 1) ? 25 : 27);
    const int h = lane >> 5, t = lane & 31;
    f32x16 mfirst, mdiag;
#pragma unroll
    for (int r = 0; r < 16; ++r) { const int key = (r & 3) + 8 * (r >> 2) + 4 * h; mfirst[r] = key > t ? 0.f : NEGBIG; mdiag[r] = key > t ? NEGBIG : 0.f; }
    const unsigned krow = L0 + L2_KV + (32 * wave + t) * 48;
    const unsigned ka0 = opaque(krow + 16 * h), ka1 = opaque(krow + 32), kb0 = opaque(krow + KV_IMG + 16 * (h ^ 1)), kb1 = opaque(krow + KV_IMG + 32);
    const unsigned vrow = L0 + L2_KV + 2 * KV_IMG + (32 * wave + 4 * h + ((lane & 15) >> 2)) * 48 + ((lane >> 4) & 1) * 32 + (lane & 3) * 8;
    const unsigned vra = opaque(vrow), vrb = opaque(vrow + KV_IMG);
    f32x16 sa[9], sb[9];
    auto qk = [&](f32x16& s, int kb, u32x4 a0, u32x4 a1, u32x4 q0, u32x4 q1) {
      if (kb == 0) smfma_c(s, a0, q0, mfirst); else if (kb == 8) smfma_c(s, a0, q0, mdiag); else smfma_0(s, a0, q0);
      smfma_a(s, a1, q1); };
    float e[8];
    auto pfma = [&](const f32x16& s, int half, float mc) {
#pragma unroll
      for (int i = 0; i < 8; ++i) e[i] = fmaf(s[8 * half + i], SCORE_C, mc); };
    auto pexp = [&]() {
#pragma unroll
      for (int i = 0; i < 8; ++i) e[i] = exp2_(e[i]); };
    auto pcvt = [&]() { return (u32x4){pk2(e[0], e[1]), pk2(e[2], e[3]), pk2(e[4], e[5]), pk2(e[6], e[7])}; };
    auto qk0 = [&](f32x16& s, int kb, u32x4 a0, u32x4 q0) { if (kb == 0) smfma_c(s, a0, q0, mfirst); else if (kb == 8) smfma_c(s, a0, q0, mdiag); else smfma_0(s, a0, q0); };
    auto maxhalf = [&](float (&m)[4], const f32x16& s, int half) {
#pragma unroll
      for (int r = 8 * half; r < 8 * half + 8; r += 2) m[(r >> 1) & 3] = fmaxf(fmaxf(m[(r >> 1) & 3], s[r]), s[r + 1]); };
    auto vfrag = [&](unsigned base, int kb, int half) { const unsigned a = base + (32 * kb + 16 * half) * 48; return trfrag(a, a + 8 * 48); };
    auto maxblk = [&](float (&m)[4], const f32x16& s) {
#pragma unroll
      for (int r = 0; r < 16; r += 2) m[(r >> 1) & 3] = fmaxf(fmaxf(m[(r >> 1) & 3], s[r]), s[r + 1]); };
    u32x4 k0 = lds_u4(ka0), k1 = lds_u4(ka1);
    float mx[4] = {NEGBIG, NEGBIG, NEGBIG, NEGBIG};
#pragma unroll
    for (int kb = 0; kb < 9; ++kb) {
      u32x4 n0, n1;
      SB();
      qk0(sa[kb], kb, k0, qa0);
      SB();
      n0 = kb < 8 ? lds_u4(ka0 + (kb + 1) * 1536) : lds_u4(kb0);
      if (kb == 2) asm volatile("s_nop 3" : "+v"(sa[0]));
      if (kb >= 2) maxhalf(mx, sa[kb - 2], 0);
      SB();
      smfma_a(sa[kb], k1, qa1);
      SB();
      n1 = kb < 8 ? lds_u4(ka1 + (kb + 1) * 1536) : lds_u4(kb1);
      if (kb >= 2) { maxhalf(mx, sa[kb - 2], 1); wo_piece(2, kb - 2); }
      k0 = n0; k1 = n1;
    }
    SB();
    asm volatile("s_nop 7" : "+v"(sa[7]));
    maxblk(mx, sa[7]);
    sfence(sa[8]);
    maxblk(mx, sa[8]);
    const float mca = -SCORE_C * xmax32(fmaxf(fmaxf(mx[0], mx[1]), fmaxf(mx[2], mx[3])));
    f32x16 oa = {}, ob = {};
    s16x8 v0 = vfrag(vra, 0, 0), v1 = vfrag(vra, 0, 1);
#pragma unroll
    for (int r = 0; r < 4; ++r) mx[r] = NEGBIG;
    SB();
#pragma unroll
    for (int kb = 0; kb < 9; ++kb) {
      qk0(sb[kb], kb, k0, qb0);
      SB();
      if (kb < 8) k0 = lds_u4(kb0 + (kb + 1) * 1536);
      pfma(sa[kb], 0, mca); pexp();
      SB();
      smfma_a(sb[kb], k1, qb1);
      SB();
      if (kb < 8) k1 = lds_u4(kb1 + (kb + 1) * 1536);
      const u32x4 p0 = pcvt();
      pfma(sa[kb], 1, mca);
      SB();
      oa = mfma32(v0, p0, oa);
      SB();
      v0 = kb < 8 ? vfrag(vra, kb + 1, 0) : vfrag(vrb, 0, 0);
      pexp();
      const u32x4 p1 = pcvt();
      if (kb >= 1) maxhalf(mx, sb[kb - 1], 0);
      SB();
      oa = mfma32(v1, p1, oa);
      SB();
      v1 = kb < 8 ? vfrag(vra, kb + 1, 1) : vfrag(vrb, 0, 1);
      if (kb >= 1) maxhalf(mx, sb[kb - 1], 1);
      if (kb < 7) wo_piece(0, kb);
      if (kb < 5) touch(rgt, tgo, kb * 2 * 3456, L0 + L_DUMMY);
      SB();
    }
    sfence(sb[8]);
    maxblk(mx, sb[8]);
    const float mcb = -SCORE_C * xmax32(fmaxf(fmaxf(mx[0], mx[1]), fmaxf(mx[2], mx[3])));
    SB();
    pfma(sb[0], 0, mcb); pexp();
    SB();
#pragma unroll
    for (int kb = 0; kb < 9; ++kb) {
      const u32x4 p0 = pcvt();
      pfma(sb[kb], 1, mcb);
      SB();
      ob = mfma32(v0, p0, ob);
      SB();
      if (kb < 8) v0 = vfrag(vrb, kb + 1, 0);
      pexp();
      const u32x4 p1 = pcvt();
      if (kb < 8) pfma(sb[kb + 1], 0, mcb);
      SB();
      ob = mfma32(v1, p1, ob);
      SB();
      if (kb < 8) { v1 = vfrag(vrb, kb + 1, 1); pexp(); }
      if (kb < 7) wo_piece(1, kb);
      SB();
    }
    const auto la = __builtin_amdgcn_permlane32_swap(__float_as_uint(oa[8]), __float_as_uint(oa[8]), false, false);
    const auto lb = __builtin_amdgcn_permlane32_swap(__float_as_uint(ob[8]), __float_as_uint(ob[8]), false, false);
    const float ia = rcpf_(__uint_as_float(la[1])), ib = rcpf_(__uint_as_float(lb[1]));
    const unsigned obase = WS_O + (4 * mb + wave) * T32_D + 5 * j * 512;
    u32x4 va, vb2, vc;
#pragma unroll
    for (int e = 0; e < 4; ++e) { va[e] = pk2(oa[2 * e] * ia, oa[2 * e + 1] * ia); vb2[e] = pk2(ob[2 * e] * ib, ob[2 * e + 1] * ib); }
    vc = (u32x4){pk2(oa[8] * ia, oa[9] * ia), pk2(oa[10] * ia, oa[11] * ia), pk2(ob[8] * ib, ob[9] * ib), pk2(ob[10] * ib, ob[11] * ib)};
    STAMP(23);
    st16_sc1(va, rws, obase + 16 * lane, 0);
    st16_sc1(vb2, rws, obase + 3 * 512 + 16 * lane, 0);
    st16_sc1(vc, rws, lane < 32 ? obase + 2 * 512 + 16 * lane : 0x40000000u, 0);
    publish(ws, FL_O, mb, j, wave, lane, tag);
  }
  BAR();
  STAMP(7);
  const int t16 = lane & 15, g4 = lane >> 4;
  const int tok3 = 128 * mbL + 64 * th + 16 * wave + t16;
  u32x2 xres[5];
  const int jidx = 32 * (xcd >> 1) + ii, jper = (xcd & 1) ? 52 : 56;
  const bool ujob = jidx < 2 * jper;
  const int jmat = jidx >= jper, junit = ((xcd & 1) ? 56 : 0) + jidx - jmat * jper;
  auto jgi = [&](int k) { const int i = 64 * k + lane; return i < 80 ? 1280 + 80 * wave + i : i < 160 ? 960 + 80 * wave + i - 80 : 1280; };
  auto goff = [&](int k) { const int i = min(64 * k + lane, 119), kind = i / 40, r = i - 40 * kind; return WS_GRAN + ((kind == 0 ? 640 : kind == 1 ? 1280 : 1600) + 40 * nt + r) * 8; };
  u32x2 jg[3], vg2[2];
  gemv16_issue<5>(mkrsrc(jmat ? wu : wg, D_ * F_ * 2), 3456, 32 * (ujob ? junit : 0) + wave * 160 * 3456, L0 + L2_JW + wave * 5120, lane);
#pragma unroll
  for (int k = 0; k < 3; ++k) ald8_sc1(jg[k], rws, WS_GRAN + jgi(k) * 8, 0);
#pragma unroll
  for (int k = 0; k < 2; ++k) ald8_sc1(vg2[k], rws, goff(k), 0);
#pragma unroll
  for (int i = 0; i < 7; ++i) wo_piece(3, i);
#pragma unroll
  for (int p = 0; p < 5; ++p) xres[p] = ld8(rx, tok3 * 1280 + 160 * nt + 32 * ct_of(p, nt & 1) + 8 * g4, 0);
  MEMFENCE();
  const int nu = j < 12 ? 7 : 6, u0 = j < 12 ? 7 * j : 84 + 6 * (j - 12), uodd = u0 & 1;
  const rsrc_t rgu = mkrsrc(wave < 2 ? wg : wu, D_ * F_ * 2);
  unsigned guo[7];
#pragma unroll
  for (int i = 0; i < 7; ++i) {
    const int isup = wave >> 1, idx = 64 * (7 * (wave & 1) + i) + lane, rq = idx / 56, rem = idx - 56 * rq;
    int q, p, hf;
    if (rem < 48) { q = (rem >> 2) & 3; const int c = (rem & 3) ^ (isup ? 2 : 0); p = 2 * (rem >> 4) + (c >> 1); hf = c & 1; } else { q = (rem - 48) >> 1; p = 6; hf = rem & 1; }
    guo[i] = opaque((4 * rq + q) * 3456 + 32 * (u0 + unit_of(p, uodd, nu)) + 16 * hf);
  }
  vmwait<12>();
  asm volatile("" : "+v"(jg[0]), "+v"(jg[1]), "+v"(jg[2]), "+v"(vg2[0]), "+v"(vg2[1]));
  if (wave == 2) {
    unsigned gv[2] = {vg2[0][0], vg2[1][0]};
    if (!__all(vg2[0][1] == tag && vg2[1][1] == tag)) sweep<2>(ws, goff, [&](int) { return true; }, tag, gv);
#pragma unroll
    for (int k = 0; k < 2; ++k) if (k == 0 || lane < 56) {
      const int i = 64 * k + lane, kind = i / 40, r = i - 40 * kind;
      const int ct = r >> 3, p = (nt & 1) ? (ct == 0 ? 4 : ct - 1) : ct;
      f32x2 v = unpk(gv[k]);
      if (kind == 1) v = bf16r2(v + 1.0f);
      lds_st2f(L0 + L3_VEC + kind * 320 + p * 64 + (r & 7) * 8, v);
    }
  }
  {
    unsigned gv[3] = {jg[0][0], jg[1][0], jg[2][0]};
    if (!__all(jg[0][1] == tag && jg[1][1] == tag && jg[2][1] == tag))
      sweep<3>(ws, [&](int k) { return WS_GRAN + jgi(k) * 8; }, [&](int) { return true; }, tag, gv);
#pragma unroll
    for (int k = 0; k < 3; ++k) if (k < 2 || lane < 32) {
      const int i = 64 * k + lane;
      unsigned v = gv[k];
      if (i < 80) { const f32x2 t = unpk(v) + 1.0f; v = pk2(t.x, t.y); }
      lds_st1(L0 + L2_JV + (i < 80 ? 320 * wave + 4 * i : 1280 + 320 * wave + 4 * (i - 80)), v);
    }
    asm volatile("s_waitcnt lgkmcnt(0)" ::: "memory");
    const f32x4 d = gemv16_run<5>(L0 + L2_JW + wave * 5120, L0 + L2_JV + wave * 320, 1, lane);
    lds_st4(L0 + L2_JV + 2560 + wave * 1024 + lane * 16, __builtin_bit_cast(u32x4, d));
  }
  if (wave == 3 && bid == 0) {
    unsigned fv[16];
    sweep<16>(ws, [&](int k) { return flagoff(FL_KV, k, lane); }, [&](int) { return true; }, tag, fv);
    if (lane == 0) __hip_atomic_store((GAS unsigned*)(ws + WS_EPOCH), tag, __ATOMIC_RELAXED, __HIP_MEMORY_SCOPE_AGENT);
  }
  if (wave == 0) wait_flags(ws, flagoff(FL_O, mbL, lane), true, tag);
  vmwait<0>();
  STAMP(20);
  BAR();
  STAMP(8);

  const unsigned b80_0 = g4 * 1024 + ((lane & 15) >> 2) * 64 + (g4 & 1) * 32 + (lane & 3) * 8;
  const unsigned b80_1 = b80_0 ^ 32u;
  const unsigned b80_2 = 4096 + 8 * lane;
  struct B80 { unsigned a, b, c; };
  auto b80 = [&](unsigned kbase) { return B80{opaque(kbase + b80_0), opaque(kbase + b80_1), opaque(kbase + b80_2)}; };
  auto w80 = [&](const B80& B, int koff, int p) { const unsigned a = (p == 4 ? B.c : ((p & 1) ? B.b : B.a) + (p >> 1) * 256) + koff; return trfrag(a, a + 512); };

  u32x2 x1p[5];
  {
    u32x4 of[20];
    const unsigned ofb = WS_O + (4 * mbL + 2 * th + (wave >> 1)) * T32_D + g4 * 512 + ((wave & 1) * 16 + t16) * 16;
    const Rot R3 = mkrot(rL, 5 * rL, 20, 2048);
    auto fo20 = [&](int s) { return R3.b[s < 5 ? 0 : s < 10 ? 1 : s < 15 ? 2 : 3] + s * 2048; };
#pragma unroll
    for (int s = 0; s < P3F0; ++s) ald16_sc1(of[s], rws, ofb, fo20(s));
    f32x4 acc[5];
#pragma unroll
    for (int p = 0; p < 5; ++p) acc[p] = (f32x4){};
    const B80 B0 = b80(L0 + L3_W0), B1 = b80(L0 + L3_W0 + 10 * 5120), B2 = b80(L0 + L3_W1);
    auto rd3 = [&](int s, int p) { return s < 10 ? w80(B0, s * 5120, p) : s < L3_NPRE ? w80(B1, (s - 10) * 5120, p) : w80(B2, (s - L3_NPRE) * 5120, p); };
    s16x8 w[3][5];
#pragma unroll
    for (int p = 0; p < 5; ++p) { w[0][p] = rd3(0, p); w[1][p] = rd3(1, p); }
    SB();
    static_for<0, 20>([&](auto SS) {
      constexpr int s = SS;
      vmwait<p3_wait_frag(s)>(of[s]);
      SB();
#pragma unroll
      for (int p = 0; p < 5; ++p) {
        acc[p] = mfma16(w[s % 3][p], of[s], acc[p]);
        SB();
        if (p == 0 && s + P3F0 < 20) ald16_sc1(of[s + P3F0 < 20 ? s + P3F0 : 0], rws, ofb, fo20(s + P3F0));
        if (s + 2 < 20) w[(s + 2) % 3][p] = rd3(s + 2, p);
        SB();
      }
    });
    BAR();
    STAMP(9);
    if (ujob && wave == 1) {
      f32x4 d = lds_f4(L0 + L2_JV + 2560 + lane * 16);
#pragma unroll
      for (int w = 1; w < 4; ++w) d += lds_f4(L0 + L2_JV + 2560 + w * 1024 + lane * 16);
      if ((lane & 15) < 2) {
#pragma unroll
        for (int r = 0; r < 4; ++r) st_u64(ws, WS_UV + ((jmat * 108 + junit) * 32 + (lane & 15) * 16 + 4 * (lane >> 4) + r) * 8, ((u64)tag << 32) | __float_as_uint(d[r]));
      }
    }

    f32x2 xv[5][2]; f32x2 s2 = {0.f, 0.f};
    u32x2 yp[5];
    const unsigned vecb = opaque(L0 + L3_VEC + g4 * 16);
#pragma unroll
    for (int p = 0; p < 5; ++p) {
      const f32x4 ga = lds_f4(vecb + p * 64), sb1 = lds_f4(vecb + 320 + p * 64);
#pragma unroll
      for (int r = 0; r < 2; ++r) {
        const f32x2 at = bf16r2((f32x2){acc[p][2 * r], acc[p][2 * r + 1]});
        const f32x2 xs = unpk(xres[p][r]) + bf16r2(at * (f32x2){ga[2 * r], ga[2 * r + 1]});
        x1p[p][r] = pk2(xs.x, xs.y);
        xv[p][r] = unpk(x1p[p][r]);
        s2 += xv[p][r];
        const f32x2 y = xv[p][r] * (f32x2){sb1[2 * r], sb1[2 * r + 1]};
        yp[p][r] = pk2(y.x, y.y);
      }
    }
    const unsigned yb = WS_Y + (4 * mbL + 2 * th + (wave >> 1)) * T32_D + 10 * nt * 512 + ((wave & 1) * 16 + t16) * 16;
#pragma unroll
    for (int pp = 0; pp < 3; ++pp) {
      const int pa = 2 * pp, pb = pp < 2 ? 2 * pp + 1 : 4;
      const auto r0 = __builtin_amdgcn_permlane16_swap(yp[pa][0], yp[pb][0], false, false), r1 = __builtin_amdgcn_permlane16_swap(yp[pa][1], yp[pb][1], false, false);
      const int ct = (g4 & 1) ? ct_of(pb, nt & 1) : ct_of(pa, nt & 1);
      st16_sc1((u32x4){r0[0], r1[0], r0[1], r1[1]}, rws, (pp < 2 || !(g4 & 1)) ? yb + (2 * ct + (g4 >> 1)) * 512 : 0x40000000u, 0);
    }
    const float mt = xsum32(xsum16(s2.x + s2.y)) * (1.0f / 80);
    f32x2 m2 = {0.f, 0.f};
#pragma unroll
    for (int p = 0; p < 5; ++p)
#pragma unroll
      for (int r = 0; r < 2; ++r) { const f32x2 d = xv[p][r] - mt; m2 += d * d; }
    const float m2s = xsum32(xsum16(m2.x + m2.y));
    if (lane < 16) st_u64(ws, WS_STAT + (((mbL * 2 + th) * 8 + nt) * 64 + 16 * wave + t16) * 8, ((u64)__float_as_uint(m2s) << 32) | __float_as_uint(mt));
    publish(ws, FL_Y, mbL, jL, wave, lane, tag);
  }
  STAMP(10);

  const int so4 = 0;
  auto gu_piece = [&](int st, int i) { dma16(rgu, guo[i], rot10(so4, st) * 64 * 3456, L0 + (st % 5) * L4_STAGE + (wave >> 1) * L4_U + (wave & 1) * 7168 + i * 1024); };
  for (int st = 0; st < 4; ++st)
#pragma unroll
    for (int i = 0; i < 7; ++i) gu_piece(st, i);
  const rsrc_t rwd = mkrsrc(wd, F_ * D_ * 2);
  unsigned wdg[8];
#pragma unroll
  for (int i = 0; i < 8; ++i) {
    const int pc = min(8 * wave + i, 29), kl = pc / 5;
    wdg[i] = opaque(w80_voff(pc - 5 * kl, lane, nt, [](int g, int t, int q) { return 16 * (g >> 1) + 8 * t + 4 * (g & 1) + q; }) + kl * 32 * 1280);
  }
  auto wd_piece = [&](int G, int i) { const int g = G + goL; dma16(rwd, wdg[i], (g >= 9 ? g - 9 : g) * 6 * 32 * 1280, L0 + (G % 5) * 30720 + min(8 * wave + i, 29) * 1024); };
  const Rot R5 = mkrot(rD, 6 * goL, 54, 2048);
  auto fo54 = [&](int s) { return R5.b[s < 12 ? 0 : s < 30 ? 1 : s < 42 ? 2 : 3] + s * 2048; };
  STAMP(19);
  if (wave == 0) wait_flags(ws, flagoff(FL_Y, mb, lane), true, tag);
  if (wave == 1) {
    unsigned uv[7];
    sweep<7>(ws, [&](int p) { return WS_UV + (((lane >> 5) * 108 + u0 + unit_of(p, uodd, nu)) * 32 + (lane & 31)) * 8; }, [&](int) { return true; }, tag, uv);
#pragma unroll
    for (int p = 0; p < 7; ++p) lds_st1(L0 + L4_UV + (p * 64 + lane) * 4, uv[p]);
  }
  STAMP(21);
  vmwait<0>();
  BAR();
  STAMP(11);
  u32x2 stv[4];
#pragma unroll
  for (int i = 0; i < 4; ++i) { const int T = 32 * wave + (lane & 31); stv[i] = ld8_sc1(rws, WS_STAT + (((mb * 2 + (T >> 6)) * 8 + 4 * (lane >> 5) + i) * 64 + (T & 63)) * 8, 0); }
  MEMFENCE();
  u32x4 yf[40];
  const unsigned yfo = WS_Y + (4 * mb + wave) * T32_D + 16 * lane;
  const Rot R4 = mkrot(0, 0, 40, 1024);
#pragma unroll
  for (int ks = 0; ks < P4F0; ++ks) ald16_sc1(yf[ks], rws, yfo, fo40(R4, ks));
  f32x16 acc4[7];
#pragma unroll
  for (int b = 0; b < 7; ++b) acc4[b] = (f32x16){};
  {
    const int h = lane >> 5, blk = (lane >> 4) & 1, q = (lane & 15) >> 2;
    const unsigned c0 = L0 + blk * L4_U + 2 * h * 896 + q * 64 + blk * 32 + (lane & 3) * 8;
    const unsigned c2 = L0 + blk * L4_U + 2 * h * 896 + 768 + q * 32 + (lane & 3) * 8;
    unsigned cb0[5], cb1[5], cb2[5];
#pragma unroll
    for (int sl = 0; sl < 5; ++sl) { cb0[sl] = opaque(c0 + sl * L4_STAGE); cb1[sl] = opaque((c0 ^ 32u) + sl * L4_STAGE); cb2[sl] = opaque(c2 + sl * L4_STAGE); }
    auto readw = [&](int ks, int p) {
      const int sl = (ks >> 2) % 5;
      const unsigned a = (p == 6 ? cb2[sl] : ((p & 1) ? cb1[sl] : cb0[sl]) + (p >> 1) * 256) + (ks & 3) * 4 * 896;
      return trfrag(a, a + 896); };
    s16x8 w[3][7];
#pragma unroll
    for (int p = 0; p < 7; ++p) { w[0][p] = readw(0, p); w[1][p] = readw(1, p); }
    SB();
    static_for<0, 40>([&](auto KS) {
      constexpr int ks = KS, c = ks % 3, n = (ks + 2) % 3;
      if constexpr ((ks & 3) == 2) {
        constexpr int st = (ks + 2) / 4;
        if constexpr (st <= 9) STAMPN(30 + st - 1);
        if constexpr (st >= 4 && st <= 9) vmwait<p4_wait_stage(st)>();
        BAR_RAW();
      }
      vmwait<p4_wait_frag(ks)>(yf[ks]);
      SB();
#pragma unroll
      for (int p = 0; p < 7; ++p) {
        acc4[p] = mfma32(w[c][p], yf[ks], acc4[p]);
        SB();
        if (p == 1 && ks + P4F0 < 40) ald16_sc1(yf[ks + P4F0 < 40 ? ks + P4F0 : 0], rws, yfo, fo40(R4, ks + P4F0));
        if (p == 3 && p4_pieces(ks) > 0) gu_piece((ks + 14) / 4, 2 * ((ks + 14) % 4));
        if (p == 5 && p4_pieces(ks) > 1) gu_piece((ks + 14) / 4, 2 * ((ks + 14) % 4) + 1);
        if (p == 3 && p4_touch(ks) > 0) wd_piece(p4_wd_before(ks) / 8, p4_wd_before(ks) % 8);
        if (p == 5 && p4_touch(ks) > 1) wd_piece((p4_wd_before(ks) + 1) / 8, (p4_wd_before(ks) + 1) % 8);
        if (ks + 2 < 40) w[n][p] = readw(ks + 2, p);
        SB();
      }
    });
  }
  STAMP(12);
  {
    const int h = lane >> 5;
    float ms = 0.f;
#pragma unroll
    for (int i = 0; i < 4; ++i) ms += __uint_as_float(stv[i][0]);
    const float mean = xsum32(ms) * 0.125f;
    float m2 = 0.f;
#pragma unroll
    for (int i = 0; i < 4; ++i) { const float d = __uint_as_float(stv[i][0]) - mean; m2 += __uint_as_float(stv[i][1]) + 80.0f * d * d; }
    const float rs = rden_of(xsum32(m2) * (1.0f / D_)), nm = -rs * bf16r(mean);
    const unsigned ab = WS_ACT + (4 * mb + wave) * T32_F + 16 * lane, uvb = opaque(L0 + L4_UV + 16 * h);
#pragma unroll
    for (int p = 0; p < 7; ++p) {
      if (p < 6 || nu == 7) {
        const unsigned tb = uvb + p * 256;
        u32x4 ov;
#pragma unroll
        for (int half = 0; half < 2; ++half) {
          const f32x4 ug = lds_f4(tb + 32 * half), vg = lds_f4(tb + 64 + 32 * half), uu = lds_f4(tb + 128 + 32 * half), vu = lds_f4(tb + 192 + 32 * half);
#pragma unroll
          for (int r = 0; r < 2; ++r) {
            const f32x2 ag = {acc4[p][4 * half + 2 * r], acc4[p][4 * half + 2 * r + 1]}, au = {acc4[p][8 + 4 * half + 2 * r], acc4[p][8 + 4 * half + 2 * r + 1]};
            const f32x2 g = rs * ag + (nm * (f32x2){ug[2 * r], ug[2 * r + 1]} + (f32x2){vg[2 * r], vg[2 * r + 1]});
            const f32x2 u = rs * au + (nm * (f32x2){uu[2 * r], uu[2 * r + 1]} + (f32x2){vu[2 * r], vu[2 * r + 1]});
            const f32x2 e = g * (-LOG2E);
            const f32x2 den = (f32x2){exp2_(e.x), exp2_(e.y)} + 1.0f;
            const f32x2 a = (g * u) * (f32x2){rcpf_(den.x), rcpf_(den.y)};
            ov[2 * half + r] = pk2(a.x, a.y);
          }
        }
        st16_sc1(ov, rws, ab + (u0 + unit_of(p, uodd, nu)) * 1024, 0);
      }
    }
    publish(ws, FL_ACT, mb, j, wave, lane, tag);
  }
  BAR();
  STAMP(13);

#pragma unroll
  for (int i = 4; i < 8; ++i) wd_piece(2, i);
#pragma unroll
  for (int i = 0; i < 8; ++i) wd_piece(3, i);
#pragma unroll
  for (int i = 0; i < 8; ++i) wd_piece(4, i);
  STAMP(22);
  if (wave == 0) wait_flags(ws, flagoff(FL_ACT, mbL, lane), true, tag);
  vmwait<0>();
  BAR();
  STAMP(14);
  {
    u32x4 af[54];
    const unsigned afb = WS_ACT + (4 * mbL + 2 * th + (wave >> 1)) * T32_F + g4 * 512 + ((wave & 1) * 16 + t16) * 16;
#pragma unroll
    for (int s = 0; s < P5F0; ++s) ald16_sc1(af[s], rws, afb, fo54(s));
    f32x4 acc[5];
#pragma unroll
    for (int p = 0; p < 5; ++p) acc[p] = (f32x4){};
    const B80 B5[3] = {b80(L0), b80(L0 + 61440), b80(L0 + 122880)};
    auto rd5 = [&](int s, int p) { const int sl = s % 30; return w80(B5[sl / 12], (sl % 12) * 5120, p); };
    s16x8 w[3][5];
#pragma unroll
    for (int p = 0; p < 5; ++p) { w[0][p] = rd5(0, p); w[1][p] = rd5(1, p); }
    SB();
    static_for<0, 54>([&](auto SS) {
      constexpr int s = SS;
      if constexpr (s % 6 == 4 && s + 2 < 54) {
        constexpr int g = (s + 2) / 6;
        STAMPN(40 + g - 1);
        if constexpr (g >= 5) vmwait<p5_wait_group(g)>();
        BAR_RAW();
      }
      vmwait<p5_wait_frag(s)>(af[s]);
      SB();
#pragma unroll
      for (int p = 0; p < 5; ++p) {
        acc[p] = mfma16(w[s % 3][p], af[s], acc[p]);
        SB();
        constexpr int r = (s + 20) % 6, i0 = r < 2 ? 2 * r : r + 2;
        if (p == 0 && s + P5F0 < 54) ald16_sc1(af[s + P5F0 < 54 ? s + P5F0 : 0], rws, afb, fo54(s + P5F0));
        if (p == 2 && p5_pieces(s) > 0) wd_piece((s + 20) / 6, i0);
        if (p == 4 && p5_pieces(s) > 1) wd_piece((s + 20) / 6, i0 + 1);
        if (s + 2 < 54) w[(s + 2) % 3][p] = rd5(s + 2, p);
        SB();
      }
    });
    STAMP(15);
    u32x2 op[5];
    const unsigned gbb = opaque(L0 + L3_VEC + 640 + g4 * 16);
#pragma unroll
    for (int p = 0; p < 5; ++p) {
      const f32x4 gb = lds_f4(gbb + p * 64);
#pragma unroll
      for (int r = 0; r < 2; ++r) {
        const f32x2 dn = bf16r2((f32x2){acc[p][2 * r], acc[p][2 * r + 1]});
        const f32x2 o = unpk(x1p[p][r]) + bf16r2(dn * (f32x2){gb[2 * r], gb[2 * r + 1]});
        op[p][r] = pk2(o.x, o.y);
      }
    }
    const rsrc_t ro = mkrsrc(out, S_ * D_ * 2);
#pragma unroll
    for (int pp = 0; pp < 3; ++pp) {
      const int pa = 2 * pp, pb = pp < 2 ? 2 * pp + 1 : 4;
      const auto r0 = __builtin_amdgcn_permlane16_swap(op[pa][0], op[pb][0], false, false), r1 = __builtin_amdgcn_permlane16_swap(op[pa][1], op[pb][1], false, false);
      const int ct = (g4 & 1) ? ct_of(pb, nt & 1) : ct_of(pa, nt & 1);
      __builtin_amdgcn_raw_buffer_store_b128((u32x4){r0[0], r1[0], r0[1], r1[1]}, ro, (pp < 2 || !(g4 & 1)) ? tok3 * 1280 + 160 * nt + 32 * ct + 16 * (g4 >> 1) : 0x40000000u, 0, 0);
    }
  }
  STAMP(16);
  STAMP_FLUSH();
}

extern "C" void kernel_launch(void* const* d_in, const int* in_sizes, int n_in, void* d_out, int out_size, void* d_ws, size_t ws_size,
                              hipStream_t stream) {
  (void)in_sizes; (void)n_in; (void)out_size; (void)ws_size;
  adaln_block_kernel<<<256, 256, 0, stream>>>((const char*)d_in[0], (const char*)d_in[1], (const char*)d_in[2], (const char*)d_in[4], (const char*)d_in[5],
                                              (const char*)d_in[6], (const char*)d_in[7], (const char*)d_in[8], (char*)d_out, (char*)d_ws);
}
```

```cpp
#include <hip/hip_runtime.h>
#include <stdint.h>
#include <type_traits>

#define LAS __attribute__((address_space(3)))
#define GAS __attribute__((address_space(1)))
#define DI __device__ __forceinline__
typedef float f32x2 __attribute__((ext_vector_type(2)));
typedef float f32x4 __attribute__((ext_vector_type(4)));
typedef float f32x16 __attribute__((ext_vector_type(16)));
typedef unsigned u32x2 __attribute__((ext_vector_type(2)));
typedef unsigned u32x4 __attribute__((ext_vector_type(4)));
typedef short s16x4 __attribute__((ext_vector_type(4)));
typedef short s16x8 __attribute__((ext_vector_type(8)));
typedef __bf16 bf16x2 __attribute__((ext_vector_type(2)));
typedef __bf16 bf16x8 __attribute__((ext_vector_type(8)));
typedef unsigned long long u64;
typedef __amdgpu_buffer_rsrc_t rsrc_t;

constexpr int S_ = 2048, D_ = 640, F_ = 1728;
constexpr int T32_D = 80 * 512;
constexpr int T32_F = 216 * 512;

constexpr int WS_EPOCH = 0;
constexpr int WS_TMO = 256;
constexpr int WS_FLAG = 29 << 20;
constexpr int FL_XN = 0, FL_KV = 1, FL_O = 2, FL_Y = 3, FL_ACT = 4;
constexpr int WS_GRAN = 256 * 1024;
constexpr int WS_UV = 320 * 1024;
constexpr int WS_STAT = 512 * 1024;
constexpr int WS_STAMP = 28 << 20;
constexpr int WS_XN = 1 << 20;
constexpr int WS_KV = 4 << 20;
constexpr int WS_O = 12 << 20;
constexpr int WS_Y = 16 << 20;
constexpr int WS_ACT = 20 << 20;
constexpr int WS_USED = 30 << 20;

constexpr int LDS_BYTES = 163840;
constexpr int L1_W = 0;
constexpr int L0_TB = 122880;
constexpr int L0_JW = 133120;
constexpr int L1_VS = 153600, L1_VA = 154880;
constexpr int L0_SC = 158720;
constexpr int L2_KV = 0;
constexpr int KV_IMG = 384 * 48;
constexpr int L3_W0 = 4 * KV_IMG;
constexpr int L3_NPRE = 15;
constexpr int L3_W1 = 0;
constexpr int L2_JW = 73728, L2_JV = 94208;
constexpr int L3_JP = 156160;
constexpr int L3_XR = 150528;
constexpr int L_DUMMY = 162816;
constexpr int L3_VEC = 161024;
constexpr int L4_STAGE = 30720, L4_U = 14464;
constexpr int L4_UV = 153600;
static_assert(L3_XR + 64 * 160 <= L3_VEC && L3_W0 + L3_NPRE * 5120 <= L3_XR, "P3 residual rows");
static_assert(L3_W0 + L3_NPRE * 5120 <= L3_VEC && L3_VEC + 960 <= LDS_BYTES, "P3 LDS map");
static_assert(30 * 5120 <= L3_VEC, "P5 LDS map");
static_assert(L4_UV + 1792 <= L3_VEC, "P4 LDS map");

constexpr float LOG2E = 1.4426950408889634f;
constexpr float SCORE_C = 0.22360679774997896f * 1.4426950408889634f;
constexpr float NEGBIG = -1.0e30f;
constexpr unsigned SPIN_CAP = 1u << 15;

#define SB() __builtin_amdgcn_sched_barrier(0)
#define BAR() do { asm volatile("s_waitcnt lgkmcnt(0)" ::: "memory"); __builtin_amdgcn_s_barrier(); asm volatile("" ::: "memory"); } while (0)
#define BAR_RAW() do { asm volatile("" ::: "memory"); __builtin_amdgcn_s_barrier(); asm volatile("" ::: "memory"); } while (0)
#define MEMFENCE() asm volatile("" ::: "memory")
template <int N> DI void vmwait() { asm volatile("s_waitcnt vmcnt(%0)" :: "n"(N) : "memory"); }
template <int N> DI void vmwait(u32x4& d) { asm volatile("s_waitcnt vmcnt(%1)" : "+v"(d) : "n"(N) : "memory"); }

DI unsigned pk2(float a, float b) { bf16x2 v = __builtin_convertvector((f32x2){a, b}, bf16x2); return __builtin_bit_cast(unsigned, v); }
DI float blo(unsigned v) { return __uint_as_float(v << 16); }
DI float bhi(unsigned v) { return __uint_as_float(v & 0xffff0000u); }
DI float bf16r(float a) { return blo(pk2(a, a)); }
DI f32x2 bf16r2(f32x2 a) { const unsigned p = pk2(a.x, a.y); return (f32x2){blo(p), bhi(p)}; }
DI f32x2 unpk(unsigned p) { return (f32x2){blo(p), bhi(p)}; }
DI float rcpf_(float x) { return __builtin_amdgcn_rcpf(x); }
DI float exp2_(float x) { return __builtin_amdgcn_exp2f(x); }
DI float siluf(float x) { return x * rcpf_(1.0f + exp2_(-LOG2E * x)); }
DI float rden_of(float var) { return rcpf_(bf16r(__builtin_amdgcn_sqrtf(bf16r(bf16r(var) + 1e-5f)))); }
template <int CTRL> DI float dpp_add(float x) { return x + __int_as_float(__builtin_amdgcn_update_dpp(0, __float_as_int(x), CTRL, 0xf, 0xf, true)); }
DI float sum16(float x) { x = dpp_add<0xB1>(x); x = dpp_add<0x4E>(x); x = dpp_add<0x141>(x); return dpp_add<0x140>(x); }
DI float xsum16(float x) { auto r = __builtin_amdgcn_permlane16_swap(__float_as_uint(x), __float_as_uint(x), false, false); return __uint_as_float(r[0]) + __uint_as_float(r[1]); }
DI float xsum32(float x) { auto r = __builtin_amdgcn_permlane32_swap(__float_as_uint(x), __float_as_uint(x), false, false); return __uint_as_float(r[0]) + __uint_as_float(r[1]); }
DI float xmax32(float x) { auto r = __builtin_amdgcn_permlane32_swap(__float_as_uint(x), __float_as_uint(x), false, false); return fmaxf(__uint_as_float(r[0]), __uint_as_float(r[1])); }
DI rsrc_t mkrsrc(const void* p, unsigned bytes) { return __builtin_amdgcn_make_buffer_rsrc((void*)p, (short)0, (int)bytes, 0x00020000); }
DI u32x4 ld16_sc1(rsrc_t r, unsigned voff, unsigned soff) { return __builtin_amdgcn_raw_buffer_load_b128(r, voff, soff, 16); }
DI u32x4 ld16(rsrc_t r, unsigned voff, unsigned soff) { return __builtin_amdgcn_raw_buffer_load_b128(r, voff, soff, 0); }
DI u32x2 ld8(rsrc_t r, unsigned voff, unsigned soff) { return __builtin_amdgcn_raw_buffer_load_b64(r, voff, soff, 0); }
DI u32x2 ld8_sc1(rsrc_t r, unsigned voff, unsigned soff) { return __builtin_amdgcn_raw_buffer_load_b64(r, voff, soff, 16); }
DI void st16_sc1(u32x4 v, rsrc_t r, unsigned voff, unsigned soff) { __builtin_amdgcn_raw_buffer_store_b128(v, r, voff, soff, 16); }
DI void ald16_sc1(u32x4& d, rsrc_t r, unsigned voff, unsigned soff) {
  asm volatile("buffer_load_dwordx4 %0, %1, %2, %3 offen sc1" : "=v"(d) : "v"(voff), "s"(r), "s"(soff) : "memory");
}
DI void ald16(u32x4& d, rsrc_t r, unsigned voff, unsigned soff) {
  asm volatile("buffer_load_dwordx4 %0, %1, %2, %3 offen" : "=v"(d) : "v"(voff), "s"(r), "s"(soff) : "memory");
}
DI void ald4(unsigned& d, rsrc_t r, unsigned voff, unsigned soff) {
  asm volatile("buffer_load_dword %0, %1, %2, %3 offen" : "=v"(d) : "v"(voff), "s"(r), "s"(soff) : "memory");
}
DI void ald8(u32x2& d, rsrc_t r, unsigned voff, unsigned soff) {
  asm volatile("buffer_load_dwordx2 %0, %1, %2, %3 offen" : "=v"(d) : "v"(voff), "s"(r), "s"(soff) : "memory");
}
DI void ald8_sc1(u32x2& d, rsrc_t r, unsigned voff, unsigned soff) {
  asm volatile("buffer_load_dwordx2 %0, %1, %2, %3 offen sc1" : "=v"(d) : "v"(voff), "s"(r), "s"(soff) : "memory");
}
DI void touch(rsrc_t r, unsigned voff, unsigned soff, unsigned ldsaddr) {
  asm volatile("s_mov_b32 m0, %0\n\ts_nop 0\n\tbuffer_load_dword %1, %2, %3 offen lds" :: "s"(ldsaddr), "v"(voff), "s"(r), "s"(soff) : "memory");
}
DI void dma16_sc1(rsrc_t r, unsigned voff, unsigned soff, unsigned ldsaddr) {
  asm volatile("s_mov_b32 m0, %0\n\ts_nop 0\n\tbuffer_load_dwordx4 %1, %2, %3 offen sc1 lds" :: "s"(ldsaddr), "v"(voff), "s"(r), "s"(soff) : "memory");
}
DI void dma16(rsrc_t r, unsigned voff, unsigned soff, unsigned ldsaddr) {
  asm volatile("s_mov_b32 m0, %0\n\ts_nop 0\n\tbuffer_load_dwordx4 %1, %2, %3 offen lds" :: "s"(ldsaddr), "v"(voff), "s"(r), "s"(soff) : "memory");
}
DI u64 ld_u64(const char* ws, unsigned off) { return __hip_atomic_load((GAS u64*)(ws + off), __ATOMIC_RELAXED, __HIP_MEMORY_SCOPE_AGENT); }
DI void st_u64(char* ws, unsigned off, u64 v) { __hip_atomic_store((GAS u64*)(ws + off), v, __ATOMIC_RELAXED, __HIP_MEMORY_SCOPE_AGENT); }
DI u64 flagval(unsigned tag) { return ((u64)tag << 32) | (u64)(~tag); }
DI unsigned flagoff(int kind, int mb, int jw) { return WS_FLAG + ((kind * 16 + mb) * 64 + jw) * 128; }
DI void publish(char* ws, int kind, int mb, int j, int wave, int lane, unsigned tag) {
  vmwait<0>();
  if (lane == 0) st_u64(ws, flagoff(kind, mb, 4 * j + wave), flagval(tag));
}
DI bool spin_fail(unsigned& spins, char* ws, unsigned tag) {
  __builtin_amdgcn_s_sleep(1);
  if (++spins > SPIN_CAP) { __hip_atomic_store((GAS unsigned*)(ws + WS_TMO), tag, __ATOMIC_RELAXED, __HIP_MEMORY_SCOPE_AGENT); return true; }
  return (spins & 127u) == 0u && (unsigned)__builtin_amdgcn_readfirstlane(__hip_atomic_load((GAS unsigned*)(ws + WS_TMO), __ATOMIC_RELAXED, __HIP_MEMORY_SCOPE_AGENT)) == tag;
}
DI void wait_flags(char* ws, unsigned off, bool on, unsigned tag) {
  const u64 want = flagval(tag);
#pragma clang loop unroll(disable)
  for (unsigned spins = 0;;) {
    u64 v = want;
    if (on) v = ld_u64(ws, off);
    if (__all(v == want)) break;
    if (spin_fail(spins, ws, tag)) break;
  }
  MEMFENCE();
}
template <int N, class OFF, class ON> DI void sweep(char* ws, OFF off, ON on, unsigned tag, unsigned (&val)[N]) {
#pragma clang loop unroll(disable)
  for (unsigned spins = 0;;) {
    bool ok = true;
#pragma unroll
    for (int k = 0; k < N; ++k) { u64 g = (u64)tag << 32; if (on(k)) g = ld_u64(ws, off(k)); val[k] = (unsigned)g; ok &= (unsigned)(g >> 32) == tag; }
    if (__all(ok)) break;
    if (spin_fail(spins, ws, tag)) break;
  }
  MEMFENCE();
}
DI s16x8 trfrag(unsigned a0, unsigned a1) {
  const s16x4 lo = __builtin_amdgcn_ds_read_tr16_b64_v4i16((LAS s16x4*)(size_t)a0);
  const s16x4 hi = __builtin_amdgcn_ds_read_tr16_b64_v4i16((LAS s16x4*)(size_t)a1);
  return __builtin_shufflevector(lo, hi, 0, 1, 2, 3, 4, 5, 6, 7);
}
DI f32x4 lds_f4(unsigned a) { return *(LAS f32x4*)(size_t)a; }
DI u32x4 lds_u4(unsigned a) { return *(LAS u32x4*)(size_t)a; }
DI void lds_st4(unsigned a, u32x4 v) { *(LAS u32x4*)(size_t)a = v; }
DI void lds_st1(unsigned a, unsigned v) { *(LAS unsigned*)(size_t)a = v; }
DI void lds_st2f(unsigned a, f32x2 v) { *(LAS f32x2*)(size_t)a = v; }
DI unsigned opaque(unsigned v) { asm volatile("" : "+v"(v)); return v; }
DI unsigned sopaque(unsigned v) { asm volatile("" : "+s"(v)); return v; }
struct Rot { unsigned b[4]; };
DI Rot mkrot(int r, int shift, int N, int step) {
  const unsigned a = shift * step, w = N * step;
  return Rot{{sopaque(a), sopaque(a - (r >= 3 ? w : 0)), sopaque(a - (r >= 2 ? w : 0)), sopaque(a - (r >= 1 ? w : 0))}};
}
DI f32x16 mfma32(s16x8 a, u32x4 b, f32x16 c) { return __builtin_amdgcn_mfma_f32_32x32x16_bf16(__builtin_bit_cast(bf16x8, a), __builtin_bit_cast(bf16x8, b), c, 0, 0, 0); }
DI f32x4 mfma16(s16x8 a, u32x4 b, f32x4 c) { return __builtin_amdgcn_mfma_f32_16x16x32_bf16(__builtin_bit_cast(bf16x8, a), __builtin_bit_cast(bf16x8, b), c, 0, 0, 0); }
DI void smfma_c(f32x16& d, u32x4 a, u32x4 b, const f32x16& c) { asm("v_mfma_f32_32x32x16_bf16 %0, %1, %2, %3" : "=&v"(d) : "v"(a), "v"(b), "v"(c)); }
DI void smfma_0(f32x16& d, u32x4 a, u32x4 b) { asm("v_mfma_f32_32x32x16_bf16 %0, %1, %2, 0" : "=&v"(d) : "v"(a), "v"(b)); }
DI void smfma_a(f32x16& d, u32x4 a, u32x4 b) { asm("v_mfma_f32_32x32x16_bf16 %0, %1, %2, %0" : "+v"(d) : "v"(a), "v"(b)); }
DI void sfence(f32x16& d) { asm volatile("s_nop 11" : "+v"(d)); }
DI float fmix_lo(unsigned x, unsigned s, unsigned a) { float r; asm("v_fma_mix_f32 %0, %1, %2, %3 op_sel_hi:[1,1,1]" : "=v"(r) : "v"(x), "v"(s), "v"(a)); return r; }
DI float fmix_hi(unsigned x, unsigned s, unsigned a) { float r; asm("v_fma_mix_f32 %0, %1, %2, %3 op_sel:[1,1,1] op_sel_hi:[1,1,1]" : "=v"(r) : "v"(x), "v"(s), "v"(a)); return r; }
DI unsigned pkh(float a, float b) { return __builtin_bit_cast(unsigned, __builtin_amdgcn_cvt_pkrtz(a, b)); }

#define STAMP(i) do { } while (0)
#define STAMPN(i) do { } while (0)
#define STAMP_INIT() do { } while (0)
#define STAMP_FLUSH() do { } while (0)

template <int NS> DI void gemv16_issue(rsrc_t rw, unsigned rowstride, unsigned srcbyte, unsigned ldsw, int lane) {
  const unsigned vo = (8 * ((lane >> 3) & 3) + 4 * (lane >> 5) + ((lane >> 1) & 3)) * rowstride + 16 * (lane & 1);
#pragma unroll 5
  for (int s = 0; s < NS; ++s) dma16(rw, vo, s * 32 * rowstride + srcbyte, ldsw + s * 1024);
}
template <int NS> DI f32x4 gemv16_run(unsigned ldsw, unsigned ldsv, int vmask, int lane) {
  f32x4 acc0 = {}, acc1 = {};
  const unsigned wa = opaque(ldsw + 8 * lane), va = opaque(ldsv + (lane & vmask) * 1280 + (lane >> 4) * 16);
  s16x8 w[3]; u32x4 v[3];
#pragma unroll
  for (int s = 0; s < 2; ++s) { w[s] = trfrag(wa + s * 1024, wa + s * 1024 + 512); v[s] = lds_u4(va + s * 64); }
  SB();
#pragma unroll
  for (int s = 0; s < NS; ++s) {
    if (s + 2 < NS) { w[(s + 2) % 3] = trfrag(wa + (s + 2) * 1024, wa + (s + 2) * 1024 + 512); v[(s + 2) % 3] = lds_u4(va + (s + 2) * 64); }
    SB();
    if (s & 1) acc1 = mfma16(w[s % 3], v[s % 3], acc1); else acc0 = mfma16(w[s % 3], v[s % 3], acc0);
    SB();
  }
  return acc0 + acc1;
}

DI int ct_of(int p, int odd) { return odd ? (p < 4 ? p + 1 : 0) : p; }
DI int unit_of(int p, int odd, int nu) { const int u = odd ? (p < 6 ? p + 1 : 0) : p; return u < nu ? u : nu - 1; }

template <class RM> DI unsigned w80_voff(int piece, int lane, int nt, RM rowmap) {
  const int odd = nt & 1;
  int g, t, q, p, hf;
  if (piece < 4) { g = piece; t = lane >> 5; const int su = (lane >> 4) & 1; q = (lane >> 2) & 3; const int c = (lane & 3) ^ (2 * (piece & 1)); p = 2 * su + (c >> 1); hf = c & 1; }
  else { t = lane >> 5; g = (lane >> 3) & 3; q = (lane >> 1) & 3; p = 4; hf = lane & 1; }
  return rowmap(g, t, q) * 1280 + 160 * nt + 32 * ct_of(p, odd) + 16 * hf;
}

template <class OPS> constexpr int vm_sum(OPS ops, int lo, int hi) { int n = 0; for (int k = lo; k <= hi; ++k) if (k >= 0) n += ops(k); return n; }
constexpr int P1F0 = 10;
constexpr int p1_pieces(int k) { return k < 24 ? 1 : 0; }
constexpr int p1_ops(int k) { return (k < 40 - P1F0 ? 1 : 0) + p1_pieces(k) + (k >= 30 && k < 38 ? 1 : 0); }
constexpr int p1_wait_frag(int f) {
  return f < P1F0 ? 8 + vm_sum(p1_ops, 0, f - 3) : p1_pieces(f - P1F0) + vm_sum(p1_ops, f - P1F0 + 1, f - 3);
}
constexpr int p1_wait_stage(int st) { return st == 2 ? 4 + vm_sum(p1_ops, 0, 5) : st == 3 ? vm_sum(p1_ops, 0, 9) : vm_sum(p1_ops, 4 * st - 12, 4 * st - 3); }
constexpr int P4F0 = 8;
constexpr int p4_pieces(int k) { return k >= 2 && k <= 25 ? ((k + 14) % 4 == 3 ? 1 : 2) : 0; }
constexpr int p4_wd_before(int k) { return k <= 26 ? 0 : k == 27 ? 2 : k == 28 ? 3 : k == 29 ? 5 : k == 30 ? 6 : k == 31 ? 8 : k == 32 ? 9 : k == 33 ? 11 : k == 34 ? 12 : k == 35 ? 14 : k <= 40 ? 16 + (k - 36) : 20; }
constexpr int p4_touch(int k) { return p4_wd_before(k + 1) - p4_wd_before(k); }
constexpr int p4_ops(int k) { return (k < 40 - P4F0 ? 1 : 0) + p4_pieces(k) + p4_touch(k); }
constexpr int p4_wait_frag(int ks) { return ks < P4F0 ? (P4F0 - 1 - ks) + vm_sum(p4_ops, 0, ks - 1) : p4_pieces(ks - P4F0) + p4_touch(ks - P4F0) + vm_sum(p4_ops, ks - P4F0 + 1, ks - 1); }
constexpr int p4_wait_stage(int st) { return vm_sum(p4_ops, 4 * st - 10, 4 * st - 3); }
constexpr int P5F0 = 12;
constexpr int p5_pieces(int k) { return k >= 10 && k <= 33 ? ((k + 20) % 6 < 2 ? 2 : 1) : 0; }
constexpr int p5_ops(int k) { return (k < 54 - P5F0 ? 1 : 0) + p5_pieces(k); }
constexpr int p5_wait_frag(int s) { return s < P5F0 ? (P5F0 - 1 - s) + vm_sum(p5_ops, 0, s - 1) : p5_pieces(s - P5F0) + vm_sum(p5_ops, s - P5F0 + 1, s - 1); }
constexpr int p5_wait_group(int g) { return vm_sum(p5_ops, 6 * g - 14, 6 * g - 3); }
constexpr int P3F0 = 6;
constexpr int p3_side(int k) { return 0; }
constexpr int p3_ops(int k) { return (k < 20 - P3F0 ? 1 : 0) + p3_side(k); }
constexpr int p3_wait_frag(int s) { return s < P3F0 ? (P3F0 - 1 - s) + vm_sum(p3_ops, 0, s - 1) : p3_side(s - P3F0) + vm_sum(p3_ops, s - P3F0 + 1, s - 1); }

template <int I, int N, class Fn> DI void static_for(Fn&& f) { if constexpr (I < N) { f(std::integral_constant<int, I>{}); static_for<I + 1, N>(f); } }

__global__ __launch_bounds__(256, 1) void adaln_block_kernel(const char* __restrict__ x, const char* __restrict__ wqkv, const char* __restrict__ wo,
                                                             const char* __restrict__ wg, const char* __restrict__ wu, const char* __restrict__ wd,
                                                             const char* __restrict__ cvec, const char* __restrict__ wmod, char* out, char* ws) {
  __shared__ __attribute__((aligned(16))) char smem[LDS_BYTES];
  const unsigned L0 = (unsigned)(size_t)(LAS char*)smem;
  const int tid = threadIdx.x, lane = tid & 63;
  const int wave = __builtin_amdgcn_readfirstlane(tid >> 6);
  const int bid = blockIdx.x, xcd = bid & 7, ii = bid >> 3;
  const int mbL = 2 * xcd + (ii >> 4), jL = ii & 15;
  const int mb = 4 * (xcd >> 1) + (ii >> 3), j = 8 * (xcd & 1) + (ii & 7);
  const int rW = mb & 3, rL = 0, rD = 2 * (mbL & 1) + (jL & 1);
  const int soW = (5 * rW) >> 1;
  const int goL = rD == 3 ? 7 : 2 * rD;
  const unsigned tag = *(const GAS unsigned*)(ws + WS_EPOCH) + 1u;
  const rsrc_t rx = mkrsrc(x, S_ * D_ * 2), rws = mkrsrc(ws, WS_USED);
  STAMP_INIT();
  STAMP(0);

  const rsrc_t rwq = mkrsrc(wqkv, D_ * 3 * D_ * 2);
  unsigned wqo[4];
  auto rot10 = [&](int so, int st) { const int g = st + so; return g >= 10 ? g - 10 : g; };
  const int so1 = (int)sopaque(soW);
  auto wq_piece = [&](int st, int i) { dma16(rwq, wqo[i], rot10(so1, st) * 64 * 3840, L0 + L1_W + (st * 15 + min(4 * wave + i, 14)) * 1024); };
  auto fo40 = [&](const Rot& R, int ks) { return R.b[ks < 12 ? 0 : ks < 20 ? 1 : ks < 32 ? 2 : 3] + ks * 1024; };
  auto wq_issue = [&](int st) {
#pragma unroll
    for (int i = 0; i < 4; ++i) wq_piece(st, i);
  };
  {
    const int c32 = lane & 31, tok = 128 * mbL + 8 * jL + 2 * wave + (lane >> 5);
    u32x4 xv[3], c4 = {}; unsigned c1 = 0;
    ald16(xv[0], rx, tok * 1280 + c32 * 16, 0);
    ald16(xv[1], rx, tok * 1280 + c32 * 16 + 512, 0);
    ald16(xv[2], rx, c32 < 16 ? tok * 1280 + c32 * 16 + 1024 : 0x40000000u, 0);
    if (wave == 3) {
      u64 pc; int len;
      asm volatile("s_getpc_b64 %0\n.Ladaln_code_begin:\n\ts_mov_b32 %1, .Ladaln_code_end-.Ladaln_code_begin" : "=s"(pc), "=s"(len));
      len = len > 0 && len < (1 << 18) ? len : 0;
      touch(mkrsrc((const void*)pc, len), (ii * 40 + min(lane, 39)) * 64, 0, L0 + L_DUMMY);
    }
    const int munit = ii < 10 ? 10 * xcd + ii : 80 + 20 * xcd + (ii - 10);
    const bool mjob = wave == 0 && ii < 30;
    if (mjob) {
      const rsrc_t rc = mkrsrc(cvec, D_ * 2);
      ald16(c4, rc, 20 * lane, 0); ald4(c1, rc, 20 * lane + 16, 0);
    }
#pragma unroll
    for (int i = 0; i < 4; ++i) {
      const int idx = 64 * min(4 * wave + i, 14) + lane, rq = idx / 60, rem = idx - 60 * rq;
      int q, ch;
      if (rem < 48) { q = (rem >> 2) & 3; ch = 4 * (rem >> 4) + (rem & 3); } else { const int r2 = rem - 48; q = r2 / 3; ch = 12 + r2 - 3 * q; }
      const int seg = ch / 5;
      wqo[i] = (4 * rq + q) * 3840 + seg * 1280 + 80 * j + (ch - 5 * seg) * 16;
    }
    if (mjob) {
      gemv16_issue<20>(mkrsrc(wmod, D_ * 6 * D_ * 2), 7680, 32 * munit, L0 + L0_JW, lane);
      vmwait<20>();
    } else {
      vmwait<0>();
    }
    asm volatile("" : "+v"(xv[0]), "+v"(xv[1]), "+v"(xv[2]), "+v"(c4), "+v"(c1));
    STAMPN(48);
    f32x2 s2 = {0.f, 0.f};
#pragma unroll
    for (int i = 0; i < 3; ++i)
#pragma unroll
      for (int e = 0; e < 4; ++e) s2 += unpk(xv[i][e]);
    const float mean = xsum16(sum16(s2.x + s2.y)) * (1.0f / D_);
    f32x2 q2 = {0.f, 0.f}, q3 = {0.f, 0.f};
#pragma unroll
    for (int e = 0; e < 4; ++e) {
      const f32x2 d0 = unpk(xv[0][e]) - mean, d1 = unpk(xv[1][e]) - mean, d2 = unpk(xv[2][e]) - mean;
      q2 += d0 * d0; q2 += d1 * d1; q3 += d2 * d2;
    }
    const float var = xsum16(sum16(q2.x + q2.y + (c32 < 16 ? q3.x + q3.y : 0.f))) * (1.0f / D_);
    const float mq = bf16r(mean), rden = rden_of(var);
#pragma unroll
    for (int i = 0; i < 3; ++i) {
      u32x4 o;
#pragma unroll
      for (int e = 0; e < 4; ++e) { const f32x2 d = bf16r2(bf16r2(unpk(xv[i][e]) - mq) * rden); o[e] = pkh(d.x, d.y); }
      const int slot = c32 + 32 * i;
      if (i < 2 || c32 < 16) lds_st4(L0 + L0_TB + slot * 128 + (((2 * wave + (lane >> 5)) ^ (slot & 7)) * 16), o);
    }
    STAMPN(49);
    BAR();
    STAMPN(50);
#pragma unroll
    for (int u = 0; u < 3; ++u) {
      const int i = 256 * u + tid, slot = i >> 3, tk = (i & 7) ^ (slot & 7);
      const unsigned voff = (u < 2 || tid < 128) ? WS_XN + (4 * mbL + (jL >> 2)) * T32_D + slot * 512 + (8 * (jL & 3) + tk) * 16 : 0x40000000u;
      st16_sc1(lds_u4(L0 + L0_TB + min(i, 639) * 16), rws, voff, 0);
    }
    STAMPN(51);
    wq_issue(0);
    vmwait<4>();
    if (lane == 0) st_u64(ws, flagoff(FL_XN, mbL, 4 * jL + wave), flagval(tag));
    STAMP(1);
    if (mjob) {
      const unsigned cc[5] = {c4[0], c4[1], c4[2], c4[3], c1};
#pragma unroll
      for (int r = 0; r < 5; ++r) lds_st1(L0 + L0_SC + 20 * lane + 4 * r, pk2(siluf(blo(cc[r])), siluf(bhi(cc[r]))));
      asm volatile("s_waitcnt lgkmcnt(0)" ::: "memory");
      const f32x4 d = gemv16_run<20>(L0 + L0_JW, L0 + L0_SC, 0, lane);
      const unsigned pv = (lane & 1) ? pk2(d[2], d[3]) : pk2(d[0], d[1]);
      if ((lane & 15) < 2) st_u64(ws, WS_GRAN + (8 * munit + 2 * (lane >> 4) + (lane & 1)) * 8, ((u64)tag << 32) | pv);
    }
    STAMPN(52);
    wq_issue(1);
    STAMPN(53);
  }
  if (wave == 0) wait_flags(ws, flagoff(FL_XN, mb, lane), true, tag);
  BAR_RAW();
  STAMP(17);
  u32x4 xf[40];
  const unsigned xfo = WS_XN + (4 * mb + wave) * T32_D + 16 * lane;
  const Rot R1 = mkrot(rW, 4 * soW, 40, 1024);
  {
    const int g0 = 160 * wave + lane;
    u32x2 ga[3];
#pragma unroll
    for (int ks = 0; ks < P1F0; ++ks) ald16_sc1(xf[ks], rws, xfo, fo40(R1, ks));
#pragma unroll
    for (int k = 0; k < 3; ++k) ald8_sc1(ga[k], rws, WS_GRAN + (k < 2 || lane < 32 ? g0 + 64 * k : g0) * 8, 0);
    wq_issue(2); wq_issue(3);
    vmwait<8>();
    asm volatile("" : "+v"(ga[0]), "+v"(ga[1]), "+v"(ga[2]));
    unsigned gv[3] = {ga[0][0], ga[1][0], ga[2][0]};
    if (!__all(ga[0][1] == tag && ga[1][1] == tag && ga[2][1] == tag))
      sweep<3>(ws, [&](int k) { return WS_GRAN + (g0 + 64 * k) * 8; }, [&](int k) { return k < 2 || lane < 32; }, tag, gv);
    STAMP(18);
#pragma unroll
    for (int k = 0; k < 3; ++k) if (k < 2 || lane < 32) {
      const int g = g0 + 64 * k;
      f32x2 v = unpk(gv[k]);
      if (g >= 320) v = bf16r2(v + 1.0f);
      const int gp = g >= 320 ? g - 320 : g;
      int kp = (gp >> 3) - 4 * soW; kp += kp < 0 ? 40 : 0;
      lds_st1(L0 + (g >= 320 ? L1_VS : L1_VA) + kp * 32 + (gp & 7) * 4, pkh(v.x, v.y));
    }
  }
  BAR();
  STAMP(2);

  const int jidx = 32 * (xcd >> 1) + ii, jper = (xcd & 1) ? 52 : 56;
  const bool ujob = jidx < 2 * jper;
  const int jmat = jidx >= jper, junit = ((xcd & 1) ? 56 : 0) + jidx - jmat * jper;
  auto jgi = [&](int k) { const int i = 64 * k + lane; return i < 80 ? 1280 + 80 * wave + i : i < 160 ? 960 + 80 * wave + i - 80 : 1280; };
  const rsrc_t rjw = mkrsrc(jmat ? wu : wg, D_ * F_ * 2);
  const unsigned jvo = (8 * ((lane >> 3) & 3) + 4 * (lane >> 5) + ((lane >> 1) & 3)) * 3456 + 16 * (lane & 1);
  const unsigned jso = sopaque(32 * (ujob ? junit : 0) + wave * 160 * 3456);
  u32x2 jg[3];
  f32x16 acc1[4];
#pragma unroll
  for (int b = 0; b < 4; ++b) acc1[b] = (f32x16){};
  {
    const int h = lane >> 5;
    const unsigned wb = opaque(L0 + L1_W + 2 * h * 960 + ((lane & 15) >> 2) * 64 + ((lane >> 4) & 1) * 32 + (lane & 3) * 8);
    const unsigned wb3 = opaque(L0 + L1_W + 2 * h * 960 + 768 + ((lane & 15) >> 2) * 48 + ((lane >> 4) & 1) * 32 + (lane & 3) * 8);
    const unsigned wbh = opaque(wb + 61440), wb3h = opaque(wb3 + 61440), wbg = opaque(wb + 122880), wb3g = opaque(wb3 + 122880);
    const unsigned vb = opaque(L0 + L1_VS + h * 16);
    s16x8 w[3][4]; u32x4 hf[2], vs[2], va[2]; float ft[2][8];
    auto readw = [&](int ks, int b) {
      const int kl = ks % 16; const unsigned base = ks < 16 ? (b < 3 ? wb : wb3) : ks < 32 ? (b < 3 ? wbh : wb3h) : (b < 3 ? wbg : wb3g);
      const unsigned a = base + (b < 3 ? b * 256 : 0) + kl * 3840; return trfrag(a, a + 960); };
    auto reads = [&](int ks) { vs[ks & 1] = lds_u4(vb + ks * 32); };
    auto reada = [&](int ks) { va[ks & 1] = lds_u4(vb + 1280 + ks * 32); };
    auto fstage = [&](int ks, int e) {
      ft[ks & 1][2 * e] = fmix_lo(xf[ks][e], vs[ks & 1][e], va[ks & 1][e]);
      ft[ks & 1][2 * e + 1] = fmix_hi(xf[ks][e], vs[ks & 1][e], va[ks & 1][e]); };
    auto cstage = [&](int ks, int e) { hf[ks & 1][e] = pk2(ft[ks & 1][2 * e], ft[ks & 1][2 * e + 1]); };
#pragma unroll
    for (int b = 0; b < 4; ++b) { w[0][b] = readw(0, b); w[1][b] = readw(1, b); }
    reads(0); reada(0); reads(1); reada(1);
    vmwait<p1_wait_frag(0)>(xf[0]);
#pragma unroll
    for (int e = 0; e < 4; ++e) fstage(0, e);
#pragma unroll
    for (int e = 0; e < 4; ++e) cstage(0, e);
    vmwait<p1_wait_frag(1)>(xf[1]);
#pragma unroll
    for (int e = 0; e < 4; ++e) fstage(1, e);
    reads(2); reada(2);
    SB();
    static_for<0, 40>([&](auto KS) {
      constexpr int ks = KS, c = ks & 1, wc = ks % 3, wn = (ks + 2) % 3;
      constexpr bool needf = ks + 2 < 40, needs = (ks & 3) == 2 && (ks + 2) / 4 >= 2 && ks + 2 < 40;
      constexpr int nf = needf ? p1_wait_frag(ks + 2) : 63, ns = needs ? p1_wait_stage((ks + 2) / 4) : 63;
      if constexpr (needs) { vmwait<(nf < ns ? nf : ns)>(); BAR_RAW(); if constexpr (ks >= 14) STAMPN(24 + (ks - 14) / 4); }
      if constexpr (needf) vmwait<nf>(xf[ks + 2]);
      SB();
      acc1[0] = mfma32(w[wc][0], hf[c], acc1[0]); SB();
      if constexpr (ks + 2 < 40) w[wn][0] = readw(ks + 2, 0);
      if constexpr (ks + 1 < 40) { cstage(ks + 1, 0); cstage(ks + 1, 1); }
      if constexpr (ks + 3 < 40) reads(ks + 3);
      SB();
      acc1[1] = mfma32(w[wc][1], hf[c], acc1[1]); SB();
      if constexpr (ks + P1F0 < 40) ald16_sc1(xf[ks + P1F0], rws, xfo, fo40(R1, ks + P1F0));
      if constexpr (ks + 2 < 40) w[wn][1] = readw(ks + 2, 1);
      if constexpr (ks + 1 < 40) { cstage(ks + 1, 2); cstage(ks + 1, 3); }
      if constexpr (ks + 3 < 40) reada(ks + 3);
      SB();
      acc1[2] = mfma32(w[wc][2], hf[c], acc1[2]); SB();
      if constexpr (ks + 2 < 40) w[wn][2] = readw(ks + 2, 2);
      if constexpr (ks + 2 < 40) { fstage(ks + 2, 0); fstage(ks + 2, 1); }
      SB();
      acc1[3] = mfma32(w[wc][3], hf[c], acc1[3]); SB();
      if constexpr (ks < 24) wq_piece(ks / 4 + 4, ks & 3);
      if constexpr (ks >= 30 && ks < 35) dma16(rjw, jvo, jso + (ks - 30) * 32 * 3456, L0 + L2_JW + wave * 5120 + (ks - 30) * 1024);
      if constexpr (ks >= 35 && ks < 38) ald8_sc1(jg[ks - 35], rws, WS_GRAN + jgi(ks - 35) * 8, 0);
      if constexpr (ks + 2 < 40) w[wn][3] = readw(ks + 2, 3);
      if constexpr (ks + 2 < 40) { fstage(ks + 2, 2); fstage(ks + 2, 3); }
      SB();
    });
  }
  STAMP(3);

  u32x4 qa0, qa1, qb0, qb1;
  {
    const int h = lane >> 5, t = lane & 31;
    u32x2 P[15];
#pragma unroll
    for (int i = 0; i < 15; ++i) { const f32x16& a = acc1[i >> 2]; const int r = 4 * (i & 3); P[i] = (u32x2){pk2(a[r], a[r + 1]), pk2(a[r + 2], a[r + 3])}; }
    const u32x2 one = {0x00003f80u, 0u}, zero = {0u, 0u};
    auto cat = [](u32x2 a, u32x2 b) { return (u32x4){a[0], a[1], b[0], b[1]}; };
    auto sel = [&](u32x2 a1, u32x2 a0) { return (u32x2){h ? a1[0] : a0[0], h ? a1[1] : a0[1]}; };
    const unsigned row = L0 + L2_KV + (256 + 32 * wave + t) * 48;
    lds_st4(row + 16 * h, cat(P[5], P[6]));
    lds_st4(row + KV_IMG + 16 * (h ^ 1), cat(sel(P[7], P[8]), sel(P[8], P[9])));
    lds_st4(row + h * KV_IMG + 32, cat(sel(P[9], P[7]), zero));
    lds_st4(row + 2 * KV_IMG + 16 * h, cat(P[10], P[11]));
    lds_st4(row + 3 * KV_IMG + 16 * (h ^ 1), cat(sel(P[12], P[13]), sel(P[13], P[14])));
    lds_st4(row + (2 + h) * KV_IMG + 32, cat(sel(P[14], P[12]), one));
    asm volatile("s_waitcnt lgkmcnt(0)" ::: "memory");
#pragma unroll
    for (int u = 0; u < 6; ++u) {
      const int i = 64 * u + lane, a = i / 96, o = (i - 96 * a) * 16;
      const u32x4 v = lds_u4(L0 + L2_KV + a * KV_IMG + (256 + 32 * wave) * 48 + o);
      st16_sc1(v, rws, WS_KV + ((mb * 16 + j) * 4 + a) * 6144 + wave * 1536 + o, 0);
    }
    qa0 = cat(P[0], P[1]);
    qa1 = cat(sel(zero, P[2]), sel(zero, one));
    qb0 = cat(sel(P[2], P[3]), sel(P[3], P[4]));
    qb1 = cat(sel(P[4], zero), sel(one, zero));
    publish(ws, FL_KV, mb, j, wave, lane, tag);
  }
  STAMP(4);
  asm volatile("" : "+v"(jg[0]), "+v"(jg[1]), "+v"(jg[2]));
  {
    unsigned gv[3] = {jg[0][0], jg[1][0], jg[2][0]};
    if (!__all(jg[0][1] == tag && jg[1][1] == tag && jg[2][1] == tag))
      sweep<3>(ws, [&](int k) { return WS_GRAN + jgi(k) * 8; }, [&](int) { return true; }, tag, gv);
#pragma unroll
    for (int k = 0; k < 3; ++k) if (k < 2 || lane < 32) {
      const int i = 64 * k + lane;
      unsigned v = gv[k];
      if (i < 80) { const f32x2 t = unpk(v) + 1.0f; v = pk2(t.x, t.y); }
      lds_st1(L0 + L2_JV + (i < 80 ? 320 * wave + 4 * i : 1280 + 320 * wave + 4 * (i - 80)), v);
    }
    asm volatile("s_waitcnt lgkmcnt(0)" ::: "memory");
    const f32x4 d = gemv16_run<5>(L0 + L2_JW + wave * 5120, L0 + L2_JV + wave * 320, 1, lane);
    lds_st4(L0 + L3_JP + wave * 1024 + lane * 16, __builtin_bit_cast(u32x4, d));
  }

  {
    const int blk = wave >> 1, src = mb - 2 + blk;
    if (src >= 0) {
      wait_flags(ws, flagoff(FL_KV, src, 4 * j + (lane & 3)), lane < 4, tag);
      STAMP(5);
#pragma unroll
      for (int i = 0; i < 12; ++i) {
        const int a = 2 * (wave & 1) + i / 6, sub = i % 6;
        dma16_sc1(rws, 16 * lane, WS_KV + ((src * 16 + j) * 4 + a) * 6144 + sub * 1024, L0 + L2_KV + a * KV_IMG + blk * 6144 + sub * 1024);
      }
    } else {
      STAMP(5);
#pragma unroll
      for (int i = 0; i < 12; ++i) {
        const int a = 2 * (wave & 1) + i / 6, o = (i % 6) * 1024 + 16 * lane;
        lds_st4(L0 + L2_KV + a * KV_IMG + blk * 6144 + o, (u32x4){0u, 0u, ((o % 48) == 32 && a < 2) ? 0x0000f149u : 0u, 0u});
      }
    }
  }
  const int th = jL & 1, nt = jL >> 1;
  const rsrc_t rwo = mkrsrc(wo, D_ * D_ * 2);
  unsigned woo[7], wol[7];
#pragma unroll
  for (int i = 0; i < 7; ++i) {
    const int pi = min(wave + 4 * i, 24), kc = pi / 5, pc = pi - 5 * kc;
    auto rowmap = [&](int g, int t, int q) { const int k = 32 * kc + 8 * g + 4 * t + q, hp = k / 40, r = k - 40 * hp; return 40 * hp + (r < 20 ? r : r < 24 ? r + 16 : r - 4); };
    woo[i] = opaque(w80_voff(pc, lane, nt, rowmap));
    wol[i] = pi * 1024;
  }
  auto wo_piece = [&](int gam, int i) { dma16(rwo, woo[i], ((gam + rL) & 3) * 160 * 1280, L0 + (gam < 3 ? L3_W0 + gam * 25600 : L3_W1) + wol[i]); };
  vmwait<0>();
  BAR();
  STAMP(6);
  if (ujob && wave == 1) {
    f32x4 d = lds_f4(L0 + L3_JP + lane * 16);
#pragma unroll
    for (int w = 1; w < 4; ++w) d += lds_f4(L0 + L3_JP + w * 1024 + lane * 16);
    if ((lane & 15) < 2) {
#pragma unroll
      for (int r = 0; r < 4; ++r) st_u64(ws, WS_UV + ((jmat * 108 + junit) * 32 + (lane & 15) * 16 + 4 * (lane >> 4) + r) * 8, ((u64)tag << 32) | __float_as_uint(d[r]));
    }
  }

  {
    const rsrc_t rgt = mkrsrc(wave < 2 ? wg : wu, D_ * F_ * 2);
    const rsrc_t rdt = mkrsrc(wd, F_ * D_ * 2);
    const unsigned tdo = (135 * bid + min(64 * wave + lane, 134)) * 64;
    unsigned xro[3];
#pragma unroll
    for (int i = 0; i < 3; ++i) { const int idx = 64 * min(3 * wave + i, 9) + lane, row = idx / 10; xro[i] = (128 * mbL + 64 * th + row) * 1280 + 160 * nt + (idx - 10 * row) * 16; }
    const unsigned tgo = (20 * ii + 10 * (wave & 1) + (lane >> 5)) * 3456 + (xcd & 1) * 1792 + 64 * min(lane & 31, (xcd & 1) ? 25 : 27);
    const int h = lane >> 5, t = lane & 31;
    f32x16 mfirst, mdiag;
#pragma unroll
    for (int r = 0; r < 16; ++r) { const int key = (r & 3) + 8 * (r >> 2) + 4 * h; mfirst[r] = key > t ? 0.f : NEGBIG; mdiag[r] = key > t ? NEGBIG : 0.f; }
    const unsigned krow = L0 + L2_KV + (32 * wave + t) * 48;
    const unsigned ka0 = opaque(krow + 16 * h), ka1 = opaque(krow + 32), kb0 = opaque(krow + KV_IMG + 16 * (h ^ 1)), kb1 = opaque(krow + KV_IMG + 32);
    const unsigned vrow = L0 + L2_KV + 2 * KV_IMG + (32 * wave + 4 * h + ((lane & 15) >> 2)) * 48 + ((lane >> 4) & 1) * 32 + (lane & 3) * 8;
    const unsigned vra = opaque(vrow), vrb = opaque(vrow + KV_IMG);
    f32x16 sa[9], sb[9];
    auto qk = [&](f32x16& s, int kb, u32x4 a0, u32x4 a1, u32x4 q0, u32x4 q1) {
      if (kb == 0) smfma_c(s, a0, q0, mfirst); else if (kb == 8) smfma_c(s, a0, q0, mdiag); else smfma_0(s, a0, q0);
      smfma_a(s, a1, q1); };
    float e[8];
    auto pfma = [&](const f32x16& s, int half, float mc) {
#pragma unroll
      for (int i = 0; i < 8; ++i) e[i] = fmaf(s[8 * half + i], SCORE_C, mc); };
    auto pexp = [&]() {
#pragma unroll
      for (int i = 0; i < 8; ++i) e[i] = exp2_(e[i]); };
    auto pcvt = [&]() { return (u32x4){pk2(e[0], e[1]), pk2(e[2], e[3]), pk2(e[4], e[5]), pk2(e[6], e[7])}; };
    auto qk0 = [&](f32x16& s, int kb, u32x4 a0, u32x4 q0) { if (kb == 0) smfma_c(s, a0, q0, mfirst); else if (kb == 8) smfma_c(s, a0, q0, mdiag); else smfma_0(s, a0, q0); };
    auto maxhalf = [&](float (&m)[4], const f32x16& s, int half) {
#pragma unroll
      for (int r = 8 * half; r < 8 * half + 8; r += 2) m[(r >> 1) & 3] = fmaxf(fmaxf(m[(r >> 1) & 3], s[r]), s[r + 1]); };
    auto vfrag = [&](unsigned base, int kb, int half) { const unsigned a = base + (32 * kb + 16 * half) * 48; return trfrag(a, a + 8 * 48); };
    auto maxblk = [&](float (&m)[4], const f32x16& s) {
#pragma unroll
      for (int r = 0; r < 16; r += 2) m[(r >> 1) & 3] = fmaxf(fmaxf(m[(r >> 1) & 3], s[r]), s[r + 1]); };
    u32x4 k0 = lds_u4(ka0), k1 = lds_u4(ka1);
    float mx[4] = {NEGBIG, NEGBIG, NEGBIG, NEGBIG};
#pragma unroll
    for (int kb = 0; kb < 9; ++kb) {
      u32x4 n0, n1;
      SB();
      qk0(sa[kb], kb, k0, qa0);
      SB();
      n0 = kb < 8 ? lds_u4(ka0 + (kb + 1) * 1536) : lds_u4(kb0);
      if (kb == 2) asm volatile("s_nop 3" : "+v"(sa[0]));
      if (kb >= 2) maxhalf(mx, sa[kb - 2], 0);
      SB();
      smfma_a(sa[kb], k1, qa1);
      SB();
      n1 = kb < 8 ? lds_u4(ka1 + (kb + 1) * 1536) : lds_u4(kb1);
      if (kb >= 2) { maxhalf(mx, sa[kb - 2], 1); wo_piece(2, kb - 2); }
      k0 = n0; k1 = n1;
    }
    SB();
    asm volatile("s_nop 7" : "+v"(sa[7]));
    maxblk(mx, sa[7]);
    sfence(sa[8]);
    maxblk(mx, sa[8]);
    const float mca = -SCORE_C * xmax32(fmaxf(fmaxf(mx[0], mx[1]), fmaxf(mx[2], mx[3])));
    f32x16 oa = {}, ob = {};
    s16x8 v0 = vfrag(vra, 0, 0), v1 = vfrag(vra, 0, 1);
#pragma unroll
    for (int r = 0; r < 4; ++r) mx[r] = NEGBIG;
    SB();
#pragma unroll
    for (int kb = 0; kb < 9; ++kb) {
      qk0(sb[kb], kb, k0, qb0);
      SB();
      if (kb < 8) k0 = lds_u4(kb0 + (kb + 1) * 1536);
      pfma(sa[kb], 0, mca); pexp();
      SB();
      smfma_a(sb[kb], k1, qb1);
      SB();
      if (kb < 8) k1 = lds_u4(kb1 + (kb + 1) * 1536);
      const u32x4 p0 = pcvt();
      pfma(sa[kb], 1, mca);
      SB();
      oa = mfma32(v0, p0, oa);
      SB();
      v0 = kb < 8 ? vfrag(vra, kb + 1, 0) : vfrag(vrb, 0, 0);
      pexp();
      const u32x4 p1 = pcvt();
      if (kb >= 1) maxhalf(mx, sb[kb - 1], 0);
      SB();
      oa = mfma32(v1, p1, oa);
      SB();
      v1 = kb < 8 ? vfrag(vra, kb + 1, 1) : vfrag(vrb, 0, 1);
      if (kb >= 1) maxhalf(mx, sb[kb - 1], 1);
      if (kb < 7) wo_piece(0, kb);
      if (kb < 5) touch(rgt, tgo, kb * 2 * 3456, L0 + L_DUMMY);
      if (kb == 5) touch(rdt, tdo, 0, L0 + L_DUMMY);
      if (kb >= 6) dma16(rx, xro[kb >= 6 ? kb - 6 : 0], 0, L0 + L3_XR + min(3 * wave + kb - 6, 9) * 1024);
      SB();
    }
    sfence(sb[8]);
    maxblk(mx, sb[8]);
    const float mcb = -SCORE_C * xmax32(fmaxf(fmaxf(mx[0], mx[1]), fmaxf(mx[2], mx[3])));
    SB();
    pfma(sb[0], 0, mcb); pexp();
    SB();
#pragma unroll
    for (int kb = 0; kb < 9; ++kb) {
      const u32x4 p0 = pcvt();
      pfma(sb[kb], 1, mcb);
      SB();
      ob = mfma32(v0, p0, ob);
      SB();
      if (kb < 8) v0 = vfrag(vrb, kb + 1, 0);
      pexp();
      const u32x4 p1 = pcvt();
      if (kb < 8) pfma(sb[kb + 1], 0, mcb);
      SB();
      ob = mfma32(v1, p1, ob);
      SB();
      if (kb < 8) { v1 = vfrag(vrb, kb + 1, 1); pexp(); }
      if (kb < 7) wo_piece(1, kb);
      SB();
    }
    const auto la = __builtin_amdgcn_permlane32_swap(__float_as_uint(oa[8]), __float_as_uint(oa[8]), false, false);
    const auto lb = __builtin_amdgcn_permlane32_swap(__float_as_uint(ob[8]), __float_as_uint(ob[8]), false, false);
    const float ia = rcpf_(__uint_as_float(la[1])), ib = rcpf_(__uint_as_float(lb[1]));
    const unsigned obase = WS_O + (4 * mb + wave) * T32_D + 5 * j * 512;
    u32x4 va, vb2, vc;
#pragma unroll
    for (int e = 0; e < 4; ++e) { va[e] = pk2(oa[2 * e] * ia, oa[2 * e + 1] * ia); vb2[e] = pk2(ob[2 * e] * ib, ob[2 * e + 1] * ib); }
    vc = (u32x4){pk2(oa[8] * ia, oa[9] * ia), pk2(oa[10] * ia, oa[11] * ia), pk2(ob[8] * ib, ob[9] * ib), pk2(ob[10] * ib, ob[11] * ib)};
    STAMP(23);
    st16_sc1(va, rws, obase + 16 * lane, 0);
    st16_sc1(vb2, rws, obase + 3 * 512 + 16 * lane, 0);
    st16_sc1(vc, rws, lane < 32 ? obase + 2 * 512 + 16 * lane : 0x40000000u, 0);
    publish(ws, FL_O, mb, j, wave, lane, tag);
  }
  BAR();
  STAMP(7);
  const int t16 = lane & 15, g4 = lane >> 4;
  const int tok3 = 128 * mbL + 64 * th + 16 * wave + t16;
  auto goff = [&](int k) { const int i = min(64 * k + lane, 119), kind = i / 40, r = i - 40 * kind; return WS_GRAN + ((kind == 0 ? 640 : kind == 1 ? 1280 : 1600) + 40 * nt + r) * 8; };
  u32x2 vg2[2];
#pragma unroll
  for (int i = 0; i < 7; ++i) wo_piece(3, i);
#pragma unroll
  for (int k = 0; k < 2; ++k) ald8_sc1(vg2[k], rws, goff(k), 0);
  if (wave == 0) wait_flags(ws, flagoff(FL_O, mbL, lane), true, tag);
  vmwait<0>();
  asm volatile("" : "+v"(vg2[0]), "+v"(vg2[1]));
  if (wave == 2) {
    unsigned gv[2] = {vg2[0][0], vg2[1][0]};
    if (!__all(vg2[0][1] == tag && vg2[1][1] == tag)) sweep<2>(ws, goff, [&](int) { return true; }, tag, gv);
#pragma unroll
    for (int k = 0; k < 2; ++k) if (k == 0 || lane < 56) {
      const int i = 64 * k + lane, kind = i / 40, r = i - 40 * kind;
      const int ct = r >> 3, p = (nt & 1) ? (ct == 0 ? 4 : ct - 1) : ct;
      f32x2 v = unpk(gv[k]);
      if (kind == 1) v = bf16r2(v + 1.0f);
      lds_st2f(L0 + L3_VEC + kind * 320 + p * 64 + (r & 7) * 8, v);
    }
  }
  if (wave == 3 && bid == 0) {
    unsigned fv[16];
    sweep<16>(ws, [&](int k) { return flagoff(FL_KV, k, lane); }, [&](int) { return true; }, tag, fv);
    if (lane == 0) __hip_atomic_store((GAS unsigned*)(ws + WS_EPOCH), tag, __ATOMIC_RELAXED, __HIP_MEMORY_SCOPE_AGENT);
  }
  STAMP(20);
  BAR();
  STAMP(8);

  const unsigned b80_0 = g4 * 1024 + ((lane & 15) >> 2) * 64 + (g4 & 1) * 32 + (lane & 3) * 8;
  const unsigned b80_1 = b80_0 ^ 32u;
  const unsigned b80_2 = 4096 + 8 * lane;
  struct B80 { unsigned a, b, c; };
  auto b80 = [&](unsigned kbase) { return B80{opaque(kbase + b80_0), opaque(kbase + b80_1), opaque(kbase + b80_2)}; };
  auto w80 = [&](const B80& B, int koff, int p) { const unsigned a = (p == 4 ? B.c : ((p & 1) ? B.b : B.a) + (p >> 1) * 256) + koff; return trfrag(a, a + 512); };

  u32x2 x1p[5];
  const int nu = j < 12 ? 7 : 6, u0 = j < 12 ? 7 * j : 84 + 6 * (j - 12), uodd = u0 & 1;
  const rsrc_t rgu = mkrsrc(wave < 2 ? wg : wu, D_ * F_ * 2);
  unsigned guo[7];
  const int so4 = 0;
  auto gu_piece = [&](int st, int i) { dma16(rgu, guo[i], rot10(so4, st) * 64 * 3456, L0 + (st % 5) * L4_STAGE + (wave >> 1) * L4_U + (wave & 1) * 7168 + i * 1024); };
  {
    u32x4 of[20];
    const unsigned ofb = WS_O + (4 * mbL + 2 * th + (wave >> 1)) * T32_D + g4 * 512 + ((wave & 1) * 16 + t16) * 16;
    const Rot R3 = mkrot(rL, 5 * rL, 20, 2048);
    auto fo20 = [&](int s) { return R3.b[s < 5 ? 0 : s < 10 ? 1 : s < 15 ? 2 : 3] + s * 2048; };
#pragma unroll
    for (int s = 0; s < P3F0; ++s) ald16_sc1(of[s], rws, ofb, fo20(s));
#pragma unroll
    for (int i = 0; i < 7; ++i) {
      const int isup = wave >> 1, idx = 64 * (7 * (wave & 1) + i) + lane, rq = idx / 56, rem = idx - 56 * rq;
      int q, p, hf;
      if (rem < 48) { q = (rem >> 2) & 3; const int c = (rem & 3) ^ (isup ? 2 : 0); p = 2 * (rem >> 4) + (c >> 1); hf = c & 1; } else { q = (rem - 48) >> 1; p = 6; hf = rem & 1; }
      guo[i] = opaque((4 * rq + q) * 3456 + 32 * (u0 + unit_of(p, uodd, nu)) + 16 * hf);
    }

    f32x4 acc[5];
#pragma unroll
    for (int p = 0; p < 5; ++p) acc[p] = (f32x4){};
    const B80 B0 = b80(L0 + L3_W0), B1 = b80(L0 + L3_W0 + 10 * 5120), B2 = b80(L0 + L3_W1);
    auto rd3 = [&](int s, int p) { return s < 10 ? w80(B0, s * 5120, p) : s < L3_NPRE ? w80(B1, (s - 10) * 5120, p) : w80(B2, (s - L3_NPRE) * 5120, p); };
    s16x8 w[3][5];
#pragma unroll
    for (int p = 0; p < 5; ++p) { w[0][p] = rd3(0, p); w[1][p] = rd3(1, p); }
    SB();
    static_for<0, 20>([&](auto SS) {
      constexpr int s = SS;
      vmwait<p3_wait_frag(s)>(of[s]);
      SB();
#pragma unroll
      for (int p = 0; p < 5; ++p) {
        acc[p] = mfma16(w[s % 3][p], of[s], acc[p]);
        SB();
        if (p == 0 && s + P3F0 < 20) ald16_sc1(of[s + P3F0 < 20 ? s + P3F0 : 0], rws, ofb, fo20(s + P3F0));
        if (s + 2 < 20) w[(s + 2) % 3][p] = rd3(s + 2, p);
        SB();
      }
    });
    BAR();
    STAMP(9);
    f32x2 xv[5][2]; f32x2 s2 = {0.f, 0.f};
    u32x2 yp[5];
    const unsigned vecb = opaque(L0 + L3_VEC + g4 * 16), xrb = opaque(L0 + L3_XR + (16 * wave + t16) * 160 + g4 * 8);
    u32x2 xres[5];
#pragma unroll
    for (int p = 0; p < 5; ++p) xres[p] = *(LAS u32x2*)(size_t)(xrb + 32 * ct_of(p, nt & 1));
#pragma unroll
    for (int p = 0; p < 5; ++p) {
      const f32x4 ga = lds_f4(vecb + p * 64), sb1 = lds_f4(vecb + 320 + p * 64);
#pragma unroll
      for (int r = 0; r < 2; ++r) {
        const f32x2 at = bf16r2((f32x2){acc[p][2 * r], acc[p][2 * r + 1]});
        const f32x2 xs = unpk(xres[p][r]) + bf16r2(at * (f32x2){ga[2 * r], ga[2 * r + 1]});
        x1p[p][r] = pk2(xs.x, xs.y);
        xv[p][r] = unpk(x1p[p][r]);
        s2 += xv[p][r];
        const f32x2 y = xv[p][r] * (f32x2){sb1[2 * r], sb1[2 * r + 1]};
        yp[p][r] = pk2(y.x, y.y);
      }
    }
    const unsigned yb = WS_Y + (4 * mbL + 2 * th + (wave >> 1)) * T32_D + 10 * nt * 512 + ((wave & 1) * 16 + t16) * 16;
#pragma unroll
    for (int pp = 0; pp < 3; ++pp) {
      const int pa = 2 * pp, pb = pp < 2 ? 2 * pp + 1 : 4;
      const auto r0 = __builtin_amdgcn_permlane16_swap(yp[pa][0], yp[pb][0], false, false), r1 = __builtin_amdgcn_permlane16_swap(yp[pa][1], yp[pb][1], false, false);
      const int ct = (g4 & 1) ? ct_of(pb, nt & 1) : ct_of(pa, nt & 1);
      st16_sc1((u32x4){r0[0], r1[0], r0[1], r1[1]}, rws, (pp < 2 || !(g4 & 1)) ? yb + (2 * ct + (g4 >> 1)) * 512 : 0x40000000u, 0);
    }
    const float mt = xsum32(xsum16(s2.x + s2.y)) * (1.0f / 80);
    f32x2 m2 = {0.f, 0.f};
#pragma unroll
    for (int p = 0; p < 5; ++p)
#pragma unroll
      for (int r = 0; r < 2; ++r) { const f32x2 d = xv[p][r] - mt; m2 += d * d; }
    const float m2s = xsum32(xsum16(m2.x + m2.y));
    if (lane < 16) st_u64(ws, WS_STAT + (((mbL * 2 + th) * 8 + nt) * 64 + 16 * wave + t16) * 8, ((u64)__float_as_uint(m2s) << 32) | __float_as_uint(mt));
#pragma unroll
    for (int i = 0; i < 7; ++i) gu_piece(0, i);
    vmwait<7>();
    if (lane == 0) st_u64(ws, flagoff(FL_Y, mbL, 4 * jL + wave), flagval(tag));
  }
  BAR_RAW();
  STAMP(10);

  for (int st = 1; st < 4; ++st)
#pragma unroll
    for (int i = 0; i < 7; ++i) gu_piece(st, i);
  STAMP(19);
  if (wave == 0) wait_flags(ws, flagoff(FL_Y, mb, lane), true, tag);
  if (wave == 1) {
    unsigned uv[7];
    sweep<7>(ws, [&](int p) { return WS_UV + (((lane >> 5) * 108 + u0 + unit_of(p, uodd, nu)) * 32 + (lane & 31)) * 8; }, [&](int) { return true; }, tag, uv);
#pragma unroll
    for (int p = 0; p < 7; ++p) lds_st1(L0 + L4_UV + (p * 64 + lane) * 4, uv[p]);
  }
  STAMP(21);
  vmwait<0>();
  BAR();
  STAMP(11);
  u32x2 stv[4];
#pragma unroll
  for (int i = 0; i < 4; ++i) { const int T = 32 * wave + (lane & 31); stv[i] = ld8_sc1(rws, WS_STAT + (((mb * 2 + (T >> 6)) * 8 + 4 * (lane >> 5) + i) * 64 + (T & 63)) * 8, 0); }
  MEMFENCE();
  u32x4 yf[40];
  const unsigned yfo = WS_Y + (4 * mb + wave) * T32_D + 16 * lane;
  const Rot R4 = mkrot(0, 0, 40, 1024);
#pragma unroll
  for (int ks = 0; ks < P4F0; ++ks) ald16_sc1(yf[ks], rws, yfo, fo40(R4, ks));
  const rsrc_t rwd = mkrsrc(wd, F_ * D_ * 2);
  unsigned wdg[8];
  {
    auto rm = [](int g, int t, int q) { return 16 * (g >> 1) + 8 * t + 4 * (g & 1) + q; };
    const unsigned b0 = w80_voff(0, lane, nt, rm), b1 = w80_voff(1, lane, nt, rm), b4 = w80_voff(4, lane, nt, rm);
#pragma unroll
    for (int i = 0; i < 8; ++i) {
      const int pc = min(8 * wave + i, 29), kl = pc / 5, pp = pc - 5 * kl;
      wdg[i] = opaque((pp == 4 ? b4 : (pp & 1) ? b1 : b0) + (kl * 32 + (pp == 4 ? 0 : 16 * (pp >> 1))) * 1280);
    }
  }
  auto wd_piece = [&](int G, int i) { const int g = G + goL; dma16(rwd, wdg[i], (g >= 9 ? g - 9 : g) * 6 * 32 * 1280, L0 + (G % 5) * 30720 + min(8 * wave + i, 29) * 1024); };
  const Rot R5 = mkrot(rD, 6 * goL, 54, 2048);
  auto fo54 = [&](int s) { return R5.b[s < 12 ? 0 : s < 30 ? 1 : s < 42 ? 2 : 3] + s * 2048; };
  f32x16 acc4[7];
#pragma unroll
  for (int b = 0; b < 7; ++b) acc4[b] = (f32x16){};
  {
    const int h = lane >> 5, blk = (lane >> 4) & 1, q = (lane & 15) >> 2;
    const unsigned c0 = L0 + blk * L4_U + 2 * h * 896 + q * 64 + blk * 32 + (lane & 3) * 8;
    const unsigned c2 = L0 + blk * L4_U + 2 * h * 896 + 768 + q * 32 + (lane & 3) * 8;
    unsigned cb0[5], cb1[5], cb2[5];
#pragma unroll
    for (int sl = 0; sl < 5; ++sl) { cb0[sl] = opaque(c0 + sl * L4_STAGE); cb1[sl] = opaque((c0 ^ 32u) + sl * L4_STAGE); cb2[sl] = opaque(c2 + sl * L4_STAGE); }
    auto readw = [&](int ks, int p) {
      const int sl = (ks >> 2) % 5;
      const unsigned a = (p == 6 ? cb2[sl] : ((p & 1) ? cb1[sl] : cb0[sl]) + (p >> 1) * 256) + (ks & 3) * 4 * 896;
      return trfrag(a, a + 896); };
    s16x8 w[3][7];
#pragma unroll
    for (int p = 0; p < 7; ++p) { w[0][p] = readw(0, p); w[1][p] = readw(1, p); }
    SB();
    static_for<0, 40>([&](auto KS) {
      constexpr int ks = KS, c = ks % 3, n = (ks + 2) % 3;
      if constexpr ((ks & 3) == 2) {
        constexpr int st = (ks + 2) / 4;
        if constexpr (st <= 9) STAMPN(30 + st - 1);
        if constexpr (st >= 4 && st <= 9) vmwait<p4_wait_stage(st)>();
        BAR_RAW();
      }
      vmwait<p4_wait_frag(ks)>(yf[ks]);
      SB();
#pragma unroll
      for (int p = 0; p < 7; ++p) {
        acc4[p] = mfma32(w[c][p], yf[ks], acc4[p]);
        SB();
        if (p == 1 && ks + P4F0 < 40) ald16_sc1(yf[ks + P4F0 < 40 ? ks + P4F0 : 0], rws, yfo, fo40(R4, ks + P4F0));
        if (p == 3 && p4_pieces(ks) > 0) gu_piece((ks + 14) / 4, 2 * ((ks + 14) % 4));
        if (p == 5 && p4_pieces(ks) > 1) gu_piece((ks + 14) / 4, 2 * ((ks + 14) % 4) + 1);
        if (p == 3 && p4_touch(ks) > 0) wd_piece(p4_wd_before(ks) / 8, p4_wd_before(ks) % 8);
        if (p == 5 && p4_touch(ks) > 1) wd_piece((p4_wd_before(ks) + 1) / 8, (p4_wd_before(ks) + 1) % 8);
        if (ks + 2 < 40) w[n][p] = readw(ks + 2, p);
        SB();
      }
    });
  }
  STAMP(12);
  {
    const int h = lane >> 5;
    float ms = 0.f;
#pragma unroll
    for (int i = 0; i < 4; ++i) ms += __uint_as_float(stv[i][0]);
    const float mean = xsum32(ms) * 0.125f;
    float m2 = 0.f;
#pragma unroll
    for (int i = 0; i < 4; ++i) { const float d = __uint_as_float(stv[i][0]) - mean; m2 += __uint_as_float(stv[i][1]) + 80.0f * d * d; }
    const float rs = rden_of(xsum32(m2) * (1.0f / D_)), nm = -rs * bf16r(mean);
    const unsigned ab = WS_ACT + (4 * mb + wave) * T32_F + 16 * lane, uvb = opaque(L0 + L4_UV + 16 * h);
#pragma unroll
    for (int p = 0; p < 7; ++p) {
      if (p < 6 || nu == 7) {
        const unsigned tb = uvb + p * 256;
        u32x4 ov;
#pragma unroll
        for (int half = 0; half < 2; ++half) {
          const f32x4 ug = lds_f4(tb + 32 * half), vg = lds_f4(tb + 64 + 32 * half), uu = lds_f4(tb + 128 + 32 * half), vu = lds_f4(tb + 192 + 32 * half);
#pragma unroll
          for (int r = 0; r < 2; ++r) {
            const f32x2 ag = {acc4[p][4 * half + 2 * r], acc4[p][4 * half + 2 * r + 1]}, au = {acc4[p][8 + 4 * half + 2 * r], acc4[p][8 + 4 * half + 2 * r + 1]};
            const f32x2 g = rs * ag + (nm * (f32x2){ug[2 * r], ug[2 * r + 1]} + (f32x2){vg[2 * r], vg[2 * r + 1]});
            const f32x2 u = rs * au + (nm * (f32x2){uu[2 * r], uu[2 * r + 1]} + (f32x2){vu[2 * r], vu[2 * r + 1]});
            const f32x2 e = g * (-LOG2E);
            const f32x2 den = (f32x2){exp2_(e.x), exp2_(e.y)} + 1.0f;
            const f32x2 a = (g * u) * (f32x2){rcpf_(den.x), rcpf_(den.y)};
            ov[2 * half + r] = pk2(a.x, a.y);
          }
        }
        st16_sc1(ov, rws, ab + (u0 + unit_of(p, uodd, nu)) * 1024, 0);
      }
    }
#pragma unroll
    for (int i = 4; i < 8; ++i) wd_piece(2, i);
#pragma unroll
    for (int i = 0; i < 3; ++i) wd_piece(3, i);
    vmwait<7>();
    if (lane == 0) st_u64(ws, flagoff(FL_ACT, mb, 4 * j + wave), flagval(tag));
  }
  BAR();
  STAMP(13);

#pragma unroll
  for (int i = 3; i < 8; ++i) wd_piece(3, i);
#pragma unroll
  for (int i = 0; i < 8; ++i) wd_piece(4, i);
  STAMP(22);
  if (wave == 0) wait_flags(ws, flagoff(FL_ACT, mbL, lane), true, tag);
  vmwait<0>();
  BAR();
  STAMP(14);
  {
    u32x4 af[54];
    const unsigned afb = WS_ACT + (4 * mbL + 2 * th + (wave >> 1)) * T32_F + g4 * 512 + ((wave & 1) * 16 + t16) * 16;
#pragma unroll
    for (int s = 0; s < P5F0; ++s) ald16_sc1(af[s], rws, afb, fo54(s));
    f32x4 acc[5];
#pragma unroll
    for (int p = 0; p < 5; ++p) acc[p] = (f32x4){};
    const B80 B5[3] = {b80(L0), b80(L0 + 61440), b80(L0 + 122880)};
    auto rd5 = [&](int s, int p) { const int sl = s % 30; return w80(B5[sl / 12], (sl % 12) * 5120, p); };
    s16x8 w[3][5];
#pragma unroll
    for (int p = 0; p < 5; ++p) { w[0][p] = rd5(0, p); w[1][p] = rd5(1, p); }
    SB();
    static_for<0, 54>([&](auto SS) {
      constexpr int s = SS;
      if constexpr (s % 6 == 4 && s + 2 < 54) {
        constexpr int g = (s + 2) / 6;
        STAMPN(40 + g - 1);
        if constexpr (g >= 5) vmwait<p5_wait_group(g)>();
        BAR_RAW();
      }
      vmwait<p5_wait_frag(s)>(af[s]);
      SB();
#pragma unroll
      for (int p = 0; p < 5; ++p) {
        acc[p] = mfma16(w[s % 3][p], af[s], acc[p]);
        SB();
        constexpr int r = (s + 20) % 6, i0 = r < 2 ? 2 * r : r + 2;
        if (p == 0 && s + P5F0 < 54) ald16_sc1(af[s + P5F0 < 54 ? s + P5F0 : 0], rws, afb, fo54(s + P5F0));
        if (p == 2 && p5_pieces(s) > 0) wd_piece((s + 20) / 6, i0);
        if (p == 4 && p5_pieces(s) > 1) wd_piece((s + 20) / 6, i0 + 1);
        if (s + 2 < 54) w[(s + 2) % 3][p] = rd5(s + 2, p);
        SB();
      }
    });
    STAMP(15);
    u32x2 op[5];
    const unsigned gbb = opaque(L0 + L3_VEC + 640 + g4 * 16);
#pragma unroll
    for (int p = 0; p < 5; ++p) {
      const f32x4 gb = lds_f4(gbb + p * 64);
#pragma unroll
      for (int r = 0; r < 2; ++r) {
        const f32x2 dn = bf16r2((f32x2){acc[p][2 * r], acc[p][2 * r + 1]});
        const f32x2 o = unpk(x1p[p][r]) + bf16r2(dn * (f32x2){gb[2 * r], gb[2 * r + 1]});
        op[p][r] = pk2(o.x, o.y);
      }
    }
    const rsrc_t ro = mkrsrc(out, S_ * D_ * 2);
#pragma unroll
    for (int pp = 0; pp < 3; ++pp) {
      const int pa = 2 * pp, pb = pp < 2 ? 2 * pp + 1 : 4;
      const auto r0 = __builtin_amdgcn_permlane16_swap(op[pa][0], op[pb][0], false, false), r1 = __builtin_amdgcn_permlane16_swap(op[pa][1], op[pb][1], false, false);
      const int ct = (g4 & 1) ? ct_of(pb, nt & 1) : ct_of(pa, nt & 1);
      __builtin_amdgcn_raw_buffer_store_b128((u32x4){r0[0], r1[0], r0[1], r1[1]}, ro, (pp < 2 || !(g4 & 1)) ? tok3 * 1280 + 160 * nt + 32 * ct + 16 * (g4 >> 1) : 0x40000000u, 0, 16);
    }
  }
  STAMP(16);
  STAMP_FLUSH();
  asm volatile(".Ladaln_code_end:");
}

extern "C" void kernel_launch(void* const* d_in, const int* in_sizes, int n_in, void* d_out, int out_size, void* d_ws, size_t ws_size,
                              hipStream_t stream) {
  (void)in_sizes; (void)n_in; (void)out_size; (void)ws_size;
  adaln_block_kernel<<<256, 256, 0, stream>>>((const char*)d_in[0], (const char*)d_in[1], (const char*)d_in[2], (const char*)d_in[4], (const char*)d_in[5],
                                              (const char*)d_in[6], (const char*)d_in[7], (const char*)d_in[8], (char*)d_out, (char*)d_ws);
}
```

```cpp
#include <hip/hip_runtime.h>
#include <stdint.h>
#include <type_traits>

#define LAS __attribute__((address_space(3)))
#define GAS __attribute__((address_space(1)))
#define DI __device__ __forceinline__
typedef float f32x2 __attribute__((ext_vector_type(2)));
typedef float f32x4 __attribute__((ext_vector_type(4)));
typedef float f32x16 __attribute__((ext_vector_type(16)));
typedef unsigned u32x2 __attribute__((ext_vector_type(2)));
typedef unsigned u32x4 __attribute__((ext_vector_type(4)));
typedef short s16x4 __attribute__((ext_vector_type(4)));
typedef short s16x8 __attribute__((ext_vector_type(8)));
typedef __bf16 bf16x2 __attribute__((ext_vector_type(2)));
typedef __bf16 bf16x8 __attribute__((ext_vector_type(8)));
typedef unsigned long long u64;
typedef __amdgpu_buffer_rsrc_t rsrc_t;

constexpr int S_ = 2048, D_ = 640, F_ = 1728;
constexpr int T32_D = 80 * 512;
constexpr int T32_F = 216 * 512;

constexpr int WS_EPOCH = 0;
constexpr int WS_TMO = 256;
constexpr int WS_FLAG = 29 << 20;
constexpr int FL_XN = 0, FL_KV = 1, FL_O = 2, FL_Y = 3, FL_ACT = 4;
constexpr int WS_GRAN = 256 * 1024;
constexpr int WS_UV = 320 * 1024;
constexpr int WS_STAT = 512 * 1024;
constexpr int WS_STAMP = 28 << 20;
constexpr int WS_XN = 1 << 20;
constexpr int WS_KV = 4 << 20;
constexpr int WS_O = 12 << 20;
constexpr int WS_Y = 16 << 20;
constexpr int WS_ACT = 20 << 20;
constexpr int WS_USED = 30 << 20;

constexpr int LDS_BYTES = 163840;
constexpr int L1_W = 0;
constexpr int L0_TB = 122880;
constexpr int L0_JW = 133120;
constexpr int L1_VS = 153600, L1_VA = 154880;
constexpr int L0_SC = 158720;
constexpr int L2_KV = 0;
constexpr int KV_IMG = 384 * 48;
constexpr int L3_W0 = 4 * KV_IMG;
constexpr int L3_NPRE = 15;
constexpr int L3_W1 = 0;
constexpr int L2_JW = 73728, L2_JV = 94208;
constexpr int L3_JP = 156160;
constexpr int L3_XR = 150528;
constexpr int L_DUMMY = 162816;
constexpr int L3_VEC = 161024;
constexpr int L4_STAGE = 30720, L4_U = 14464;
constexpr int L4_UV = 153600;
static_assert(L3_XR + 64 * 160 <= L3_VEC && L3_W0 + L3_NPRE * 5120 <= L3_XR, "P3 residual rows");
static_assert(L3_W0 + L3_NPRE * 5120 <= L3_VEC && L3_VEC + 960 <= LDS_BYTES, "P3 LDS map");
static_assert(30 * 5120 <= L3_VEC, "P5 LDS map");
static_assert(L4_UV + 1792 <= L3_VEC, "P4 LDS map");

constexpr float LOG2E = 1.4426950408889634f;
constexpr float SCORE_C = 0.22360679774997896f * 1.4426950408889634f;
constexpr float NEGBIG = -1.0e30f;
constexpr unsigned SPIN_CAP = 1u << 15;

#define SB() __builtin_amdgcn_sched_barrier(0)
#define BAR() do { asm volatile("s_waitcnt lgkmcnt(0)" ::: "memory"); __builtin_amdgcn_s_barrier(); asm volatile("" ::: "memory"); } while (0)
#define BAR_RAW() do { asm volatile("" ::: "memory"); __builtin_amdgcn_s_barrier(); asm volatile("" ::: "memory"); } while (0)
#define MEMFENCE() asm volatile("" ::: "memory")
template <int N> DI void vmwait() { asm volatile("s_waitcnt vmcnt(%0)" :: "n"(N) : "memory"); }
template <int N> DI void vmwait(u32x4& d) { asm volatile("s_waitcnt vmcnt(%1)" : "+v"(d) : "n"(N) : "memory"); }

DI unsigned pk2(float a, float b) { bf16x2 v = __builtin_convertvector((f32x2){a, b}, bf16x2); return __builtin_bit_cast(unsigned, v); }
DI float blo(unsigned v) { return __uint_as_float(v << 16); }
DI float bhi(unsigned v) { return __uint_as_float(v & 0xffff0000u); }
DI float bf16r(float a) { return blo(pk2(a, a)); }
DI f32x2 bf16r2(f32x2 a) { const unsigned p = pk2(a.x, a.y); return (f32x2){blo(p), bhi(p)}; }
DI f32x2 unpk(unsigned p) { return (f32x2){blo(p), bhi(p)}; }
DI float rcpf_(float x) { return __builtin_amdgcn_rcpf(x); }
DI float exp2_(float x) { return __builtin_amdgcn_exp2f(x); }
DI float siluf(float x) { return x * rcpf_(1.0f + exp2_(-LOG2E * x)); }
DI float rden_of(float var) { return rcpf_(bf16r(__builtin_amdgcn_sqrtf(bf16r(bf16r(var) + 1e-5f)))); }
template <int CTRL> DI float dpp_add(float x) { return x + __int_as_float(__builtin_amdgcn_update_dpp(0, __float_as_int(x), CTRL, 0xf, 0xf, true)); }
DI float sum16(float x) { x = dpp_add<0xB1>(x); x = dpp_add<0x4E>(x); x = dpp_add<0x141>(x); return dpp_add<0x140>(x); }
DI float xsum16(float x) { auto r = __builtin_amdgcn_permlane16_swap(__float_as_uint(x), __float_as_uint(x), false, false); return __uint_as_float(r[0]) + __uint_as_float(r[1]); }
DI float xsum32(float x) { auto r = __builtin_amdgcn_permlane32_swap(__float_as_uint(x), __float_as_uint(x), false, false); return __uint_as_float(r[0]) + __uint_as_float(r[1]); }
DI float xmax32(float x) { auto r = __builtin_amdgcn_permlane32_swap(__float_as_uint(x), __float_as_uint(x), false, false); return fmaxf(__uint_as_float(r[0]), __uint_as_float(r[1])); }
DI rsrc_t mkrsrc(const void* p, unsigned bytes) { return __builtin_amdgcn_make_buffer_rsrc((void*)p, (short)0, (int)bytes, 0x00020000); }
DI u32x4 ld16_sc1(rsrc_t r, unsigned voff, unsigned soff) { return __builtin_amdgcn_raw_buffer_load_b128(r, voff, soff, 16); }
DI u32x4 ld16(rsrc_t r, unsigned voff, unsigned soff) { return __builtin_amdgcn_raw_buffer_load_b128(r, voff, soff, 0); }
DI u32x2 ld8(rsrc_t r, unsigned voff, unsigned soff) { return __builtin_amdgcn_raw_buffer_load_b64(r, voff, soff, 0); }
DI u32x2 ld8_sc1(rsrc_t r, unsigned voff, unsigned soff) { return __builtin_amdgcn_raw_buffer_load_b64(r, voff, soff, 16); }
DI void st16_sc1(u32x4 v, rsrc_t r, unsigned voff, unsigned soff) { __builtin_amdgcn_raw_buffer_store_b128(v, r, voff, soff, 16); }
DI void ald16_sc1(u32x4& d, rsrc_t r, unsigned voff, unsigned soff) {
  asm volatile("buffer_load_dwordx4 %0, %1, %2, %3 offen sc1" : "=v"(d) : "v"(voff), "s"(r), "s"(soff) : "memory");
}
DI void ald16(u32x4& d, rsrc_t r, unsigned voff, unsigned soff) {
  asm volatile("buffer_load_dwordx4 %0, %1, %2, %3 offen" : "=v"(d) : "v"(voff), "s"(r), "s"(soff) : "memory");
}
DI void ald4(unsigned& d, rsrc_t r, unsigned voff, unsigned soff) {
  asm volatile("buffer_load_dword %0, %1, %2, %3 offen" : "=v"(d) : "v"(voff), "s"(r), "s"(soff) : "memory");
}
DI void ald8(u32x2& d, rsrc_t r, unsigned voff, unsigned soff) {
  asm volatile("buffer_load_dwordx2 %0, %1, %2, %3 offen" : "=v"(d) : "v"(voff), "s"(r), "s"(soff) : "memory");
}
DI void ald8_sc1(u32x2& d, rsrc_t r, unsigned voff, unsigned soff) {
  asm volatile("buffer_load_dwordx2 %0, %1, %2, %3 offen sc1" : "=v"(d) : "v"(voff), "s"(r), "s"(soff) : "memory");
}
DI void touch(rsrc_t r, unsigned voff, unsigned soff, unsigned ldsaddr) {
  asm volatile("s_mov_b32 m0, %0\n\ts_nop 0\n\tbuffer_load_dword %1, %2, %3 offen lds" :: "s"(ldsaddr), "v"(voff), "s"(r), "s"(soff) : "memory");
}
DI void dma16_sc1(rsrc_t r, unsigned voff, unsigned soff, unsigned ldsaddr) {
  asm volatile("s_mov_b32 m0, %0\n\ts_nop 0\n\tbuffer_load_dwordx4 %1, %2, %3 offen sc1 lds" :: "s"(ldsaddr), "v"(voff), "s"(r), "s"(soff) : "memory");
}
DI void dma16(rsrc_t r, unsigned voff, unsigned soff, unsigned ldsaddr) {
  asm volatile("s_mov_b32 m0, %0\n\ts_nop 0\n\tbuffer_load_dwordx4 %1, %2, %3 offen lds" :: "s"(ldsaddr), "v"(voff), "s"(r), "s"(soff) : "memory");
}
DI u64 ld_u64(const char* ws, unsigned off) { return __hip_atomic_load((GAS u64*)(ws + off), __ATOMIC_RELAXED, __HIP_MEMORY_SCOPE_AGENT); }
DI void st_u64(char* ws, unsigned off, u64 v) { __hip_atomic_store((GAS u64*)(ws + off), v, __ATOMIC_RELAXED, __HIP_MEMORY_SCOPE_AGENT); }
DI u64 flagval(unsigned tag) { return ((u64)tag << 32) | (u64)(~tag); }
DI unsigned flagoff(int kind, int mb, int jw) { return WS_FLAG + ((kind * 16 + mb) * 64 + jw) * 128; }
DI void publish(char* ws, int kind, int mb, int j, int wave, int lane, unsigned tag) {
  vmwait<0>();
  if (lane == 0) st_u64(ws, flagoff(kind, mb, 4 * j + wave), flagval(tag));
}
DI bool spin_fail(unsigned& spins, char* ws, unsigned tag) {
  __builtin_amdgcn_s_sleep(1);
  if (++spins > SPIN_CAP) { __hip_atomic_store((GAS unsigned*)(ws + WS_TMO), tag, __ATOMIC_RELAXED, __HIP_MEMORY_SCOPE_AGENT); return true; }
  return (spins & 127u) == 0u && (unsigned)__builtin_amdgcn_readfirstlane(__hip_atomic_load((GAS unsigned*)(ws + WS_TMO), __ATOMIC_RELAXED, __HIP_MEMORY_SCOPE_AGENT)) == tag;
}
DI bool flags_up(const u32x2& f, unsigned tag) { return __all(f[0] == ~tag && f[1] == tag); }
DI void wait_flags(char* ws, unsigned off, bool on, unsigned tag) {
  const u64 want = flagval(tag);
#pragma clang loop unroll(disable)
  for (unsigned spins = 0;;) {
    u64 v = want;
    if (on) v = ld_u64(ws, off);
    if (__all(v == want)) break;
    if (spin_fail(spins, ws, tag)) break;
  }
  MEMFENCE();
}
template <int N, class OFF, class ON> DI void sweep(char* ws, OFF off, ON on, unsigned tag, unsigned (&val)[N]) {
#pragma clang loop unroll(disable)
  for (unsigned spins = 0;;) {
    bool ok = true;
#pragma unroll
    for (int k = 0; k < N; ++k) { u64 g = (u64)tag << 32; if (on(k)) g = ld_u64(ws, off(k)); val[k] = (unsigned)g; ok &= (unsigned)(g >> 32) == tag; }
    if (__all(ok)) break;
    if (spin_fail(spins, ws, tag)) break;
  }
  MEMFENCE();
}
DI s16x8 trfrag(unsigned a0, unsigned a1) {
  const s16x4 lo = __builtin_amdgcn_ds_read_tr16_b64_v4i16((LAS s16x4*)(size_t)a0);
  const s16x4 hi = __builtin_amdgcn_ds_read_tr16_b64_v4i16((LAS s16x4*)(size_t)a1);
  return __builtin_shufflevector(lo, hi, 0, 1, 2, 3, 4, 5, 6, 7);
}
DI f32x4 lds_f4(unsigned a) { return *(LAS f32x4*)(size_t)a; }
DI u32x4 lds_u4(unsigned a) { return *(LAS u32x4*)(size_t)a; }
DI void lds_st4(unsigned a, u32x4 v) { *(LAS u32x4*)(size_t)a = v; }
DI void lds_st1(unsigned a, unsigned v) { *(LAS unsigned*)(size_t)a = v; }
DI void lds_st2f(unsigned a, f32x2 v) { *(LAS f32x2*)(size_t)a = v; }
DI unsigned opaque(unsigned v) { asm volatile("" : "+v"(v)); return v; }
DI unsigned sopaque(unsigned v) { asm volatile("" : "+s"(v)); return v; }
struct Rot { unsigned b[4]; };
DI Rot mkrot(int r, int shift, int N, int step) {
  const unsigned a = shift * step, w = N * step;
  return Rot{{sopaque(a), sopaque(a - (r >= 3 ? w : 0)), sopaque(a - (r >= 2 ? w : 0)), sopaque(a - (r >= 1 ? w : 0))}};
}
DI f32x16 mfma32(s16x8 a, u32x4 b, f32x16 c) { return __builtin_amdgcn_mfma_f32_32x32x16_bf16(__builtin_bit_cast(bf16x8, a), __builtin_bit_cast(bf16x8, b), c, 0, 0, 0); }
DI f32x4 mfma16(s16x8 a, u32x4 b, f32x4 c) { return __builtin_amdgcn_mfma_f32_16x16x32_bf16(__builtin_bit_cast(bf16x8, a), __builtin_bit_cast(bf16x8, b), c, 0, 0, 0); }
DI void smfma_c(f32x16& d, u32x4 a, u32x4 b, const f32x16& c) { asm("v_mfma_f32_32x32x16_bf16 %0, %1, %2, %3" : "=&v"(d) : "v"(a), "v"(b), "v"(c)); }
DI void smfma_0(f32x16& d, u32x4 a, u32x4 b) { asm("v_mfma_f32_32x32x16_bf16 %0, %1, %2, 0" : "=&v"(d) : "v"(a), "v"(b)); }
DI void smfma_a(f32x16& d, u32x4 a, u32x4 b) { asm("v_mfma_f32_32x32x16_bf16 %0, %1, %2, %0" : "+v"(d) : "v"(a), "v"(b)); }
DI void gmfma_0(f32x16& d, s16x8 a, u32x4 b) { asm("v_mfma_f32_32x32x16_bf16 %0, %1, %2, 0" : "=&v"(d) : "a"(a), "v"(b)); }
DI void gmfma_a(f32x16& d, s16x8 a, u32x4 b) { asm("v_mfma_f32_32x32x16_bf16 %0, %1, %2, %0" : "+v"(d) : "a"(a), "v"(b)); }
DI void hmfma_0(f32x4& d, s16x8 a, u32x4 b) { asm("v_mfma_f32_16x16x32_bf16 %0, %1, %2, 0" : "=&v"(d) : "a"(a), "v"(b)); }
DI void hmfma_a(f32x4& d, s16x8 a, u32x4 b) { asm("v_mfma_f32_16x16x32_bf16 %0, %1, %2, %0" : "+v"(d) : "a"(a), "v"(b)); }
DI void sfence(f32x16& d) { asm volatile("s_nop 11" : "+v"(d)); }
DI float fmix_lo(unsigned x, unsigned s, unsigned a) { float r; asm("v_fma_mix_f32 %0, %1, %2, %3 op_sel_hi:[1,1,1]" : "=v"(r) : "v"(x), "v"(s), "v"(a)); return r; }
DI float fmix_hi(unsigned x, unsigned s, unsigned a) { float r; asm("v_fma_mix_f32 %0, %1, %2, %3 op_sel:[1,1,1] op_sel_hi:[1,1,1]" : "=v"(r) : "v"(x), "v"(s), "v"(a)); return r; }
DI unsigned pkh(float a, float b) { return __builtin_bit_cast(unsigned, __builtin_amdgcn_cvt_pkrtz(a, b)); }

#define STAMP(i) do { } while (0)
#define STAMPN(i) do { } while (0)
#define STAMP_INIT() do { } while (0)
#define STAMP_FLUSH() do { } while (0)

template <int NS> DI void gemv16_issue(rsrc_t rw, unsigned rowstride, unsigned srcbyte, unsigned ldsw, int lane) {
  const unsigned vo = (8 * ((lane >> 3) & 3) + 4 * (lane >> 5) + ((lane >> 1) & 3)) * rowstride + 16 * (lane & 1);
#pragma unroll 5
  for (int s = 0; s < NS; ++s) dma16(rw, vo, s * 32 * rowstride + srcbyte, ldsw + s * 1024);
}
template <int NS> DI f32x4 gemv16_run(unsigned ldsw, unsigned ldsv, int vmask, int lane) {
  f32x4 acc0 = {}, acc1 = {};
  const unsigned wa = opaque(ldsw + 8 * lane), va = opaque(ldsv + (lane & vmask) * 1280 + (lane >> 4) * 16);
  s16x8 w[3]; u32x4 v[3];
#pragma unroll
  for (int s = 0; s < 2; ++s) { w[s] = trfrag(wa + s * 1024, wa + s * 1024 + 512); v[s] = lds_u4(va + s * 64); }
  SB();
#pragma unroll
  for (int s = 0; s < NS; ++s) {
    if (s + 2 < NS) { w[(s + 2) % 3] = trfrag(wa + (s + 2) * 1024, wa + (s + 2) * 1024 + 512); v[(s + 2) % 3] = lds_u4(va + (s + 2) * 64); }
    SB();
    if (s & 1) acc1 = mfma16(w[s % 3], v[s % 3], acc1); else acc0 = mfma16(w[s % 3], v[s % 3], acc0);
    SB();
  }
  return acc0 + acc1;
}

DI int ct_of(int p, int odd) { return odd ? (p < 4 ? p + 1 : 0) : p; }
DI int unit_of(int p, int odd, int nu) { const int u = odd ? (p < 6 ? p + 1 : 0) : p; return u < nu ? u : nu - 1; }

template <class RM> DI unsigned w80_voff(int piece, int lane, int nt, RM rowmap) {
  const int odd = nt & 1;
  int g, t, q, p, hf;
  if (piece < 4) { g = piece; t = lane >> 5; const int su = (lane >> 4) & 1; q = (lane >> 2) & 3; const int c = (lane & 3) ^ (2 * (piece & 1)); p = 2 * su + (c >> 1); hf = c & 1; }
  else { t = lane >> 5; g = (lane >> 3) & 3; q = (lane >> 1) & 3; p = 4; hf = lane & 1; }
  return rowmap(g, t, q) * 1280 + 160 * nt + 32 * ct_of(p, odd) + 16 * hf;
}

template <class OPS> constexpr int vm_sum(OPS ops, int lo, int hi) { int n = 0; for (int k = lo; k <= hi; ++k) if (k >= 0) n += ops(k); return n; }
constexpr int P1F0 = 10;
constexpr int p1_pieces(int k) { return k < 24 ? 1 : 0; }
constexpr int p1_ops(int k) { return (k < 40 - P1F0 ? 1 : 0) + p1_pieces(k) + (k >= 30 && k < 38 ? 1 : 0); }
constexpr int p1_wait_frag(int f) {
  return f < P1F0 ? 8 + vm_sum(p1_ops, 0, f - 3) : p1_pieces(f - P1F0) + vm_sum(p1_ops, f - P1F0 + 1, f - 3);
}
constexpr int p1_wait_stage(int st) { return st == 2 ? 4 + vm_sum(p1_ops, 0, 5) : st == 3 ? vm_sum(p1_ops, 0, 9) : vm_sum(p1_ops, 4 * st - 12, 4 * st - 3); }
constexpr int P4F0 = 8;
constexpr int p4_pieces(int k) { return k >= 2 && k <= 25 ? ((k + 14) % 4 == 3 ? 1 : 2) : 0; }
constexpr int p4_wd_before(int k) { return k <= 26 ? 0 : k == 27 ? 2 : k == 28 ? 3 : k == 29 ? 5 : k == 30 ? 6 : k == 31 ? 8 : k == 32 ? 9 : k == 33 ? 11 : k == 34 ? 12 : k == 35 ? 14 : k <= 40 ? 16 + (k - 36) : 20; }
constexpr int p4_touch(int k) { return p4_wd_before(k + 1) - p4_wd_before(k); }
constexpr int p4_ops(int k) { return (k < 40 - P4F0 ? 1 : 0) + p4_pieces(k) + p4_touch(k); }
constexpr int p4_wait_frag(int ks) { return ks < P4F0 ? (P4F0 - 1 - ks) + vm_sum(p4_ops, 0, ks - 1) : p4_pieces(ks - P4F0) + p4_touch(ks - P4F0) + vm_sum(p4_ops, ks - P4F0 + 1, ks - 1); }
constexpr int p4_wait_stage(int st) { return vm_sum(p4_ops, 4 * st - 10, 4 * st - 3); }
constexpr int P5F0 = 12;
constexpr int p5_pieces(int k) { return k >= 10 && k <= 33 ? ((k + 20) % 6 < 2 ? 2 : 1) : 0; }
constexpr int p5_ops(int k) { return (k < 54 - P5F0 ? 1 : 0) + p5_pieces(k); }
constexpr int p5_wait_frag(int s) { return s < P5F0 ? (P5F0 - 1 - s) + vm_sum(p5_ops, 0, s - 1) : p5_pieces(s - P5F0) + vm_sum(p5_ops, s - P5F0 + 1, s - 1); }
constexpr int p5_wait_group(int g) { return vm_sum(p5_ops, 6 * g - 14, 6 * g - 3); }
constexpr int P3F0 = 6;
constexpr int p3_side(int k) { return 0; }
constexpr int p3_ops(int k) { return (k < 20 - P3F0 ? 1 : 0) + p3_side(k); }
constexpr int p3_wait_frag(int s) { return s < P3F0 ? (P3F0 - 1 - s) + vm_sum(p3_ops, 0, s - 1) : p3_side(s - P3F0) + vm_sum(p3_ops, s - P3F0 + 1, s - 1); }

template <int I, int N, class Fn> DI void static_for(Fn&& f) { if constexpr (I < N) { f(std::integral_constant<int, I>{}); static_for<I + 1, N>(f); } }

__global__ __launch_bounds__(256, 1) void adaln_block_kernel(const char* __restrict__ x, const char* __restrict__ wqkv, const char* __restrict__ wo,
                                                             const char* __restrict__ wg, const char* __restrict__ wu, const char* __restrict__ wd,
                                                             const char* __restrict__ cvec, const char* __restrict__ wmod, char* out, char* ws) {
  __shared__ __attribute__((aligned(16))) char smem[LDS_BYTES];
  const unsigned L0 = (unsigned)(size_t)(LAS char*)smem;
  const int tid = threadIdx.x, lane = tid & 63;
  const int wave = __builtin_amdgcn_readfirstlane(tid >> 6);
  const int bid = blockIdx.x, xcd = bid & 7, ii = bid >> 3;
  const int mbL = 2 * xcd + (ii >> 4), jL = ii & 15;
  const int mb = 4 * (xcd >> 1) + (ii >> 3), j = 8 * (xcd & 1) + (ii & 7);
  const int rW = mb & 3, rL = 0, rD = 2 * (mbL & 1) + (jL & 1);
  const int soW = (5 * rW) >> 1;
  const int goL = rD == 3 ? 7 : 2 * rD;
  const unsigned tag = *(const GAS unsigned*)(ws + WS_EPOCH) + 1u;
  const rsrc_t rx = mkrsrc(x, S_ * D_ * 2), rws = mkrsrc(ws, WS_USED);
  STAMP_INIT();
  STAMP(0);

  const rsrc_t rwq = mkrsrc(wqkv, D_ * 3 * D_ * 2);
  unsigned wqo[4];
  auto rot10 = [&](int so, int st) { const int g = st + so; return g >= 10 ? g - 10 : g; };
  const int so1 = (int)sopaque(soW);
  auto wq_piece = [&](int st, int i) { dma16(rwq, wqo[i], rot10(so1, st) * 64 * 3840, L0 + L1_W + (st * 15 + min(4 * wave + i, 14)) * 1024); };
  auto fo40 = [&](const Rot& R, int ks) { return R.b[ks < 12 ? 0 : ks < 20 ? 1 : ks < 32 ? 2 : 3] + ks * 1024; };
  auto wq_issue = [&](int st) {
#pragma unroll
    for (int i = 0; i < 4; ++i) wq_piece(st, i);
  };
  {
    const int c32 = lane & 31, tok = 128 * mbL + 8 * jL + 2 * wave + (lane >> 5);
    u32x4 xv[3], c4 = {}; unsigned c1 = 0;
    ald16(xv[0], rx, tok * 1280 + c32 * 16, 0);
    ald16(xv[1], rx, tok * 1280 + c32 * 16 + 512, 0);
    ald16(xv[2], rx, c32 < 16 ? tok * 1280 + c32 * 16 + 1024 : 0x40000000u, 0);
    if (wave == 3) {
      u64 pc; int len;
      asm volatile("s_getpc_b64 %0\n.Ladaln_code_begin:\n\ts_mov_b32 %1, .Ladaln_code_end-.Ladaln_code_begin" : "=s"(pc), "=s"(len));
      len = len > 0 && len < (1 << 18) ? len : 0;
      touch(mkrsrc((const void*)pc, len), (ii * 40 + min(lane, 39)) * 64, 0, L0 + L_DUMMY);
    }
    const int munit = ii < 10 ? 10 * xcd + ii : 80 + 20 * xcd + (ii - 10);
    const bool mjob = wave == 0 && ii < 30;
    if (mjob) {
      const rsrc_t rc = mkrsrc(cvec, D_ * 2);
      ald16(c4, rc, 20 * lane, 0); ald4(c1, rc, 20 * lane + 16, 0);
    }
#pragma unroll
    for (int i = 0; i < 4; ++i) {
      const int idx = 64 * min(4 * wave + i, 14) + lane, rq = idx / 60, rem = idx - 60 * rq;
      int q, ch;
      if (rem < 48) { q = (rem >> 2) & 3; ch = 4 * (rem >> 4) + (rem & 3); } else { const int r2 = rem - 48; q = r2 / 3; ch = 12 + r2 - 3 * q; }
      const int seg = ch / 5;
      wqo[i] = (4 * rq + q) * 3840 + seg * 1280 + 80 * j + (ch - 5 * seg) * 16;
    }
    if (mjob) {
      gemv16_issue<20>(mkrsrc(wmod, D_ * 6 * D_ * 2), 7680, 32 * munit, L0 + L0_JW, lane);
      vmwait<20>();
    } else {
      vmwait<0>();
    }
    asm volatile("" : "+v"(xv[0]), "+v"(xv[1]), "+v"(xv[2]), "+v"(c4), "+v"(c1));
    STAMPN(48);
    f32x2 s2 = {0.f, 0.f};
#pragma unroll
    for (int i = 0; i < 3; ++i)
#pragma unroll
      for (int e = 0; e < 4; ++e) s2 += unpk(xv[i][e]);
    const float mean = xsum16(sum16(s2.x + s2.y)) * (1.0f / D_);
    f32x2 q2 = {0.f, 0.f}, q3 = {0.f, 0.f};
#pragma unroll
    for (int e = 0; e < 4; ++e) {
      const f32x2 d0 = unpk(xv[0][e]) - mean, d1 = unpk(xv[1][e]) - mean, d2 = unpk(xv[2][e]) - mean;
      q2 += d0 * d0; q2 += d1 * d1; q3 += d2 * d2;
    }
    const float var = xsum16(sum16(q2.x + q2.y + (c32 < 16 ? q3.x + q3.y : 0.f))) * (1.0f / D_);
    const float mq = bf16r(mean), rden = rden_of(var);
#pragma unroll
    for (int i = 0; i < 3; ++i) {
      u32x4 o;
#pragma unroll
      for (int e = 0; e < 4; ++e) { const f32x2 d = bf16r2(bf16r2(unpk(xv[i][e]) - mq) * rden); o[e] = pkh(d.x, d.y); }
      const int slot = c32 + 32 * i;
      if (i < 2 || c32 < 16) lds_st4(L0 + L0_TB + slot * 128 + (((2 * wave + (lane >> 5)) ^ (slot & 7)) * 16), o);
    }
    STAMPN(49);
    BAR();
    STAMPN(50);
#pragma unroll
    for (int u = 0; u < 3; ++u) {
      const int i = 256 * u + tid, slot = i >> 3, tk = (i & 7) ^ (slot & 7);
      const unsigned voff = (u < 2 || tid < 128) ? WS_XN + (4 * mbL + (jL >> 2)) * T32_D + slot * 512 + (8 * (jL & 3) + tk) * 16 : 0x40000000u;
      st16_sc1(lds_u4(L0 + L0_TB + min(i, 639) * 16), rws, voff, 0);
    }
    STAMPN(51);
    wq_issue(0);
    vmwait<4>();
    if (lane == 0) st_u64(ws, flagoff(FL_XN, mbL, 4 * jL + wave), flagval(tag));
    STAMP(1);
    if (mjob) {
      const unsigned cc[5] = {c4[0], c4[1], c4[2], c4[3], c1};
#pragma unroll
      for (int r = 0; r < 5; ++r) lds_st1(L0 + L0_SC + 20 * lane + 4 * r, pk2(siluf(blo(cc[r])), siluf(bhi(cc[r]))));
      asm volatile("s_waitcnt lgkmcnt(0)" ::: "memory");
      const f32x4 d = gemv16_run<20>(L0 + L0_JW, L0 + L0_SC, 0, lane);
      const unsigned pv = (lane & 1) ? pk2(d[2], d[3]) : pk2(d[0], d[1]);
      if ((lane & 15) < 2) st_u64(ws, WS_GRAN + (8 * munit + 2 * (lane >> 4) + (lane & 1)) * 8, ((u64)tag << 32) | pv);
    }
    STAMPN(52);
    wq_issue(1);
    STAMPN(53);
  }
  if (wave == 0) wait_flags(ws, flagoff(FL_XN, mb, lane), true, tag);
  BAR_RAW();
  STAMP(17);
  u32x4 xf[40];
  const unsigned xfo = WS_XN + (4 * mb + wave) * T32_D + 16 * lane;
  const Rot R1 = mkrot(rW, 4 * soW, 40, 1024);
  {
    const int g0 = 160 * wave + lane;
    u32x2 ga[3];
#pragma unroll
    for (int ks = 0; ks < P1F0; ++ks) ald16_sc1(xf[ks], rws, xfo, fo40(R1, ks));
#pragma unroll
    for (int k = 0; k < 3; ++k) ald8_sc1(ga[k], rws, WS_GRAN + (k < 2 || lane < 32 ? g0 + 64 * k : g0) * 8, 0);
    wq_issue(2); wq_issue(3);
    vmwait<8>();
    asm volatile("" : "+v"(ga[0]), "+v"(ga[1]), "+v"(ga[2]));
    unsigned gv[3] = {ga[0][0], ga[1][0], ga[2][0]};
    if (!__all(ga[0][1] == tag && ga[1][1] == tag && ga[2][1] == tag))
      sweep<3>(ws, [&](int k) { return WS_GRAN + (g0 + 64 * k) * 8; }, [&](int k) { return k < 2 || lane < 32; }, tag, gv);
    STAMP(18);
#pragma unroll
    for (int k = 0; k < 3; ++k) if (k < 2 || lane < 32) {
      const int g = g0 + 64 * k;
      f32x2 v = unpk(gv[k]);
      if (g >= 320) v = bf16r2(v + 1.0f);
      const int gp = g >= 320 ? g - 320 : g;
      int kp = (gp >> 3) - 4 * soW; kp += kp < 0 ? 40 : 0;
      lds_st1(L0 + (g >= 320 ? L1_VS : L1_VA) + kp * 32 + (gp & 7) * 4, pkh(v.x, v.y));
    }
  }
  BAR();
  STAMP(2);

  const int jidx = 32 * (xcd >> 1) + ii, jper = (xcd & 1) ? 52 : 56;
  const bool ujob = jidx < 2 * jper;
  const int jmat = jidx >= jper, junit = ((xcd & 1) ? 56 : 0) + jidx - jmat * jper;
  auto jgi = [&](int k) { const int i = 64 * k + lane; return i < 80 ? 1280 + 80 * wave + i : i < 160 ? 960 + 80 * wave + i - 80 : 1280; };
  const rsrc_t rjw = mkrsrc(jmat ? wu : wg, D_ * F_ * 2);
  const unsigned jvo = (8 * ((lane >> 3) & 3) + 4 * (lane >> 5) + ((lane >> 1) & 3)) * 3456 + 16 * (lane & 1);
  const unsigned jso = sopaque(32 * (ujob ? junit : 0) + wave * 160 * 3456);
  u32x2 jg[3];
  f32x16 acc1[4];
  {
    const int h = lane >> 5;
    const unsigned wb = opaque(L0 + L1_W + 2 * h * 960 + ((lane & 15) >> 2) * 64 + ((lane >> 4) & 1) * 32 + (lane & 3) * 8);
    const unsigned wb3 = opaque(L0 + L1_W + 2 * h * 960 + 768 + ((lane & 15) >> 2) * 48 + ((lane >> 4) & 1) * 32 + (lane & 3) * 8);
    const unsigned wbh = opaque(wb + 61440), wb3h = opaque(wb3 + 61440), wbg = opaque(wb + 122880), wb3g = opaque(wb3 + 122880);
    const unsigned vb = opaque(L0 + L1_VS + h * 16);
    s16x8 w[3][4]; u32x4 hf[2], vs[2], va[2]; float ft[2][8];
    auto readw = [&](int ks, int b) {
      const int kl = ks % 16; const unsigned base = ks < 16 ? (b < 3 ? wb : wb3) : ks < 32 ? (b < 3 ? wbh : wb3h) : (b < 3 ? wbg : wb3g);
      const unsigned a = base + (b < 3 ? b * 256 : 0) + kl * 3840; return trfrag(a, a + 960); };
    auto reads = [&](int ks) { vs[ks & 1] = lds_u4(vb + ks * 32); };
    auto reada = [&](int ks) { va[ks & 1] = lds_u4(vb + 1280 + ks * 32); };
    auto fstage = [&](int ks, int e) {
      ft[ks & 1][2 * e] = fmix_lo(xf[ks][e], vs[ks & 1][e], va[ks & 1][e]);
      ft[ks & 1][2 * e + 1] = fmix_hi(xf[ks][e], vs[ks & 1][e], va[ks & 1][e]); };
    auto cstage = [&](int ks, int e) { hf[ks & 1][e] = pk2(ft[ks & 1][2 * e], ft[ks & 1][2 * e + 1]); };
#pragma unroll
    for (int b = 0; b < 4; ++b) { w[0][b] = readw(0, b); w[1][b] = readw(1, b); }
    reads(0); reada(0); reads(1); reada(1);
    vmwait<p1_wait_frag(0)>(xf[0]);
#pragma unroll
    for (int e = 0; e < 4; ++e) fstage(0, e);
#pragma unroll
    for (int e = 0; e < 4; ++e) cstage(0, e);
    vmwait<p1_wait_frag(1)>(xf[1]);
#pragma unroll
    for (int e = 0; e < 4; ++e) fstage(1, e);
    reads(2); reada(2);
    asm volatile("s_nop 1" : "+v"(hf[0]));
    SB();
    static_for<0, 40>([&](auto KS) {
      constexpr int ks = KS, c = ks & 1, wc = ks % 3, wn = (ks + 2) % 3;
      constexpr bool needf = ks + 2 < 40, needs = (ks & 3) == 2 && (ks + 2) / 4 >= 2 && ks + 2 < 40;
      constexpr int nf = needf ? p1_wait_frag(ks + 2) : 63, ns = needs ? p1_wait_stage((ks + 2) / 4) : 63;
      if constexpr (needs) { vmwait<(nf < ns ? nf : ns)>(); BAR_RAW(); if constexpr (ks >= 14) STAMPN(24 + (ks - 14) / 4); }
      if constexpr (needf) vmwait<nf>(xf[ks + 2]);
      SB();
      if constexpr (ks == 0) gmfma_0(acc1[0], w[wc][0], hf[c]); else gmfma_a(acc1[0], w[wc][0], hf[c]);
      SB();
      if constexpr (ks + 2 < 40) w[wn][0] = readw(ks + 2, 0);
      if constexpr (ks + 1 < 40) { cstage(ks + 1, 0); cstage(ks + 1, 1); }
      if constexpr (ks + 3 < 40) reads(ks + 3);
      SB();
      if constexpr (ks == 0) gmfma_0(acc1[1], w[wc][1], hf[c]); else gmfma_a(acc1[1], w[wc][1], hf[c]);
      SB();
      if constexpr (ks + P1F0 < 40) ald16_sc1(xf[ks + P1F0], rws, xfo, fo40(R1, ks + P1F0));
      if constexpr (ks + 2 < 40) w[wn][1] = readw(ks + 2, 1);
      if constexpr (ks + 1 < 40) { cstage(ks + 1, 2); cstage(ks + 1, 3); }
      if constexpr (ks + 3 < 40) reada(ks + 3);
      SB();
      if constexpr (ks == 0) gmfma_0(acc1[2], w[wc][2], hf[c]); else gmfma_a(acc1[2], w[wc][2], hf[c]);
      SB();
      if constexpr (ks + 2 < 40) w[wn][2] = readw(ks + 2, 2);
      if constexpr (ks + 2 < 40) { fstage(ks + 2, 0); fstage(ks + 2, 1); }
      SB();
      if constexpr (ks == 0) gmfma_0(acc1[3], w[wc][3], hf[c]); else gmfma_a(acc1[3], w[wc][3], hf[c]);
      SB();
      if constexpr (ks < 24) wq_piece(ks / 4 + 4, ks & 3);
      if constexpr (ks >= 30 && ks < 35) dma16(rjw, jvo, jso + (ks - 30) * 32 * 3456, L0 + L2_JW + wave * 5120 + (ks - 30) * 1024);
      if constexpr (ks >= 35 && ks < 38) ald8_sc1(jg[ks - 35], rws, WS_GRAN + jgi(ks - 35) * 8, 0);
      if constexpr (ks + 2 < 40) w[wn][3] = readw(ks + 2, 3);
      if constexpr (ks + 2 < 40) { fstage(ks + 2, 2); fstage(ks + 2, 3); }
      SB();
    });
  }
  asm volatile("s_nop 11" : "+v"(acc1[0]), "+v"(acc1[1]), "+v"(acc1[2]), "+v"(acc1[3]));
  STAMP(3);

  u32x4 qa0, qa1, qb0, qb1;
  {
    const int h = lane >> 5, t = lane & 31;
    u32x2 P[15];
#pragma unroll
    for (int i = 0; i < 15; ++i) { const f32x16& a = acc1[i >> 2]; const int r = 4 * (i & 3); P[i] = (u32x2){pk2(a[r], a[r + 1]), pk2(a[r + 2], a[r + 3])}; }
    const u32x2 one = {0x00003f80u, 0u}, zero = {0u, 0u};
    auto cat = [](u32x2 a, u32x2 b) { return (u32x4){a[0], a[1], b[0], b[1]}; };
    auto sel = [&](u32x2 a1, u32x2 a0) { return (u32x2){h ? a1[0] : a0[0], h ? a1[1] : a0[1]}; };
    const unsigned row = L0 + L2_KV + (256 + 32 * wave + t) * 48;
    lds_st4(row + 16 * h, cat(P[5], P[6]));
    lds_st4(row + KV_IMG + 16 * (h ^ 1), cat(sel(P[7], P[8]), sel(P[8], P[9])));
    lds_st4(row + h * KV_IMG + 32, cat(sel(P[9], P[7]), zero));
    lds_st4(row + 2 * KV_IMG + 16 * h, cat(P[10], P[11]));
    lds_st4(row + 3 * KV_IMG + 16 * (h ^ 1), cat(sel(P[12], P[13]), sel(P[13], P[14])));
    lds_st4(row + (2 + h) * KV_IMG + 32, cat(sel(P[14], P[12]), one));
    asm volatile("s_waitcnt lgkmcnt(0)" ::: "memory");
#pragma unroll
    for (int u = 0; u < 6; ++u) {
      const int i = 64 * u + lane, a = i / 96, o = (i - 96 * a) * 16;
      const u32x4 v = lds_u4(L0 + L2_KV + a * KV_IMG + (256 + 32 * wave) * 48 + o);
      st16_sc1(v, rws, WS_KV + ((mb * 16 + j) * 4 + a) * 6144 + wave * 1536 + o, 0);
    }
    qa0 = cat(P[0], P[1]);
    qa1 = cat(sel(zero, P[2]), sel(zero, one));
    qb0 = cat(sel(P[2], P[3]), sel(P[3], P[4]));
    qb1 = cat(sel(P[4], zero), sel(one, zero));
    publish(ws, FL_KV, mb, j, wave, lane, tag);
  }
  STAMP(4);
  asm volatile("" : "+v"(jg[0]), "+v"(jg[1]), "+v"(jg[2]));
  {
    unsigned gv[3] = {jg[0][0], jg[1][0], jg[2][0]};
    if (!__all(jg[0][1] == tag && jg[1][1] == tag && jg[2][1] == tag))
      sweep<3>(ws, [&](int k) { return WS_GRAN + jgi(k) * 8; }, [&](int) { return true; }, tag, gv);
#pragma unroll
    for (int k = 0; k < 3; ++k) if (k < 2 || lane < 32) {
      const int i = 64 * k + lane;
      unsigned v = gv[k];
      if (i < 80) { const f32x2 t = unpk(v) + 1.0f; v = pk2(t.x, t.y); }
      lds_st1(L0 + L2_JV + (i < 80 ? 320 * wave + 4 * i : 1280 + 320 * wave + 4 * (i - 80)), v);
    }
    asm volatile("s_waitcnt lgkmcnt(0)" ::: "memory");
    const f32x4 d = gemv16_run<5>(L0 + L2_JW + wave * 5120, L0 + L2_JV + wave * 320, 1, lane);
    lds_st4(L0 + L3_JP + wave * 1024 + lane * 16, __builtin_bit_cast(u32x4, d));
  }

  {
    const int blk = wave >> 1, src = mb - 2 + blk;
    if (src >= 0) {
      wait_flags(ws, flagoff(FL_KV, src, 4 * j + (lane & 3)), lane < 4, tag);
      STAMP(5);
#pragma unroll
      for (int i = 0; i < 12; ++i) {
        const int a = 2 * (wave & 1) + i / 6, sub = i % 6;
        dma16_sc1(rws, 16 * lane, WS_KV + ((src * 16 + j) * 4 + a) * 6144 + sub * 1024, L0 + L2_KV + a * KV_IMG + blk * 6144 + sub * 1024);
      }
    } else {
      STAMP(5);
#pragma unroll
      for (int i = 0; i < 12; ++i) {
        const int a = 2 * (wave & 1) + i / 6, o = (i % 6) * 1024 + 16 * lane;
        lds_st4(L0 + L2_KV + a * KV_IMG + blk * 6144 + o, (u32x4){0u, 0u, ((o % 48) == 32 && a < 2) ? 0x0000f149u : 0u, 0u});
      }
    }
  }
  const int th = jL & 1, nt = jL >> 1;
  const rsrc_t rwo = mkrsrc(wo, D_ * D_ * 2);
  unsigned woo[7], wol[7];
#pragma unroll
  for (int i = 0; i < 7; ++i) {
    const int pi = min(wave + 4 * i, 24), kc = pi / 5, pc = pi - 5 * kc;
    auto rowmap = [&](int g, int t, int q) { const int k = 32 * kc + 8 * g + 4 * t + q, hp = k / 40, r = k - 40 * hp; return 40 * hp + (r < 20 ? r : r < 24 ? r + 16 : r - 4); };
    woo[i] = opaque(w80_voff(pc, lane, nt, rowmap));
    wol[i] = pi * 1024;
  }
  auto wo_piece = [&](int gam, int i) { dma16(rwo, woo[i], ((gam + rL) & 3) * 160 * 1280, L0 + (gam < 3 ? L3_W0 + gam * 25600 : L3_W1) + wol[i]); };
  vmwait<0>();
  BAR();
  STAMP(6);
  if (ujob && wave == 1) {
    f32x4 d = lds_f4(L0 + L3_JP + lane * 16);
#pragma unroll
    for (int w = 1; w < 4; ++w) d += lds_f4(L0 + L3_JP + w * 1024 + lane * 16);
    if ((lane & 15) < 2) {
#pragma unroll
      for (int r = 0; r < 4; ++r) st_u64(ws, WS_UV + ((jmat * 108 + junit) * 32 + (lane & 15) * 16 + 4 * (lane >> 4) + r) * 8, ((u64)tag << 32) | __float_as_uint(d[r]));
    }
  }

  {
    const rsrc_t rgt = mkrsrc(wave < 2 ? wg : wu, D_ * F_ * 2);
    const rsrc_t rdt = mkrsrc(wd, F_ * D_ * 2);
    const unsigned tdo = (135 * bid + min(64 * wave + lane, 134)) * 64;
    unsigned xro[3];
#pragma unroll
    for (int i = 0; i < 3; ++i) { const int idx = 64 * min(3 * wave + i, 9) + lane, row = idx / 10; xro[i] = (128 * mbL + 64 * th + row) * 1280 + 160 * nt + (idx - 10 * row) * 16; }
    const unsigned tgo = (20 * ii + 10 * (wave & 1) + (lane >> 5)) * 3456 + (xcd & 1) * 1792 + 64 * min(lane & 31, (xcd & 1) ? 25 : 27);
    const int h = lane >> 5, t = lane & 31;
    f32x16 mfirst, mdiag;
#pragma unroll
    for (int r = 0; r < 16; ++r) { const int key = (r & 3) + 8 * (r >> 2) + 4 * h; mfirst[r] = key > t ? 0.f : NEGBIG; mdiag[r] = key > t ? NEGBIG : 0.f; }
    const unsigned krow = L0 + L2_KV + (32 * wave + t) * 48;
    const unsigned ka0 = opaque(krow + 16 * h), ka1 = opaque(krow + 32), kb0 = opaque(krow + KV_IMG + 16 * (h ^ 1)), kb1 = opaque(krow + KV_IMG + 32);
    const unsigned vrow = L0 + L2_KV + 2 * KV_IMG + (32 * wave + 4 * h + ((lane & 15) >> 2)) * 48 + ((lane >> 4) & 1) * 32 + (lane & 3) * 8;
    const unsigned vra = opaque(vrow), vrb = opaque(vrow + KV_IMG);
    f32x16 sa[9], sb[9];
    auto qk = [&](f32x16& s, int kb, u32x4 a0, u32x4 a1, u32x4 q0, u32x4 q1) {
      if (kb == 0) smfma_c(s, a0, q0, mfirst); else if (kb == 8) smfma_c(s, a0, q0, mdiag); else smfma_0(s, a0, q0);
      smfma_a(s, a1, q1); };
    float e[8];
    auto pfma = [&](const f32x16& s, int half, float mc) {
#pragma unroll
      for (int i = 0; i < 8; ++i) e[i] = fmaf(s[8 * half + i], SCORE_C, mc); };
    auto pexp = [&]() {
#pragma unroll
      for (int i = 0; i < 8; ++i) e[i] = exp2_(e[i]); };
    auto pcvt = [&]() { return (u32x4){pk2(e[0], e[1]), pk2(e[2], e[3]), pk2(e[4], e[5]), pk2(e[6], e[7])}; };
    auto qk0 = [&](f32x16& s, int kb, u32x4 a0, u32x4 q0) { if (kb == 0) smfma_c(s, a0, q0, mfirst); else if (kb == 8) smfma_c(s, a0, q0, mdiag); else smfma_0(s, a0, q0); };
    auto maxhalf = [&](float (&m)[4], const f32x16& s, int half) {
#pragma unroll
      for (int r = 8 * half; r < 8 * half + 8; r += 2) m[(r >> 1) & 3] = fmaxf(fmaxf(m[(r >> 1) & 3], s[r]), s[r + 1]); };
    auto vfrag = [&](unsigned base, int kb, int half) { const unsigned a = base + (32 * kb + 16 * half) * 48; return trfrag(a, a + 8 * 48); };
    auto maxblk = [&](float (&m)[4], const f32x16& s) {
#pragma unroll
      for (int r = 0; r < 16; r += 2) m[(r >> 1) & 3] = fmaxf(fmaxf(m[(r >> 1) & 3], s[r]), s[r + 1]); };
    u32x4 k0 = lds_u4(ka0), k1 = lds_u4(ka1);
    float mx[4] = {NEGBIG, NEGBIG, NEGBIG, NEGBIG};
#pragma unroll
    for (int kb = 0; kb < 9; ++kb) {
      u32x4 n0, n1;
      SB();
      qk0(sa[kb], kb, k0, qa0);
      SB();
      n0 = kb < 8 ? lds_u4(ka0 + (kb + 1) * 1536) : lds_u4(kb0);
      if (kb == 2) asm volatile("s_nop 3" : "+v"(sa[0]));
      if (kb >= 2) maxhalf(mx, sa[kb - 2], 0);
      SB();
      smfma_a(sa[kb], k1, qa1);
      SB();
      n1 = kb < 8 ? lds_u4(ka1 + (kb + 1) * 1536) : lds_u4(kb1);
      if (kb >= 2) { maxhalf(mx, sa[kb - 2], 1); wo_piece(2, kb - 2); }
      k0 = n0; k1 = n1;
    }
    SB();
    asm volatile("s_nop 7" : "+v"(sa[7]));
    maxblk(mx, sa[7]);
    sfence(sa[8]);
    maxblk(mx, sa[8]);
    const float mca = -SCORE_C * xmax32(fmaxf(fmaxf(mx[0], mx[1]), fmaxf(mx[2], mx[3])));
    f32x16 oa = {}, ob = {};
    s16x8 v0 = vfrag(vra, 0, 0), v1 = vfrag(vra, 0, 1);
#pragma unroll
    for (int r = 0; r < 4; ++r) mx[r] = NEGBIG;
    SB();
#pragma unroll
    for (int kb = 0; kb < 9; ++kb) {
      qk0(sb[kb], kb, k0, qb0);
      SB();
      if (kb < 8) k0 = lds_u4(kb0 + (kb + 1) * 1536);
      pfma(sa[kb], 0, mca); pexp();
      SB();
      smfma_a(sb[kb], k1, qb1);
      SB();
      if (kb < 8) k1 = lds_u4(kb1 + (kb + 1) * 1536);
      const u32x4 p0 = pcvt();
      pfma(sa[kb], 1, mca);
      SB();
      oa = mfma32(v0, p0, oa);
      SB();
      v0 = kb < 8 ? vfrag(vra, kb + 1, 0) : vfrag(vrb, 0, 0);
      pexp();
      const u32x4 p1 = pcvt();
      if (kb >= 1) maxhalf(mx, sb[kb - 1], 0);
      SB();
      oa = mfma32(v1, p1, oa);
      SB();
      v1 = kb < 8 ? vfrag(vra, kb + 1, 1) : vfrag(vrb, 0, 1);
      if (kb >= 1) maxhalf(mx, sb[kb - 1], 1);
      if (kb < 7) wo_piece(0, kb);
      if (kb < 5) touch(rgt, tgo, kb * 2 * 3456, L0 + L_DUMMY);
      if (kb >= 6) dma16(rx, xro[kb >= 6 ? kb - 6 : 0], 0, L0 + L3_XR + min(3 * wave + kb - 6, 9) * 1024);
      SB();
    }
    sfence(sb[8]);
    maxblk(mx, sb[8]);
    const float mcb = -SCORE_C * xmax32(fmaxf(fmaxf(mx[0], mx[1]), fmaxf(mx[2], mx[3])));
    SB();
    pfma(sb[0], 0, mcb); pexp();
    SB();
#pragma unroll
    for (int kb = 0; kb < 9; ++kb) {
      const u32x4 p0 = pcvt();
      pfma(sb[kb], 1, mcb);
      SB();
      ob = mfma32(v0, p0, ob);
      SB();
      if (kb < 8) v0 = vfrag(vrb, kb + 1, 0);
      pexp();
      const u32x4 p1 = pcvt();
      if (kb < 8) pfma(sb[kb + 1], 0, mcb);
      SB();
      ob = mfma32(v1, p1, ob);
      SB();
      if (kb < 8) { v1 = vfrag(vrb, kb + 1, 1); pexp(); }
      if (kb < 7) wo_piece(1, kb);
      SB();
    }
    const auto la = __builtin_amdgcn_permlane32_swap(__float_as_uint(oa[8]), __float_as_uint(oa[8]), false, false);
    const auto lb = __builtin_amdgcn_permlane32_swap(__float_as_uint(ob[8]), __float_as_uint(ob[8]), false, false);
    const float ia = rcpf_(__uint_as_float(la[1])), ib = rcpf_(__uint_as_float(lb[1]));
    const unsigned obase = WS_O + (4 * mb + wave) * T32_D + 5 * j * 512;
    u32x4 va, vb2, vc;
#pragma unroll
    for (int e = 0; e < 4; ++e) { va[e] = pk2(oa[2 * e] * ia, oa[2 * e + 1] * ia); vb2[e] = pk2(ob[2 * e] * ib, ob[2 * e + 1] * ib); }
    vc = (u32x4){pk2(oa[8] * ia, oa[9] * ia), pk2(oa[10] * ia, oa[11] * ia), pk2(ob[8] * ib, ob[9] * ib), pk2(ob[10] * ib, ob[11] * ib)};
    STAMP(23);
    st16_sc1(va, rws, obase + 16 * lane, 0);
    st16_sc1(vb2, rws, obase + 3 * 512 + 16 * lane, 0);
    st16_sc1(vc, rws, lane < 32 ? obase + 2 * 512 + 16 * lane : 0x40000000u, 0);
    publish(ws, FL_O, mb, j, wave, lane, tag);
  }
  BAR();
  STAMP(7);
  const int t16 = lane & 15, g4 = lane >> 4;
  const int tok3 = 128 * mbL + 64 * th + 16 * wave + t16;
  auto goff = [&](int k) { const int i = min(64 * k + lane, 119), kind = i / 40, r = i - 40 * kind; return WS_GRAN + ((kind == 0 ? 640 : kind == 1 ? 1280 : 1600) + 40 * nt + r) * 8; };
  u32x2 vg2[2];
#pragma unroll
  for (int i = 0; i < 7; ++i) wo_piece(3, i);
#pragma unroll
  for (int k = 0; k < 2; ++k) ald8_sc1(vg2[k], rws, goff(k), 0);
  const int nu = j < 12 ? 7 : 6, u0 = j < 12 ? 7 * j : 84 + 6 * (j - 12), uodd = u0 & 1;
  u32x2 fo, uvg[7];
  const unsigned uvl = WS_UV + ((lane >> 5) * 108 * 32 + (lane & 31)) * 8;
#pragma unroll
  for (int p = 0; p < 7; ++p) ald8_sc1(uvg[p], rws, uvl, (u0 + unit_of(p, uodd, nu)) * 256);
  ald8_sc1(fo, rws, flagoff(FL_O, mbL, lane), 0);
  const rsrc_t rgu = mkrsrc(wave < 2 ? wg : wu, D_ * F_ * 2);
  unsigned guo[7];
#pragma unroll
  for (int i = 0; i < 7; ++i) {
    const int isup = wave >> 1, idx = 64 * (7 * (wave & 1) + i) + lane, rq = idx / 56, rem = idx - 56 * rq;
    int q, p, hf;
    if (rem < 48) { q = (rem >> 2) & 3; const int c = (rem & 3) ^ (isup ? 2 : 0); p = 2 * (rem >> 4) + (c >> 1); hf = c & 1; } else { q = (rem - 48) >> 1; p = 6; hf = rem & 1; }
    guo[i] = opaque((4 * rq + q) * 3456 + 32 * (u0 + unit_of(p, uodd, nu)) + 16 * hf);
  }
  vmwait<0>();
  asm volatile("" : "+v"(vg2[0]), "+v"(vg2[1]), "+v"(fo));
  unsigned uvv[7];
  {
    bool ok = true;
#pragma unroll
    for (int p = 0; p < 7; ++p) { asm volatile("" : "+v"(uvg[p])); uvv[p] = uvg[p][0]; ok &= uvg[p][1] == tag; }
    if (wave == 1 && !__all(ok)) sweep<7>(ws, [&](int p) { return uvl + (u0 + unit_of(p, uodd, nu)) * 256; }, [&](int) { return true; }, tag, uvv);
  }
  if (wave == 0 && !flags_up(fo, tag)) wait_flags(ws, flagoff(FL_O, mbL, lane), true, tag);
  if (wave == 2) {
    unsigned gv[2] = {vg2[0][0], vg2[1][0]};
    if (!__all(vg2[0][1] == tag && vg2[1][1] == tag)) sweep<2>(ws, goff, [&](int) { return true; }, tag, gv);
#pragma unroll
    for (int k = 0; k < 2; ++k) if (k == 0 || lane < 56) {
      const int i = 64 * k + lane, kind = i / 40, r = i - 40 * kind;
      const int ct = r >> 3, p = (nt & 1) ? (ct == 0 ? 4 : ct - 1) : ct;
      f32x2 v = unpk(gv[k]);
      if (kind == 1) v = bf16r2(v + 1.0f);
      lds_st2f(L0 + L3_VEC + kind * 320 + p * 64 + (r & 7) * 8, v);
    }
  }
  if (wave == 3 && bid == 0) {
    unsigned fv[16];
    sweep<16>(ws, [&](int k) { return flagoff(FL_KV, k, lane); }, [&](int) { return true; }, tag, fv);
    if (lane == 0) __hip_atomic_store((GAS unsigned*)(ws + WS_EPOCH), tag, __ATOMIC_RELAXED, __HIP_MEMORY_SCOPE_AGENT);
  }
  STAMP(20);
  BAR();
  STAMP(8);

  const unsigned b80_0 = g4 * 1024 + ((lane & 15) >> 2) * 64 + (g4 & 1) * 32 + (lane & 3) * 8;
  const unsigned b80_1 = b80_0 ^ 32u;
  const unsigned b80_2 = 4096 + 8 * lane;
  struct B80 { unsigned a, b, c; };
  auto b80 = [&](unsigned kbase) { return B80{opaque(kbase + b80_0), opaque(kbase + b80_1), opaque(kbase + b80_2)}; };
  auto w80 = [&](const B80& B, int koff, int p) { const unsigned a = (p == 4 ? B.c : ((p & 1) ? B.b : B.a) + (p >> 1) * 256) + koff; return trfrag(a, a + 512); };

  u32x2 x1p[5];
  const int so4 = 0;
  auto gu_piece = [&](int st, int i) { dma16(rgu, guo[i], rot10(so4, st) * 64 * 3456, L0 + (st % 5) * L4_STAGE + (wave >> 1) * L4_U + (wave & 1) * 7168 + i * 1024); };
  {
    u32x4 of[20];
    const unsigned ofb = WS_O + (4 * mbL + 2 * th + (wave >> 1)) * T32_D + g4 * 512 + ((wave & 1) * 16 + t16) * 16;
    const Rot R3 = mkrot(rL, 5 * rL, 20, 2048);
    auto fo20 = [&](int s) { return R3.b[s < 5 ? 0 : s < 10 ? 1 : s < 15 ? 2 : 3] + s * 2048; };
#pragma unroll
    for (int s = 0; s < P3F0; ++s) ald16_sc1(of[s], rws, ofb, fo20(s));
    f32x4 acc[5];
    const B80 B0 = b80(L0 + L3_W0), B1 = b80(L0 + L3_W0 + 10 * 5120), B2 = b80(L0 + L3_W1);
    auto rd3 = [&](int s, int p) { return s < 10 ? w80(B0, s * 5120, p) : s < L3_NPRE ? w80(B1, (s - 10) * 5120, p) : w80(B2, (s - L3_NPRE) * 5120, p); };
    s16x8 w[3][5];
#pragma unroll
    for (int p = 0; p < 5; ++p) { w[0][p] = rd3(0, p); w[1][p] = rd3(1, p); }
    SB();
    static_for<0, 20>([&](auto SS) {
      constexpr int s = SS;
      vmwait<p3_wait_frag(s)>(of[s]);
      SB();
#pragma unroll
      for (int p = 0; p < 5; ++p) {
        if (s == 0) hmfma_0(acc[p], w[s % 3][p], of[s]); else hmfma_a(acc[p], w[s % 3][p], of[s]);
        SB();
        if (p == 0 && s + P3F0 < 20) ald16_sc1(of[s + P3F0 < 20 ? s + P3F0 : 0], rws, ofb, fo20(s + P3F0));
        if (s + 2 < 20) w[(s + 2) % 3][p] = rd3(s + 2, p);
        SB();
      }
    });
    asm volatile("s_nop 7" : "+v"(acc[0]), "+v"(acc[1]), "+v"(acc[2]), "+v"(acc[3]), "+v"(acc[4]));
    BAR();
    STAMP(9);
    f32x2 xv[5][2]; f32x2 s2 = {0.f, 0.f};
    u32x2 yp[5];
    const unsigned vecb = opaque(L0 + L3_VEC + g4 * 16), xrb = opaque(L0 + L3_XR + (16 * wave + t16) * 160 + g4 * 8);
    u32x2 xres[5];
#pragma unroll
    for (int p = 0; p < 5; ++p) xres[p] = *(LAS u32x2*)(size_t)(xrb + 32 * ct_of(p, nt & 1));
#pragma unroll
    for (int p = 0; p < 5; ++p) {
      const f32x4 ga = lds_f4(vecb + p * 64), sb1 = lds_f4(vecb + 320 + p * 64);
#pragma unroll
      for (int r = 0; r < 2; ++r) {
        const f32x2 at = bf16r2((f32x2){acc[p][2 * r], acc[p][2 * r + 1]});
        const f32x2 xs = unpk(xres[p][r]) + bf16r2(at * (f32x2){ga[2 * r], ga[2 * r + 1]});
        x1p[p][r] = pk2(xs.x, xs.y);
        xv[p][r] = unpk(x1p[p][r]);
        s2 += xv[p][r];
        const f32x2 y = xv[p][r] * (f32x2){sb1[2 * r], sb1[2 * r + 1]};
        yp[p][r] = pk2(y.x, y.y);
      }
    }
    const unsigned yb = WS_Y + (4 * mbL + 2 * th + (wave >> 1)) * T32_D + 10 * nt * 512 + ((wave & 1) * 16 + t16) * 16;
#pragma unroll
    for (int pp = 0; pp < 3; ++pp) {
      const int pa = 2 * pp, pb = pp < 2 ? 2 * pp + 1 : 4;
      const auto r0 = __builtin_amdgcn_permlane16_swap(yp[pa][0], yp[pb][0], false, false), r1 = __builtin_amdgcn_permlane16_swap(yp[pa][1], yp[pb][1], false, false);
      const int ct = (g4 & 1) ? ct_of(pb, nt & 1) : ct_of(pa, nt & 1);
      st16_sc1((u32x4){r0[0], r1[0], r0[1], r1[1]}, rws, (pp < 2 || !(g4 & 1)) ? yb + (2 * ct + (g4 >> 1)) * 512 : 0x40000000u, 0);
    }
    const float mt = xsum32(xsum16(s2.x + s2.y)) * (1.0f / 80);
    f32x2 m2 = {0.f, 0.f};
#pragma unroll
    for (int p = 0; p < 5; ++p)
#pragma unroll
      for (int r = 0; r < 2; ++r) { const f32x2 d = xv[p][r] - mt; m2 += d * d; }
    const float m2s = xsum32(xsum16(m2.x + m2.y));
    if (lane < 16) st_u64(ws, WS_STAT + (((mbL * 2 + th) * 8 + nt) * 64 + 16 * wave + t16) * 8, ((u64)__float_as_uint(m2s) << 32) | __float_as_uint(mt));
#pragma unroll
    for (int i = 0; i < 7; ++i) gu_piece(0, i);
    vmwait<7>();
    if (lane == 0) st_u64(ws, flagoff(FL_Y, mbL, 4 * jL + wave), flagval(tag));
  }
  BAR_RAW();
  STAMP(10);

  for (int st = 1; st < 4; ++st)
#pragma unroll
    for (int i = 0; i < 7; ++i) gu_piece(st, i);
  STAMP(19);
  u32x2 fy;
  ald8_sc1(fy, rws, flagoff(FL_Y, mb, lane), 0);
  if (wave == 1) {
#pragma unroll
    for (int p = 0; p < 7; ++p) lds_st1(L0 + L4_UV + (p * 64 + lane) * 4, uvv[p]);
  }
  const rsrc_t rwd = mkrsrc(wd, F_ * D_ * 2);
  unsigned wdg[8];
  {
    auto rm = [](int g, int t, int q) { return 16 * (g >> 1) + 8 * t + 4 * (g & 1) + q; };
    const unsigned b0 = w80_voff(0, lane, nt, rm), b1 = w80_voff(1, lane, nt, rm), b4 = w80_voff(4, lane, nt, rm);
#pragma unroll
    for (int i = 0; i < 8; ++i) {
      const int pc = min(8 * wave + i, 29), kl = pc / 5, pp = pc - 5 * kl;
      wdg[i] = opaque((pp == 4 ? b4 : (pp & 1) ? b1 : b0) + (kl * 32 + (pp == 4 ? 0 : 16 * (pp >> 1))) * 1280);
    }
  }
  auto wd_piece = [&](int G, int i) { const int g = G + goL; dma16(rwd, wdg[i], (g >= 9 ? g - 9 : g) * 6 * 32 * 1280, L0 + (G % 5) * 30720 + min(8 * wave + i, 29) * 1024); };
  const Rot R5 = mkrot(rD, 6 * goL, 54, 2048);
  auto fo54 = [&](int s) { return R5.b[s < 12 ? 0 : s < 30 ? 1 : s < 42 ? 2 : 3] + s * 2048; };
  vmwait<0>(); asm volatile("" : "+v"(fy));
  if (wave == 0 && !flags_up(fy, tag)) wait_flags(ws, flagoff(FL_Y, mb, lane), true, tag);
  STAMP(21);
  vmwait<0>();
  BAR();
  STAMP(11);
  u32x2 stv[4];
#pragma unroll
  for (int i = 0; i < 4; ++i) { const int T = 32 * wave + (lane & 31); stv[i] = ld8_sc1(rws, WS_STAT + (((mb * 2 + (T >> 6)) * 8 + 4 * (lane >> 5) + i) * 64 + (T & 63)) * 8, 0); }
  MEMFENCE();
  u32x4 yf[40];
  const unsigned yfo = WS_Y + (4 * mb + wave) * T32_D + 16 * lane;
  const Rot R4 = mkrot(0, 0, 40, 1024);
#pragma unroll
  for (int ks = 0; ks < P4F0; ++ks) ald16_sc1(yf[ks], rws, yfo, fo40(R4, ks));
  f32x16 acc4[7];
  {
    const int h = lane >> 5, blk = (lane >> 4) & 1, q = (lane & 15) >> 2;
    const unsigned c0 = L0 + blk * L4_U + 2 * h * 896 + q * 64 + blk * 32 + (lane & 3) * 8;
    const unsigned c2 = L0 + blk * L4_U + 2 * h * 896 + 768 + q * 32 + (lane & 3) * 8;
    unsigned cb0[5], cb1[5], cb2[5];
#pragma unroll
    for (int sl = 0; sl < 5; ++sl) { cb0[sl] = opaque(c0 + sl * L4_STAGE); cb1[sl] = opaque((c0 ^ 32u) + sl * L4_STAGE); cb2[sl] = opaque(c2 + sl * L4_STAGE); }
    auto readw = [&](int ks, int p) {
      const int sl = (ks >> 2) % 5;
      const unsigned a = (p == 6 ? cb2[sl] : ((p & 1) ? cb1[sl] : cb0[sl]) + (p >> 1) * 256) + (ks & 3) * 4 * 896;
      return trfrag(a, a + 896); };
    s16x8 w[3][7];
#pragma unroll
    for (int p = 0; p < 7; ++p) { w[0][p] = readw(0, p); w[1][p] = readw(1, p); }
    SB();
    static_for<0, 40>([&](auto KS) {
      constexpr int ks = KS, c = ks % 3, n = (ks + 2) % 3;
      if constexpr ((ks & 3) == 2) {
        constexpr int st = (ks + 2) / 4;
        if constexpr (st <= 9) STAMPN(30 + st - 1);
        if constexpr (st >= 4 && st <= 9) vmwait<p4_wait_stage(st)>();
        BAR_RAW();
      }
      vmwait<p4_wait_frag(ks)>(yf[ks]);
      SB();
#pragma unroll
      for (int p = 0; p < 7; ++p) {
        if (ks == 0) gmfma_0(acc4[p], w[c][p], yf[ks]); else gmfma_a(acc4[p], w[c][p], yf[ks]);
        SB();
        if (p == 1 && ks + P4F0 < 40) ald16_sc1(yf[ks + P4F0 < 40 ? ks + P4F0 : 0], rws, yfo, fo40(R4, ks + P4F0));
        if (p == 3 && p4_pieces(ks) > 0) gu_piece((ks + 14) / 4, 2 * ((ks + 14) % 4));
        if (p == 5 && p4_pieces(ks) > 1) gu_piece((ks + 14) / 4, 2 * ((ks + 14) % 4) + 1);
        if (p == 3 && p4_touch(ks) > 0) wd_piece(p4_wd_before(ks) / 8, p4_wd_before(ks) % 8);
        if (p == 5 && p4_touch(ks) > 1) wd_piece((p4_wd_before(ks) + 1) / 8, (p4_wd_before(ks) + 1) % 8);
        if (ks + 2 < 40) w[n][p] = readw(ks + 2, p);
        SB();
      }
    });
  }
  asm volatile("s_nop 11" : "+v"(acc4[0]), "+v"(acc4[1]), "+v"(acc4[2]), "+v"(acc4[3]), "+v"(acc4[4]), "+v"(acc4[5]), "+v"(acc4[6]));
  STAMP(12);
  {
    const int h = lane >> 5;
    float ms = 0.f;
#pragma unroll
    for (int i = 0; i < 4; ++i) ms += __uint_as_float(stv[i][0]);
    const float mean = xsum32(ms) * 0.125f;
    float m2 = 0.f;
#pragma unroll
    for (int i = 0; i < 4; ++i) { const float d = __uint_as_float(stv[i][0]) - mean; m2 += __uint_as_float(stv[i][1]) + 80.0f * d * d; }
    const float rs = rden_of(xsum32(m2) * (1.0f / D_)), nm = -rs * bf16r(mean);
    const unsigned ab = WS_ACT + (4 * mb + wave) * T32_F + 16 * lane, uvb = opaque(L0 + L4_UV + 16 * h);
#pragma unroll
    for (int p = 0; p < 7; ++p) {
      if (p < 6 || nu == 7) {
        const unsigned tb = uvb + p * 256;
        u32x4 ov;
#pragma unroll
        for (int half = 0; half < 2; ++half) {
          const f32x4 ug = lds_f4(tb + 32 * half), vg = lds_f4(tb + 64 + 32 * half), uu = lds_f4(tb + 128 + 32 * half), vu = lds_f4(tb + 192 + 32 * half);
#pragma unroll
          for (int r = 0; r < 2; ++r) {
            const f32x2 ag = {acc4[p][4 * half + 2 * r], acc4[p][4 * half + 2 * r + 1]}, au = {acc4[p][8 + 4 * half + 2 * r], acc4[p][8 + 4 * half + 2 * r + 1]};
            const f32x2 g = rs * ag + (nm * (f32x2){ug[2 * r], ug[2 * r + 1]} + (f32x2){vg[2 * r], vg[2 * r + 1]});
            const f32x2 u = rs * au + (nm * (f32x2){uu[2 * r], uu[2 * r + 1]} + (f32x2){vu[2 * r], vu[2 * r + 1]});
            const f32x2 e = g * (-LOG2E);
            const f32x2 den = (f32x2){exp2_(e.x), exp2_(e.y)} + 1.0f;
            const f32x2 a = (g * u) * (f32x2){rcpf_(den.x), rcpf_(den.y)};
            ov[2 * half + r] = pk2(a.x, a.y);
          }
        }
        st16_sc1(ov, rws, ab + (u0 + unit_of(p, uodd, nu)) * 1024, 0);
      }
    }
#pragma unroll
    for (int i = 4; i < 8; ++i) wd_piece(2, i);
#pragma unroll
    for (int i = 0; i < 3; ++i) wd_piece(3, i);
    vmwait<7>();
    if (lane == 0) st_u64(ws, flagoff(FL_ACT, mb, 4 * j + wave), flagval(tag));
  }
  BAR();
  STAMP(13);

#pragma unroll
  for (int i = 3; i < 8; ++i) wd_piece(3, i);
#pragma unroll
  for (int i = 0; i < 8; ++i) wd_piece(4, i);
  STAMP(22);
  if (wave == 0) wait_flags(ws, flagoff(FL_ACT, mbL, lane), true, tag);
  vmwait<0>();
  BAR();
  STAMP(14);
  {
    u32x4 af[54];
    const unsigned afb = WS_ACT + (4 * mbL + 2 * th + (wave >> 1)) * T32_F + g4 * 512 + ((wave & 1) * 16 + t16) * 16;
#pragma unroll
    for (int s = 0; s < P5F0; ++s) ald16_sc1(af[s], rws, afb, fo54(s));
    f32x4 acc[5];
    const B80 B5[3] = {b80(L0), b80(L0 + 61440), b80(L0 + 122880)};
    auto rd5 = [&](int s, int p) { const int sl = s % 30; return w80(B5[sl / 12], (sl % 12) * 5120, p); };
    s16x8 w[3][5];
#pragma unroll
    for (int p = 0; p < 5; ++p) { w[0][p] = rd5(0, p); w[1][p] = rd5(1, p); }
    SB();
    static_for<0, 54>([&](auto SS) {
      constexpr int s = SS;
      if constexpr (s % 6 == 4 && s + 2 < 54) {
        constexpr int g = (s + 2) / 6;
        STAMPN(40 + g - 1);
        if constexpr (g >= 5) vmwait<p5_wait_group(g)>();
        BAR_RAW();
      }
      vmwait<p5_wait_frag(s)>(af[s]);
      SB();
#pragma unroll
      for (int p = 0; p < 5; ++p) {
        if (s == 0) hmfma_0(acc[p], w[s % 3][p], af[s]); else hmfma_a(acc[p], w[s % 3][p], af[s]);
        SB();
        constexpr int r = (s + 20) % 6, i0 = r < 2 ? 2 * r : r + 2;
        if (p == 0 && s + P5F0 < 54) ald16_sc1(af[s + P5F0 < 54 ? s + P5F0 : 0], rws, afb, fo54(s + P5F0));
        if (p == 2 && p5_pieces(s) > 0) wd_piece((s + 20) / 6, i0);
        if (p == 4 && p5_pieces(s) > 1) wd_piece((s + 20) / 6, i0 + 1);
        if (s + 2 < 54) w[(s + 2) % 3][p] = rd5(s + 2, p);
        SB();
      }
    });
    asm volatile("s_nop 7" : "+v"(acc[0]), "+v"(acc[1]), "+v"(acc[2]), "+v"(acc[3]), "+v"(acc[4]));
    STAMP(15);
    u32x2 op[5];
    const unsigned gbb = opaque(L0 + L3_VEC + 640 + g4 * 16);
#pragma unroll
    for (int p = 0; p < 5; ++p) {
      const f32x4 gb = lds_f4(gbb + p * 64);
#pragma unroll
      for (int r = 0; r < 2; ++r) {
        const f32x2 dn = bf16r2((f32x2){acc[p][2 * r], acc[p][2 * r + 1]});
        const f32x2 o = unpk(x1p[p][r]) + bf16r2(dn * (f32x2){gb[2 * r], gb[2 * r + 1]});
        op[p][r] = pk2(o.x, o.y);
      }
    }
    const rsrc_t ro = mkrsrc(out, S_ * D_ * 2);
#pragma unroll
    for (int pp = 0; pp < 3; ++pp) {
      const int pa = 2 * pp, pb = pp < 2 ? 2 * pp + 1 : 4;
      const auto r0 = __builtin_amdgcn_permlane16_swap(op[pa][0], op[pb][0], false, false), r1 = __builtin_amdgcn_permlane16_swap(op[pa][1], op[pb][1], false, false);
      const int ct = (g4 & 1) ? ct_of(pb, nt & 1) : ct_of(pa, nt & 1);
      __builtin_amdgcn_raw_buffer_store_b128((u32x4){r0[0], r1[0], r0[1], r1[1]}, ro, (pp < 2 || !(g4 & 1)) ? tok3 * 1280 + 160 * nt + 32 * ct + 16 * (g4 >> 1) : 0x40000000u, 0, 16);
    }
  }
  STAMP(16);
  STAMP_FLUSH();
  asm volatile(".Ladaln_code_end:");
}

extern "C" void kernel_launch(void* const* d_in, const int* in_sizes, int n_in, void* d_out, int out_size, void* d_ws, size_t ws_size,
                              hipStream_t stream) {
  (void)in_sizes; (void)n_in; (void)out_size; (void)ws_size;
  adaln_block_kernel<<<256, 256, 0, stream>>>((const char*)d_in[0], (const char*)d_in[1], (const char*)d_in[2], (const char*)d_in[4], (const char*)d_in[5],
                                              (const char*)d_in[6], (const char*)d_in[7], (const char*)d_in[8], (char*)d_out, (char*)d_ws);
}
```

```cpp
#include <hip/hip_runtime.h>
#include <stdint.h>
#include <type_traits>

#define LAS __attribute__((address_space(3)))
#define GAS __attribute__((address_space(1)))
#define DI __device__ __forceinline__
typedef float f32x2 __attribute__((ext_vector_type(2)));
typedef float f32x4 __attribute__((ext_vector_type(4)));
typedef float f32x16 __attribute__((ext_vector_type(16)));
typedef unsigned u32x2 __attribute__((ext_vector_type(2)));
typedef unsigned u32x4 __attribute__((ext_vector_type(4)));
typedef short s16x4 __attribute__((ext_vector_type(4)));
typedef short s16x8 __attribute__((ext_vector_type(8)));
typedef __bf16 bf16x2 __attribute__((ext_vector_type(2)));
typedef __bf16 bf16x8 __attribute__((ext_vector_type(8)));
typedef unsigned long long u64;
typedef __amdgpu_buffer_rsrc_t rsrc_t;

constexpr int S_ = 2048, D_ = 640, F_ = 1728;
constexpr int T32_D = 80 * 512;
constexpr int T32_F = 216 * 512;

constexpr int WS_EPOCH = 0;
constexpr int WS_TMO = 256;
constexpr int WS_FLAG = 29 << 20;
constexpr int FL_XN = 0, FL_KV = 1, FL_O = 2, FL_Y = 3, FL_ACT = 4;
constexpr int WS_GRAN = 256 * 1024;
constexpr int WS_UV = 320 * 1024;
constexpr int WS_STAT = 512 * 1024;
constexpr int WS_XN = 1 << 20;
constexpr int WS_KV = 4 << 20;
constexpr int WS_O = 12 << 20;
constexpr int WS_Y = 16 << 20;
constexpr int WS_ACT = 20 << 20;
constexpr int WS_USED = 30 << 20;

constexpr int LDS_BYTES = 163840;
constexpr int L1_W = 0;
constexpr int L0_TB = 122880;
constexpr int L0_JW = 133120;
constexpr int L1_VS = 153600, L1_VA = 154880;
constexpr int L0_SC = 158720;
constexpr int L2_KV = 0;
constexpr int KV_IMG = 384 * 48;
constexpr int L3_W0 = 4 * KV_IMG;
constexpr int L3_NPRE = 15;
constexpr int L3_W1 = 0;
constexpr int L2_JW = 73728, L2_JV = 94208;
constexpr int L3_JP = 161984;
constexpr int L3_XR = 150528;
constexpr int L_DUMMY = 162816;
constexpr int L3_VEC = 161024;
constexpr int L4_STAGE = 30720, L4_U = 14464;
constexpr int L4_UV = 153600;
static_assert(L3_XR + 64 * 160 <= L3_VEC && L3_W0 + L3_NPRE * 5120 <= L3_XR, "P3 residual rows");
static_assert(L3_W0 + L3_NPRE * 5120 <= L3_VEC && L3_VEC + 960 <= LDS_BYTES, "P3 LDS map");
static_assert(30 * 5120 <= L3_VEC, "P5 LDS map");
static_assert(L4_UV + 1792 <= L3_VEC, "P4 LDS map");
static_assert(L3_VEC + 960 <= L3_JP && L3_JP + 512 <= L_DUMMY && L_DUMMY + 256 <= LDS_BYTES, "small LDS areas");

constexpr float LOG2E = 1.4426950408889634f;
constexpr float SCORE_C = 0.22360679774997896f * 1.4426950408889634f;
constexpr float NEGBIG = -1.0e30f;
constexpr unsigned SPIN_CAP = 1u << 15;

#define SB() __builtin_amdgcn_sched_barrier(0)
#define BAR() do { asm volatile("s_waitcnt lgkmcnt(0)" ::: "memory"); __builtin_amdgcn_s_barrier(); asm volatile("" ::: "memory"); } while (0)
#define BAR_RAW() do { asm volatile("" ::: "memory"); __builtin_amdgcn_s_barrier(); asm volatile("" ::: "memory"); } while (0)
#define MEMFENCE() asm volatile("" ::: "memory")
template <int N> DI void vmwait() { asm volatile("s_waitcnt vmcnt(%0)" :: "n"(N) : "memory"); }
template <int N> DI void vmwait(u32x4& d) { asm volatile("s_waitcnt vmcnt(%1)" : "+v"(d) : "n"(N) : "memory"); }

DI unsigned pk2(float a, float b) { bf16x2 v = __builtin_convertvector((f32x2){a, b}, bf16x2); return __builtin_bit_cast(unsigned, v); }
DI float blo(unsigned v) { return __uint_as_float(v << 16); }
DI float bhi(unsigned v) { return __uint_as_float(v & 0xffff0000u); }
DI float bf16r(float a) { return blo(pk2(a, a)); }
DI f32x2 bf16r2(f32x2 a) { const unsigned p = pk2(a.x, a.y); return (f32x2){blo(p), bhi(p)}; }
DI f32x2 unpk(unsigned p) { return (f32x2){blo(p), bhi(p)}; }
DI float rcpf_(float x) { return __builtin_amdgcn_rcpf(x); }
DI float exp2_(float x) { return __builtin_amdgcn_exp2f(x); }
DI float siluf(float x) { return x * rcpf_(1.0f + exp2_(-LOG2E * x)); }
DI float rden_of(float var) { return rcpf_(bf16r(__builtin_amdgcn_sqrtf(bf16r(bf16r(var) + 1e-5f)))); }
template <int CTRL> DI float dpp_add(float x) { return x + __int_as_float(__builtin_amdgcn_update_dpp(0, __float_as_int(x), CTRL, 0xf, 0xf, true)); }
DI float sum16(float x) { x = dpp_add<0xB1>(x); x = dpp_add<0x4E>(x); x = dpp_add<0x141>(x); return dpp_add<0x140>(x); }
DI float xsum16(float x) { auto r = __builtin_amdgcn_permlane16_swap(__float_as_uint(x), __float_as_uint(x), false, false); return __uint_as_float(r[0]) + __uint_as_float(r[1]); }
DI float xsum32(float x) { auto r = __builtin_amdgcn_permlane32_swap(__float_as_uint(x), __float_as_uint(x), false, false); return __uint_as_float(r[0]) + __uint_as_float(r[1]); }
DI float xmax32(float x) { auto r = __builtin_amdgcn_permlane32_swap(__float_as_uint(x), __float_as_uint(x), false, false); return fmaxf(__uint_as_float(r[0]), __uint_as_float(r[1])); }
DI rsrc_t mkrsrc(const void* p, unsigned bytes) { return __builtin_amdgcn_make_buffer_rsrc((void*)p, (short)0, (int)bytes, 0x00020000); }
DI void st16_sc1(u32x4 v, rsrc_t r, unsigned voff, unsigned soff) { __builtin_amdgcn_raw_buffer_store_b128(v, r, voff, soff, 16); }
DI void ald16_sc1(u32x4& d, rsrc_t r, unsigned voff, unsigned soff) {
  asm volatile("buffer_load_dwordx4 %0, %1, %2, %3 offen sc1" : "=v"(d) : "v"(voff), "s"(r), "s"(soff) : "memory");
}
DI void ald16(u32x4& d, rsrc_t r, unsigned voff, unsigned soff) {
  asm volatile("buffer_load_dwordx4 %0, %1, %2, %3 offen" : "=v"(d) : "v"(voff), "s"(r), "s"(soff) : "memory");
}
DI void ald4(unsigned& d, rsrc_t r, unsigned voff, unsigned soff) {
  asm volatile("buffer_load_dword %0, %1, %2, %3 offen" : "=v"(d) : "v"(voff), "s"(r), "s"(soff) : "memory");
}
DI void ald8_sc1(u32x2& d, rsrc_t r, unsigned voff, unsigned soff) {
  asm volatile("buffer_load_dwordx2 %0, %1, %2, %3 offen sc1" : "=v"(d) : "v"(voff), "s"(r), "s"(soff) : "memory");
}
DI void touch(rsrc_t r, unsigned voff, unsigned soff, unsigned ldsaddr) {
  asm volatile("s_mov_b32 m0, %0\n\ts_nop 0\n\tbuffer_load_dword %1, %2, %3 offen lds" :: "s"(ldsaddr), "v"(voff), "s"(r), "s"(soff) : "memory");
}
DI void dma16_sc1(rsrc_t r, unsigned voff, unsigned soff, unsigned ldsaddr) {
  asm volatile("s_mov_b32 m0, %0\n\ts_nop 0\n\tbuffer_load_dwordx4 %1, %2, %3 offen sc1 lds" :: "s"(ldsaddr), "v"(voff), "s"(r), "s"(soff) : "memory");
}
DI void dma16(rsrc_t r, unsigned voff, unsigned soff, unsigned ldsaddr) {
  asm volatile("s_mov_b32 m0, %0\n\ts_nop 0\n\tbuffer_load_dwordx4 %1, %2, %3 offen lds" :: "s"(ldsaddr), "v"(voff), "s"(r), "s"(soff) : "memory");
}
DI u64 ld_u64(const char* ws, unsigned off) { return __hip_atomic_load((GAS u64*)(ws + off), __ATOMIC_RELAXED, __HIP_MEMORY_SCOPE_AGENT); }
DI void st_u64(char* ws, unsigned off, u64 v) { __hip_atomic_store((GAS u64*)(ws + off), v, __ATOMIC_RELAXED, __HIP_MEMORY_SCOPE_AGENT); }
DI u64 flagval(unsigned tag) { return ((u64)tag << 32) | (u64)(~tag); }
DI unsigned flagoff(int kind, int mb, int jw) { return WS_FLAG + ((kind * 16 + mb) * 64 + jw) * 128; }
DI void publish(char* ws, int kind, int mb, int j, int wave, int lane, unsigned tag) {
  vmwait<0>();
  if (lane == 0) st_u64(ws, flagoff(kind, mb, 4 * j + wave), flagval(tag));
}
DI bool spin_fail(unsigned& spins, char* ws, unsigned tag) {
  __builtin_amdgcn_s_sleep(1);
  if (++spins > SPIN_CAP) { __hip_atomic_store((GAS unsigned*)(ws + WS_TMO), tag, __ATOMIC_RELAXED, __HIP_MEMORY_SCOPE_AGENT); return true; }
  return (spins & 127u) == 0u && (unsigned)__builtin_amdgcn_readfirstlane(__hip_atomic_load((GAS unsigned*)(ws + WS_TMO), __ATOMIC_RELAXED, __HIP_MEMORY_SCOPE_AGENT)) == tag;
}
DI bool flags_up(const u32x2& f, unsigned tag) { return __all(f[0] == ~tag && f[1] == tag); }
DI void wait_flags(char* ws, unsigned off, bool on, unsigned tag) {
  const u64 want = flagval(tag);
#pragma clang loop unroll(disable)
  for (unsigned spins = 0;;) {
    u64 v = want;
    if (on) v = ld_u64(ws, off);
    if (__all(v == want)) break;
    if (spin_fail(spins, ws, tag)) break;
  }
  MEMFENCE();
}
template <int N, class OFF, class ON> DI void sweep(char* ws, OFF off, ON on, unsigned tag, unsigned (&val)[N]) {
#pragma clang loop unroll(disable)
  for (unsigned spins = 0;;) {
    bool ok = true;
#pragma unroll
    for (int k = 0; k < N; ++k) { u64 g = (u64)tag << 32; if (on(k)) g = ld_u64(ws, off(k)); val[k] = (unsigned)g; ok &= (unsigned)(g >> 32) == tag; }
    if (__all(ok)) break;
    if (spin_fail(spins, ws, tag)) break;
  }
  MEMFENCE();
}
DI s16x8 trfrag(unsigned a0, unsigned a1) {
  const s16x4 lo = __builtin_amdgcn_ds_read_tr16_b64_v4i16((LAS s16x4*)(size_t)a0);
  const s16x4 hi = __builtin_amdgcn_ds_read_tr16_b64_v4i16((LAS s16x4*)(size_t)a1);
  return __builtin_shufflevector(lo, hi, 0, 1, 2, 3, 4, 5, 6, 7);
}
DI f32x4 lds_f4(unsigned a) { return *(LAS f32x4*)(size_t)a; }
DI u32x4 lds_u4(unsigned a) { return *(LAS u32x4*)(size_t)a; }
DI void lds_st4(unsigned a, u32x4 v) { *(LAS u32x4*)(size_t)a = v; }
DI void lds_st1(unsigned a, unsigned v) { *(LAS unsigned*)(size_t)a = v; }
DI void lds_st2f(unsigned a, f32x2 v) { *(LAS f32x2*)(size_t)a = v; }
DI unsigned opaque(unsigned v) { asm volatile("" : "+v"(v)); return v; }
DI unsigned sopaque(unsigned v) { asm volatile("" : "+s"(v)); return v; }
struct Rot { unsigned b[4]; };
DI Rot mkrot(int r, int shift, int N, int step) {
  const unsigned a = shift * step, w = N * step;
  return Rot{{sopaque(a), sopaque(a - (r >= 3 ? w : 0)), sopaque(a - (r >= 2 ? w : 0)), sopaque(a - (r >= 1 ? w : 0))}};
}
DI f32x16 mfma32(s16x8 a, u32x4 b, f32x16 c) { return __builtin_amdgcn_mfma_f32_32x32x16_bf16(__builtin_bit_cast(bf16x8, a), __builtin_bit_cast(bf16x8, b), c, 0, 0, 0); }
DI f32x4 mfma16(s16x8 a, u32x4 b, f32x4 c) { return __builtin_amdgcn_mfma_f32_16x16x32_bf16(__builtin_bit_cast(bf16x8, a), __builtin_bit_cast(bf16x8, b), c, 0, 0, 0); }
DI void smfma_c(f32x16& d, u32x4 a, u32x4 b, const f32x16& c) { asm("v_mfma_f32_32x32x16_bf16 %0, %1, %2, %3" : "=&v"(d) : "v"(a), "v"(b), "v"(c)); }
DI void smfma_0(f32x16& d, u32x4 a, u32x4 b) { asm("v_mfma_f32_32x32x16_bf16 %0, %1, %2, 0" : "=&v"(d) : "v"(a), "v"(b)); }
DI void smfma_a(f32x16& d, u32x4 a, u32x4 b) { asm("v_mfma_f32_32x32x16_bf16 %0, %1, %2, %0" : "+v"(d) : "v"(a), "v"(b)); }
DI void gmfma_0(f32x16& d, s16x8 a, u32x4 b) { asm("v_mfma_f32_32x32x16_bf16 %0, %1, %2, 0" : "=&v"(d) : "a"(a), "v"(b)); }
DI void gmfma_a(f32x16& d, s16x8 a, u32x4 b) { asm("v_mfma_f32_32x32x16_bf16 %0, %1, %2, %0" : "+v"(d) : "a"(a), "v"(b)); }
DI void hmfma_0(f32x4& d, s16x8 a, u32x4 b) { asm("v_mfma_f32_16x16x32_bf16 %0, %1, %2, 0" : "=&v"(d) : "a"(a), "v"(b)); }
DI void hmfma_a(f32x4& d, s16x8 a, u32x4 b) { asm("v_mfma_f32_16x16x32_bf16 %0, %1, %2, %0" : "+v"(d) : "a"(a), "v"(b)); }
DI void sfence(f32x16& d) { asm volatile("s_nop 11" : "+v"(d)); }
DI float fmix_lo(unsigned x, unsigned s, unsigned a) { float r; asm("v_fma_mix_f32 %0, %1, %2, %3 op_sel_hi:[1,1,1]" : "=v"(r) : "v"(x), "v"(s), "v"(a)); return r; }
DI float fmix_hi(unsigned x, unsigned s, unsigned a) { float r; asm("v_fma_mix_f32 %0, %1, %2, %3 op_sel:[1,1,1] op_sel_hi:[1,1,1]" : "=v"(r) : "v"(x), "v"(s), "v"(a)); return r; }
DI unsigned pkh(float a, float b) { return __builtin_bit_cast(unsigned, __builtin_amdgcn_cvt_pkrtz(a, b)); }


template <int NS> DI void gemv16_issue(rsrc_t rw, unsigned rowstride, unsigned srcbyte, unsigned ldsw, int lane) {
  const unsigned vo = (8 * ((lane >> 3) & 3) + 4 * (lane >> 5) + ((lane >> 1) & 3)) * rowstride + 16 * (lane & 1);
#pragma unroll 5
  for (int s = 0; s < NS; ++s) dma16(rw, vo, s * 32 * rowstride + srcbyte, ldsw + s * 1024);
}
template <int NS> DI f32x4 gemv16_run(unsigned ldsw, unsigned ldsv, int vmask, int lane) {
  f32x4 acc0 = {}, acc1 = {};
  const unsigned wa = opaque(ldsw + 8 * lane), va = opaque(ldsv + (lane & vmask) * 1280 + (lane >> 4) * 16);
  s16x8 w[3]; u32x4 v[3];
#pragma unroll
  for (int s = 0; s < 2; ++s) { w[s] = trfrag(wa + s * 1024, wa + s * 1024 + 512); v[s] = lds_u4(va + s * 64); }
  SB();
#pragma unroll
  for (int s = 0; s < NS; ++s) {
    if (s + 2 < NS) { w[(s + 2) % 3] = trfrag(wa + (s + 2) * 1024, wa + (s + 2) * 1024 + 512); v[(s + 2) % 3] = lds_u4(va + (s + 2) * 64); }
    SB();
    if (s & 1) acc1 = mfma16(w[s % 3], v[s % 3], acc1); else acc0 = mfma16(w[s % 3], v[s % 3], acc0);
    SB();
  }
  return acc0 + acc1;
}

DI int ct_of(int p, int odd) { return odd ? (p < 4 ? p + 1 : 0) : p; }
DI int unit_of(int p, int odd, int nu) { const int u = odd ? (p < 6 ? p + 1 : 0) : p; return u < nu ? u : nu - 1; }

template <class RM> DI unsigned w80_voff(int piece, int lane, int nt, RM rowmap) {
  const int odd = nt & 1;
  int g, t, q, p, hf;
  if (piece < 4) { g = piece; t = lane >> 5; const int su = (lane >> 4) & 1; q = (lane >> 2) & 3; const int c = (lane & 3) ^ (2 * (piece & 1)); p = 2 * su + (c >> 1); hf = c & 1; }
  else { t = lane >> 5; g = (lane >> 3) & 3; q = (lane >> 1) & 3; p = 4; hf = lane & 1; }
  return rowmap(g, t, q) * 1280 + 160 * nt + 32 * ct_of(p, odd) + 16 * hf;
}

template <class OPS> constexpr int vm_sum(OPS ops, int lo, int hi) { int n = 0; for (int k = lo; k <= hi; ++k) if (k >= 0) n += ops(k); return n; }
constexpr int P1F0 = 10;
constexpr int p1_pieces(int k) { return k < 24 ? 1 : 0; }
constexpr int p1_ops(int k) { return (k < 40 - P1F0 ? 1 : 0) + p1_pieces(k) + (k >= 30 && k < 38 ? 1 : 0); }
constexpr int p1_wait_frag(int f) {
  return f < P1F0 ? 8 + vm_sum(p1_ops, 0, f - 3) : p1_pieces(f - P1F0) + vm_sum(p1_ops, f - P1F0 + 1, f - 3);
}
constexpr int p1_wait_stage(int st) { return st == 2 ? 4 + vm_sum(p1_ops, 0, 5) : st == 3 ? vm_sum(p1_ops, 0, 9) : vm_sum(p1_ops, 4 * st - 12, 4 * st - 3); }
constexpr int P4F0 = 8;
constexpr int p4_pieces(int k) { return k >= 2 && k <= 25 ? ((k + 14) % 4 == 3 ? 1 : 2) : 0; }
constexpr int p4_wd_before(int k) { return k <= 26 ? 0 : k == 27 ? 2 : k == 28 ? 3 : k == 29 ? 5 : k == 30 ? 6 : k == 31 ? 8 : k == 32 ? 9 : k == 33 ? 11 : k == 34 ? 12 : k == 35 ? 14 : k <= 40 ? 16 + (k - 36) : 20; }
constexpr int p4_touch(int k) { return p4_wd_before(k + 1) - p4_wd_before(k); }
constexpr int p4_ops(int k) { return (k < 40 - P4F0 ? 1 : 0) + p4_pieces(k) + p4_touch(k); }
constexpr int p4_wait_frag(int ks) { return ks < P4F0 ? (P4F0 - 1 - ks) + vm_sum(p4_ops, 0, ks - 1) : p4_pieces(ks - P4F0) + p4_touch(ks - P4F0) + vm_sum(p4_ops, ks - P4F0 + 1, ks - 1); }
constexpr int p4_wait_stage(int st) { return vm_sum(p4_ops, 4 * st - 10, 4 * st - 3); }
constexpr int P5F0 = 12;
constexpr int p5_pieces(int k) { return k >= 10 && k <= 33 ? ((k + 20) % 6 < 2 ? 2 : 1) : 0; }
constexpr int p5_ops(int k) { return (k < 54 - P5F0 ? 1 : 0) + p5_pieces(k); }
constexpr int p5_wait_frag(int s) { return s < P5F0 ? (P5F0 - 1 - s) + vm_sum(p5_ops, 0, s - 1) : p5_pieces(s - P5F0) + vm_sum(p5_ops, s - P5F0 + 1, s - 1); }
constexpr int p5_wait_group(int g) { return vm_sum(p5_ops, 6 * g - 14, 6 * g - 3); }
constexpr int P3F0 = 6;
constexpr int p3_ops(int k) { return k < 20 - P3F0 ? 1 : 0; }
constexpr int p3_wait_frag(int s) { return s < P3F0 ? (P3F0 - 1 - s) + vm_sum(p3_ops, 0, s - 1) : vm_sum(p3_ops, s - P3F0 + 1, s - 1); }

template <int I, int N, class Fn> DI void static_for(Fn&& f) { if constexpr (I < N) { f(std::integral_constant<int, I>{}); static_for<I + 1, N>(f); } }

__global__ __launch_bounds__(256, 1) void adaln_block_kernel(const char* __restrict__ x, const char* __restrict__ wqkv, const char* __restrict__ wo,
                                                             const char* __restrict__ wg, const char* __restrict__ wu, const char* __restrict__ wd,
                                                             const char* __restrict__ cvec, const char* __restrict__ wmod, char* out, char* ws) {
  __shared__ __attribute__((aligned(16))) char smem[LDS_BYTES];
  const unsigned L0 = (unsigned)(size_t)(LAS char*)smem;
  const int tid = threadIdx.x, lane = tid & 63;
  const int wave = __builtin_amdgcn_readfirstlane(tid >> 6);
  const int bid = blockIdx.x, xcd = bid & 7, ii = bid >> 3;
  const int mbL = 2 * xcd + (ii >> 4), jL = ii & 15;
  const int mb = 4 * (xcd >> 1) + (ii >> 3), j = 8 * (xcd & 1) + (ii & 7);
  const int rW = mb & 3, rL = 0, rD = 2 * (mbL & 1) + (jL & 1);
  const int soW = (5 * rW) >> 1;
  const int goL = rD == 3 ? 7 : 2 * rD;
  const rsrc_t rx = mkrsrc(x, S_ * D_ * 2), rws = mkrsrc(ws, WS_USED);

  unsigned epoch, tag;
  const rsrc_t rwq = mkrsrc(wqkv, D_ * 3 * D_ * 2);
  unsigned wqo[4];
  auto rot10 = [&](int so, int st) { const int g = st + so; return g >= 10 ? g - 10 : g; };
  const int so1 = (int)sopaque(soW);
  auto wq_piece = [&](int st, int i) { dma16(rwq, wqo[i], rot10(so1, st) * 64 * 3840, L0 + L1_W + (st * 15 + min(4 * wave + i, 14)) * 1024); };
  auto fo40 = [&](const Rot& R, int ks) { return R.b[ks < 12 ? 0 : ks < 20 ? 1 : ks < 32 ? 2 : 3] + ks * 1024; };
  auto wq_issue = [&](int st) {
#pragma unroll
    for (int i = 0; i < 4; ++i) wq_piece(st, i);
  };
  {
    const int c32 = lane & 31, tok = 128 * mbL + 8 * jL + 2 * wave + (lane >> 5);
    u32x4 xv[3], c4 = {}; unsigned c1 = 0;
    asm volatile("buffer_load_dword %0, %1, %2, 0 offen sc1" : "=v"(epoch) : "v"(WS_EPOCH), "s"(rws) : "memory");
    ald16(xv[0], rx, tok * 1280 + c32 * 16, 0);
    ald16(xv[1], rx, tok * 1280 + c32 * 16 + 512, 0);
    ald16(xv[2], rx, c32 < 16 ? tok * 1280 + c32 * 16 + 1024 : 0x40000000u, 0);
    if (wave == 3) {
      u64 pc; int len;
      asm volatile("s_getpc_b64 %0\n.Ladaln_code_begin:\n\ts_mov_b32 %1, .Ladaln_code_end-.Ladaln_code_begin" : "=s"(pc), "=s"(len));
      len = len > 0 && len < (1 << 18) ? len : 0;
      touch(mkrsrc((const void*)pc, len), (ii * 40 + min(lane, 39)) * 64, 0, L0 + L_DUMMY);
    }
    const int munit = ii < 10 ? 10 * xcd + ii : 80 + 20 * xcd + (ii - 10);
    const bool mjob = wave == 0 && ii < 30;
    if (mjob) {
      const rsrc_t rc = mkrsrc(cvec, D_ * 2);
      ald16(c4, rc, 20 * lane, 0); ald4(c1, rc, 20 * lane + 16, 0);
    }
#pragma unroll
    for (int i = 0; i < 4; ++i) {
      const int idx = 64 * min(4 * wave + i, 14) + lane, rq = idx / 60, rem = idx - 60 * rq;
      int q, ch;
      if (rem < 48) { q = (rem >> 2) & 3; ch = 4 * (rem >> 4) + (rem & 3); } else { const int r2 = rem - 48; q = r2 / 3; ch = 12 + r2 - 3 * q; }
      const int seg = ch / 5;
      wqo[i] = (4 * rq + q) * 3840 + seg * 1280 + 80 * j + (ch - 5 * seg) * 16;
    }
    if (mjob) {
      gemv16_issue<20>(mkrsrc(wmod, D_ * 6 * D_ * 2), 7680, 32 * munit, L0 + L0_JW, lane);
      vmwait<20>();
    } else {
      vmwait<0>();
    }
    asm volatile("" : "+v"(xv[0]), "+v"(xv[1]), "+v"(xv[2]), "+v"(c4), "+v"(c1), "+v"(epoch));
    tag = (unsigned)__builtin_amdgcn_readfirstlane(epoch) + 1u;
    f32x2 s2 = {0.f, 0.f};
#pragma unroll
    for (int i = 0; i < 3; ++i)
#pragma unroll
      for (int e = 0; e < 4; ++e) s2 += unpk(xv[i][e]);
    const float mean = xsum16(sum16(s2.x + s2.y)) * (1.0f / D_);
    f32x2 q2 = {0.f, 0.f}, q3 = {0.f, 0.f};
#pragma unroll
    for (int e = 0; e < 4; ++e) {
      const f32x2 d0 = unpk(xv[0][e]) - mean, d1 = unpk(xv[1][e]) - mean, d2 = unpk(xv[2][e]) - mean;
      q2 += d0 * d0; q2 += d1 * d1; q3 += d2 * d2;
    }
    const float var = xsum16(sum16(q2.x + q2.y + (c32 < 16 ? q3.x + q3.y : 0.f))) * (1.0f / D_);
    const float mq = bf16r(mean), rden = rden_of(var);
#pragma unroll
    for (int i = 0; i < 3; ++i) {
      u32x4 o;
#pragma unroll
      for (int e = 0; e < 4; ++e) { const f32x2 d = bf16r2(bf16r2(unpk(xv[i][e]) - mq) * rden); o[e] = pkh(d.x, d.y); }
      const int slot = c32 + 32 * i;
      if (i < 2 || c32 < 16) lds_st4(L0 + L0_TB + slot * 128 + (((2 * wave + (lane >> 5)) ^ (slot & 7)) * 16), o);
    }
    BAR();
#pragma unroll
    for (int u = 0; u < 3; ++u) {
      const int i = 256 * u + tid, slot = i >> 3, tk = (i & 7) ^ (slot & 7);
      const unsigned voff = (u < 2 || tid < 128) ? WS_XN + (4 * mbL + (jL >> 2)) * T32_D + slot * 512 + (8 * (jL & 3) + tk) * 16 : 0x40000000u;
      st16_sc1(lds_u4(L0 + L0_TB + min(i, 639) * 16), rws, voff, 0);
    }
    wq_issue(0);
    vmwait<4>();
    if (lane == 0) st_u64(ws, flagoff(FL_XN, mbL, 4 * jL + wave), flagval(tag));
    if (mjob) {
      const unsigned cc[5] = {c4[0], c4[1], c4[2], c4[3], c1};
#pragma unroll
      for (int r = 0; r < 5; ++r) lds_st1(L0 + L0_SC + 20 * lane + 4 * r, pk2(siluf(blo(cc[r])), siluf(bhi(cc[r]))));
      asm volatile("s_waitcnt lgkmcnt(0)" ::: "memory");
      const f32x4 d = gemv16_run<20>(L0 + L0_JW, L0 + L0_SC, 0, lane);
      const unsigned pv = (lane & 1) ? pk2(d[2], d[3]) : pk2(d[0], d[1]);
      if ((lane & 15) < 2) st_u64(ws, WS_GRAN + (8 * munit + 2 * (lane >> 4) + (lane & 1)) * 8, ((u64)tag << 32) | pv);
    }
    wq_issue(1);
  }
  if (wave == 0) wait_flags(ws, flagoff(FL_XN, mb, lane), true, tag);
  BAR_RAW();
  u32x4 xf[40];
  const unsigned xfo = WS_XN + (4 * mb + wave) * T32_D + 16 * lane;
  const Rot R1 = mkrot(rW, 4 * soW, 40, 1024);
  {
    const int g0 = 160 * wave + lane;
    u32x2 ga[3];
#pragma unroll
    for (int ks = 0; ks < P1F0; ++ks) ald16_sc1(xf[ks], rws, xfo, fo40(R1, ks));
#pragma unroll
    for (int k = 0; k < 3; ++k) ald8_sc1(ga[k], rws, WS_GRAN + (k < 2 || lane < 32 ? g0 + 64 * k : g0) * 8, 0);
    wq_issue(2); wq_issue(3);
    vmwait<8>();
    asm volatile("" : "+v"(ga[0]), "+v"(ga[1]), "+v"(ga[2]));
    unsigned gv[3] = {ga[0][0], ga[1][0], ga[2][0]};
    if (!__all(ga[0][1] == tag && ga[1][1] == tag && ga[2][1] == tag))
      sweep<3>(ws, [&](int k) { return WS_GRAN + (g0 + 64 * k) * 8; }, [&](int k) { return k < 2 || lane < 32; }, tag, gv);
#pragma unroll
    for (int k = 0; k < 3; ++k) if (k < 2 || lane < 32) {
      const int g = g0 + 64 * k;
      f32x2 v = unpk(gv[k]);
      if (g >= 320) v = bf16r2(v + 1.0f);
      const int gp = g >= 320 ? g - 320 : g;
      int kp = (gp >> 3) - 4 * soW; kp += kp < 0 ? 40 : 0;
      lds_st1(L0 + (g >= 320 ? L1_VS : L1_VA) + kp * 32 + (gp & 7) * 4, pkh(v.x, v.y));
    }
  }
  BAR();

  const int jidx = 32 * (xcd >> 1) + ii, jper = (xcd & 1) ? 52 : 56;
  const bool ujob = jidx < 2 * jper;
  const int jmat = jidx >= jper, junit = ((xcd & 1) ? 56 : 0) + jidx - jmat * jper;
  auto jgi = [&](int k) { const int i = 64 * k + lane; return i < 80 ? 1280 + 80 * wave + i : i < 160 ? 960 + 80 * wave + i - 80 : 1280; };
  const rsrc_t rjw = mkrsrc(jmat ? wu : wg, D_ * F_ * 2);
  const unsigned jvo = (8 * ((lane >> 3) & 3) + 4 * (lane >> 5) + ((lane >> 1) & 3)) * 3456 + 16 * (lane & 1);
  const unsigned jso = sopaque(32 * (ujob ? junit : 0) + wave * 160 * 3456);
  u32x2 jg[3];
  f32x16 acc1[4];
  {
    const int h = lane >> 5;
    const unsigned wb = opaque(L0 + L1_W + 2 * h * 960 + ((lane & 15) >> 2) * 64 + ((lane >> 4) & 1) * 32 + (lane & 3) * 8);
    const unsigned wb3 = opaque(L0 + L1_W + 2 * h * 960 + 768 + ((lane & 15) >> 2) * 48 + ((lane >> 4) & 1) * 32 + (lane & 3) * 8);
    const unsigned wbh = opaque(wb + 61440), wb3h = opaque(wb3 + 61440), wbg = opaque(wb + 122880), wb3g = opaque(wb3 + 122880);
    const unsigned vb = opaque(L0 + L1_VS + h * 16);
    s16x8 w[3][4]; u32x4 hf[2], vs[2], va[2]; float ft[2][8];
    auto readw = [&](int ks, int b) {
      const int kl = ks % 16; const unsigned base = ks < 16 ? (b < 3 ? wb : wb3) : ks < 32 ? (b < 3 ? wbh : wb3h) : (b < 3 ? wbg : wb3g);
      const unsigned a = base + (b < 3 ? b * 256 : 0) + kl * 3840; return trfrag(a, a + 960); };
    auto reads = [&](int ks) { vs[ks & 1] = lds_u4(vb + ks * 32); };
    auto reada = [&](int ks) { va[ks & 1] = lds_u4(vb + 1280 + ks * 32); };
    auto fstage = [&](int ks, int e) {
      ft[ks & 1][2 * e] = fmix_lo(xf[ks][e], vs[ks & 1][e], va[ks & 1][e]);
      ft[ks & 1][2 * e + 1] = fmix_hi(xf[ks][e], vs[ks & 1][e], va[ks & 1][e]); };
    auto cstage = [&](int ks, int e) { hf[ks & 1][e] = pk2(ft[ks & 1][2 * e], ft[ks & 1][2 * e + 1]); };
#pragma unroll
    for (int b = 0; b < 4; ++b) { w[0][b] = readw(0, b); w[1][b] = readw(1, b); }
    reads(0); reada(0); reads(1); reada(1);
    vmwait<p1_wait_frag(0)>(xf[0]);
#pragma unroll
    for (int e = 0; e < 4; ++e) fstage(0, e);
#pragma unroll
    for (int e = 0; e < 4; ++e) cstage(0, e);
    vmwait<p1_wait_frag(1)>(xf[1]);
#pragma unroll
    for (int e = 0; e < 4; ++e) fstage(1, e);
    reads(2); reada(2);
    asm volatile("s_nop 1" : "+v"(hf[0]));
    SB();
    static_for<0, 40>([&](auto KS) {
      constexpr int ks = KS, c = ks & 1, wc = ks % 3, wn = (ks + 2) % 3;
      constexpr bool needf = ks + 2 < 40, needs = (ks & 3) == 2 && (ks + 2) / 4 >= 2 && ks + 2 < 40;
      constexpr int nf = needf ? p1_wait_frag(ks + 2) : 63, ns = needs ? p1_wait_stage((ks + 2) / 4) : 63;
      if constexpr (needs) { vmwait<(nf < ns ? nf : ns)>(); BAR_RAW(); }
      if constexpr (needf) vmwait<nf>(xf[ks + 2]);
      SB();
      if constexpr (ks == 0) gmfma_0(acc1[0], w[wc][0], hf[c]); else gmfma_a(acc1[0], w[wc][0], hf[c]);
      SB();
      if constexpr (ks + 2 < 40) w[wn][0] = readw(ks + 2, 0);
      if constexpr (ks + 1 < 40) { cstage(ks + 1, 0); cstage(ks + 1, 1); }
      if constexpr (ks + 3 < 40) reads(ks + 3);
      SB();
      if constexpr (ks == 0) gmfma_0(acc1[1], w[wc][1], hf[c]); else gmfma_a(acc1[1], w[wc][1], hf[c]);
      SB();
      if constexpr (ks + P1F0 < 40) ald16_sc1(xf[ks + P1F0], rws, xfo, fo40(R1, ks + P1F0));
      if constexpr (ks + 2 < 40) w[wn][1] = readw(ks + 2, 1);
      if constexpr (ks + 1 < 40) { cstage(ks + 1, 2); cstage(ks + 1, 3); }
      if constexpr (ks + 3 < 40) reada(ks + 3);
      SB();
      if constexpr (ks == 0) gmfma_0(acc1[2], w[wc][2], hf[c]); else gmfma_a(acc1[2], w[wc][2], hf[c]);
      SB();
      if constexpr (ks + 2 < 40) w[wn][2] = readw(ks + 2, 2);
      if constexpr (ks + 2 < 40) { fstage(ks + 2, 0); fstage(ks + 2, 1); }
      SB();
      if constexpr (ks == 0) gmfma_0(acc1[3], w[wc][3], hf[c]); else gmfma_a(acc1[3], w[wc][3], hf[c]);
      SB();
      if constexpr (ks < 24) wq_piece(ks / 4 + 4, ks & 3);
      if constexpr (ks >= 30 && ks < 35) dma16(rjw, jvo, jso + (ks - 30) * 32 * 3456, L0 + L2_JW + wave * 5120 + (ks - 30) * 1024);
      if constexpr (ks >= 35 && ks < 38) ald8_sc1(jg[ks - 35], rws, WS_GRAN + jgi(ks - 35) * 8, 0);
      if constexpr (ks + 2 < 40) w[wn][3] = readw(ks + 2, 3);
      if constexpr (ks + 2 < 40) { fstage(ks + 2, 2); fstage(ks + 2, 3); }
      SB();
    });
  }
  asm volatile("s_nop 11" : "+v"(acc1[0]), "+v"(acc1[1]), "+v"(acc1[2]), "+v"(acc1[3]));

  u32x4 qa0, qa1, qb0, qb1;
  {
    const int h = lane >> 5, t = lane & 31;
    u32x2 P[15];
#pragma unroll
    for (int i = 0; i < 15; ++i) { const f32x16& a = acc1[i >> 2]; const int r = 4 * (i & 3); P[i] = (u32x2){pk2(a[r], a[r + 1]), pk2(a[r + 2], a[r + 3])}; }
    const u32x2 one = {0x00003f80u, 0u}, zero = {0u, 0u};
    auto cat = [](u32x2 a, u32x2 b) { return (u32x4){a[0], a[1], b[0], b[1]}; };
    auto sel = [&](u32x2 a1, u32x2 a0) { return (u32x2){h ? a1[0] : a0[0], h ? a1[1] : a0[1]}; };
    const unsigned row = L0 + L2_KV + (256 + 32 * wave + t) * 48;
    lds_st4(row + 16 * h, cat(P[5], P[6]));
    lds_st4(row + KV_IMG + 16 * (h ^ 1), cat(sel(P[7], P[8]), sel(P[8], P[9])));
    lds_st4(row + h * KV_IMG + 32, cat(sel(P[9], P[7]), zero));
    lds_st4(row + 2 * KV_IMG + 16 * h, cat(P[10], P[11]));
    lds_st4(row + 3 * KV_IMG + 16 * (h ^ 1), cat(sel(P[12], P[13]), sel(P[13], P[14])));
    lds_st4(row + (2 + h) * KV_IMG + 32, cat(sel(P[14], P[12]), one));
    asm volatile("s_waitcnt lgkmcnt(0)" ::: "memory");
#pragma unroll
    for (int u = 0; u < 6; ++u) {
      const int i = 64 * u + lane, a = i / 96, o = (i - 96 * a) * 16;
      const u32x4 v = lds_u4(L0 + L2_KV + a * KV_IMG + (256 + 32 * wave) * 48 + o);
      st16_sc1(v, rws, WS_KV + ((mb * 16 + j) * 4 + a) * 6144 + wave * 1536 + o, 0);
    }
    qa0 = cat(P[0], P[1]);
    qa1 = cat(sel(zero, P[2]), sel(zero, one));
    qb0 = cat(sel(P[2], P[3]), sel(P[3], P[4]));
    qb1 = cat(sel(P[4], zero), sel(one, zero));
    publish(ws, FL_KV, mb, j, wave, lane, tag);
  }
  asm volatile("" : "+v"(jg[0]), "+v"(jg[1]), "+v"(jg[2]));
  {
    unsigned gv[3] = {jg[0][0], jg[1][0], jg[2][0]};
    if (!__all(jg[0][1] == tag && jg[1][1] == tag && jg[2][1] == tag))
      sweep<3>(ws, [&](int k) { return WS_GRAN + jgi(k) * 8; }, [&](int) { return true; }, tag, gv);
#pragma unroll
    for (int k = 0; k < 3; ++k) if (k < 2 || lane < 32) {
      const int i = 64 * k + lane;
      unsigned v = gv[k];
      if (i < 80) { const f32x2 t = unpk(v) + 1.0f; v = pk2(t.x, t.y); }
      lds_st1(L0 + L2_JV + (i < 80 ? 320 * wave + 4 * i : 1280 + 320 * wave + 4 * (i - 80)), v);
    }
    asm volatile("s_waitcnt lgkmcnt(0)" ::: "memory");
    const f32x4 d = gemv16_run<5>(L0 + L2_JW + wave * 5120, L0 + L2_JV + wave * 320, 1, lane);
    if ((lane & 15) < 2) lds_st4(L0 + L3_JP + wave * 128 + (2 * (lane >> 4) + (lane & 1)) * 16, __builtin_bit_cast(u32x4, d));
  }

  {
    const int blk = wave >> 1, src = mb - 2 + blk;
    if (src >= 0) {
      wait_flags(ws, flagoff(FL_KV, src, 4 * j + (lane & 3)), lane < 4, tag);
#pragma unroll
      for (int i = 0; i < 12; ++i) {
        const int a = 2 * (wave & 1) + i / 6, sub = i % 6;
        dma16_sc1(rws, 16 * lane, WS_KV + ((src * 16 + j) * 4 + a) * 6144 + sub * 1024, L0 + L2_KV + a * KV_IMG + blk * 6144 + sub * 1024);
      }
    } else {
#pragma unroll
      for (int i = 0; i < 12; ++i) {
        const int a = 2 * (wave & 1) + i / 6, o = (i % 6) * 1024 + 16 * lane;
        lds_st4(L0 + L2_KV + a * KV_IMG + blk * 6144 + o, (u32x4){0u, 0u, ((o % 48) == 32 && a < 2) ? 0x0000f149u : 0u, 0u});
      }
    }
  }
  const int th = jL & 1, nt = jL >> 1;
  const rsrc_t rwo = mkrsrc(wo, D_ * D_ * 2);
  unsigned woo[7], wol[7];
#pragma unroll
  for (int i = 0; i < 7; ++i) {
    const int pi = min(wave + 4 * i, 24), kc = pi / 5, pc = pi - 5 * kc;
    auto rowmap = [&](int g, int t, int q) { const int k = 32 * kc + 8 * g + 4 * t + q, hp = k / 40, r = k - 40 * hp; return 40 * hp + (r < 20 ? r : r < 24 ? r + 16 : r - 4); };
    woo[i] = opaque(w80_voff(pc, lane, nt, rowmap));
    wol[i] = pi * 1024;
  }
  auto wo_piece = [&](int gam, int i) { dma16(rwo, woo[i], ((gam + rL) & 3) * 160 * 1280, L0 + (gam < 3 ? L3_W0 + gam * 25600 : L3_W1) + wol[i]); };
  vmwait<0>();
  BAR();
  if (ujob && wave == 1) {
    const unsigned pa = L0 + L3_JP + (2 * (lane >> 4) + (lane & 1)) * 16;
    const f32x4 d = (lds_f4(pa) + lds_f4(pa + 128)) + (lds_f4(pa + 256) + lds_f4(pa + 384));
    if ((lane & 15) < 2) {
#pragma unroll
      for (int r = 0; r < 4; ++r) st_u64(ws, WS_UV + ((jmat * 108 + junit) * 32 + (lane & 15) * 16 + 4 * (lane >> 4) + r) * 8, ((u64)tag << 32) | __float_as_uint(d[r]));
    }
  }


  {
    const rsrc_t rgt = mkrsrc(wave < 2 ? wg : wu, D_ * F_ * 2);
    const rsrc_t rdt = mkrsrc(wd, F_ * D_ * 2);
    const unsigned tdo = (135 * bid + min(64 * wave + lane, 134)) * 64;
    unsigned xro[3];
#pragma unroll
    for (int i = 0; i < 3; ++i) { const int idx = 64 * min(3 * wave + i, 9) + lane, row = idx / 10; xro[i] = (128 * mbL + 64 * th + row) * 1280 + 160 * nt + (idx - 10 * row) * 16; }
    const unsigned tgo = (20 * ii + 10 * (wave & 1) + (lane >> 5)) * 3456 + (xcd & 1) * 1792 + 64 * min(lane & 31, (xcd & 1) ? 25 : 27);
    const int h = lane >> 5, t = lane & 31;
    f32x16 mfirst, mdiag;
#pragma unroll
    for (int r = 0; r < 16; ++r) { const int key = (r & 3) + 8 * (r >> 2) + 4 * h; mfirst[r] = key > t ? 0.f : NEGBIG; mdiag[r] = key > t ? NEGBIG : 0.f; }
    const unsigned krow = L0 + L2_KV + (32 * wave + t) * 48;
    const unsigned ka0 = opaque(krow + 16 * h), ka1 = opaque(krow + 32), kb0 = opaque(krow + KV_IMG + 16 * (h ^ 1)), kb1 = opaque(krow + KV_IMG + 32);
    const unsigned vrow = L0 + L2_KV + 2 * KV_IMG + (32 * wave + 4 * h + ((lane & 15) >> 2)) * 48 + ((lane >> 4) & 1) * 32 + (lane & 3) * 8;
    const unsigned vra = opaque(vrow), vrb = opaque(vrow + KV_IMG);
    f32x16 sa[9], sb[9];
    float e[8];
    auto pfma = [&](const f32x16& s, int half, float mc) {
#pragma unroll
      for (int i = 0; i < 8; i += 2) {
        const f32x2 r = __builtin_elementwise_fma((f32x2){s[8 * half + i], s[8 * half + i + 1]}, (f32x2){SCORE_C, SCORE_C}, (f32x2){mc, mc});
        e[i] = r.x; e[i + 1] = r.y;
      } };
    auto pexp = [&]() {
#pragma unroll
      for (int i = 0; i < 8; ++i) e[i] = exp2_(e[i]); };
    auto pcvt = [&]() { return (u32x4){pk2(e[0], e[1]), pk2(e[2], e[3]), pk2(e[4], e[5]), pk2(e[6], e[7])}; };
    auto qk0 = [&](f32x16& s, int kb, u32x4 a0, u32x4 q0) { if (kb == 0) smfma_c(s, a0, q0, mfirst); else if (kb == 8) smfma_c(s, a0, q0, mdiag); else smfma_0(s, a0, q0); };
    auto maxhalf = [&](float (&m)[4], const f32x16& s, int half) {
#pragma unroll
      for (int r = 8 * half; r < 8 * half + 8; r += 2) m[(r >> 1) & 3] = fmaxf(fmaxf(m[(r >> 1) & 3], s[r]), s[r + 1]); };
    auto vfrag = [&](unsigned base, int kb, int half) { const unsigned a = base + (32 * kb + 16 * half) * 48; return trfrag(a, a + 8 * 48); };
    auto maxblk = [&](float (&m)[4], const f32x16& s) {
#pragma unroll
      for (int r = 0; r < 16; r += 2) m[(r >> 1) & 3] = fmaxf(fmaxf(m[(r >> 1) & 3], s[r]), s[r + 1]); };
    u32x4 k0 = lds_u4(ka0), k1 = lds_u4(ka1);
    float mx[4] = {NEGBIG, NEGBIG, NEGBIG, NEGBIG};
#pragma unroll
    for (int kb = 0; kb < 9; ++kb) {
      u32x4 n0, n1;
      SB();
      qk0(sa[kb], kb, k0, qa0);
      SB();
      n0 = kb < 8 ? lds_u4(ka0 + (kb + 1) * 1536) : lds_u4(kb0);
      if (kb == 2) asm volatile("s_nop 3" : "+v"(sa[0]));
      if (kb >= 2) maxhalf(mx, sa[kb - 2], 0);
      SB();
      smfma_a(sa[kb], k1, qa1);
      SB();
      n1 = kb < 8 ? lds_u4(ka1 + (kb + 1) * 1536) : lds_u4(kb1);
      if (kb >= 2) { maxhalf(mx, sa[kb - 2], 1); wo_piece(2, kb - 2); }
      k0 = n0; k1 = n1;
    }
    SB();
    asm volatile("s_nop 7" : "+v"(sa[7]));
    maxblk(mx, sa[7]);
    sfence(sa[8]);
    maxblk(mx, sa[8]);
    const float mca = -SCORE_C * xmax32(fmaxf(fmaxf(mx[0], mx[1]), fmaxf(mx[2], mx[3])));
    f32x16 oa = {}, ob = {};
    s16x8 v0 = vfrag(vra, 0, 0), v1 = vfrag(vra, 0, 1);
#pragma unroll
    for (int r = 0; r < 4; ++r) mx[r] = NEGBIG;
    SB();
#pragma unroll
    for (int kb = 0; kb < 9; ++kb) {
      qk0(sb[kb], kb, k0, qb0);
      SB();
      if (kb < 8) k0 = lds_u4(kb0 + (kb + 1) * 1536);
      pfma(sa[kb], 0, mca); pexp();
      SB();
      smfma_a(sb[kb], k1, qb1);
      SB();
      if (kb < 8) k1 = lds_u4(kb1 + (kb + 1) * 1536);
      const u32x4 p0 = pcvt();
      pfma(sa[kb], 1, mca);
      SB();
      oa = mfma32(v0, p0, oa);
      SB();
      v0 = kb < 8 ? vfrag(vra, kb + 1, 0) : vfrag(vrb, 0, 0);
      pexp();
      const u32x4 p1 = pcvt();
      if (kb >= 1) maxhalf(mx, sb[kb - 1], 0);
      SB();
      oa = mfma32(v1, p1, oa);
      SB();
      v1 = kb < 8 ? vfrag(vra, kb + 1, 1) : vfrag(vrb, 0, 1);
      if (kb >= 1) maxhalf(mx, sb[kb - 1], 1);
      if (kb < 7) wo_piece(0, kb);
      if (kb < 5) touch(rgt, tgo, kb * 2 * 3456, L0 + L_DUMMY);
      if (kb == 5) touch(rdt, tdo, 0, L0 + L_DUMMY);
      if (kb >= 6) dma16(rx, xro[kb >= 6 ? kb - 6 : 0], 0, L0 + L3_XR + min(3 * wave + kb - 6, 9) * 1024);
      SB();
    }
    sfence(sb[8]);
    maxblk(mx, sb[8]);
    const float mcb = -SCORE_C * xmax32(fmaxf(fmaxf(mx[0], mx[1]), fmaxf(mx[2], mx[3])));
    SB();
    pfma(sb[0], 0, mcb); pexp();
    SB();
#pragma unroll
    for (int kb = 0; kb < 9; ++kb) {
      const u32x4 p0 = pcvt();
      pfma(sb[kb], 1, mcb);
      SB();
      ob = mfma32(v0, p0, ob);
      SB();
      if (kb < 8) v0 = vfrag(vrb, kb + 1, 0);
      pexp();
      const u32x4 p1 = pcvt();
      if (kb < 8) pfma(sb[kb + 1], 0, mcb);
      SB();
      ob = mfma32(v1, p1, ob);
      SB();
      if (kb < 8) { v1 = vfrag(vrb, kb + 1, 1); pexp(); }
      if (kb < 7) wo_piece(1, kb);
      SB();
    }
    const auto la = __builtin_amdgcn_permlane32_swap(__float_as_uint(oa[8]), __float_as_uint(oa[8]), false, false);
    const auto lb = __builtin_amdgcn_permlane32_swap(__float_as_uint(ob[8]), __float_as_uint(ob[8]), false, false);
    const float ia = rcpf_(__uint_as_float(la[1])), ib = rcpf_(__uint_as_float(lb[1]));
    const unsigned obase = WS_O + (4 * mb + wave) * T32_D + 5 * j * 512;
    u32x4 va, vb2, vc;
#pragma unroll
    for (int e = 0; e < 4; ++e) { va[e] = pk2(oa[2 * e] * ia, oa[2 * e + 1] * ia); vb2[e] = pk2(ob[2 * e] * ib, ob[2 * e + 1] * ib); }
    vc = (u32x4){pk2(oa[8] * ia, oa[9] * ia), pk2(oa[10] * ia, oa[11] * ia), pk2(ob[8] * ib, ob[9] * ib), pk2(ob[10] * ib, ob[11] * ib)};
    st16_sc1(va, rws, obase + 16 * lane, 0);
    st16_sc1(vb2, rws, obase + 3 * 512 + 16 * lane, 0);
    st16_sc1(vc, rws, lane < 32 ? obase + 2 * 512 + 16 * lane : 0x40000000u, 0);
    publish(ws, FL_O, mb, j, wave, lane, tag);
  }
  BAR();
  const int t16 = lane & 15, g4 = lane >> 4;
  const int tok3 = 128 * mbL + 64 * th + 16 * wave + t16;
  auto goff = [&](int k) { const int i = min(64 * k + lane, 119), kind = i / 40, r = i - 40 * kind; return WS_GRAN + ((kind == 0 ? 640 : kind == 1 ? 1280 : 1600) + 40 * nt + r) * 8; };
  u32x2 vg2[2];
#pragma unroll
  for (int i = 0; i < 7; ++i) wo_piece(3, i);
#pragma unroll
  for (int k = 0; k < 2; ++k) ald8_sc1(vg2[k], rws, goff(k), 0);
  const int nu = j < 12 ? 7 : 6, u0 = j < 12 ? 7 * j : 84 + 6 * (j - 12), uodd = u0 & 1;
  u32x2 fo, uvg[7];
  const unsigned uvl = WS_UV + ((lane >> 5) * 108 * 32 + (lane & 31)) * 8;
#pragma unroll
  for (int p = 0; p < 7; ++p) ald8_sc1(uvg[p], rws, uvl, (u0 + unit_of(p, uodd, nu)) * 256);
  ald8_sc1(fo, rws, flagoff(FL_O, mbL, lane), 0);
  const rsrc_t rgu = mkrsrc(wave < 2 ? wg : wu, D_ * F_ * 2);
  unsigned guo[7];
#pragma unroll
  for (int i = 0; i < 7; ++i) {
    const int isup = wave >> 1, idx = 64 * (7 * (wave & 1) + i) + lane, rq = idx / 56, rem = idx - 56 * rq;
    int q, p, hf;
    if (rem < 48) { q = (rem >> 2) & 3; const int c = (rem & 3) ^ (isup ? 2 : 0); p = 2 * (rem >> 4) + (c >> 1); hf = c & 1; } else { q = (rem - 48) >> 1; p = 6; hf = rem & 1; }
    guo[i] = opaque((4 * rq + q) * 3456 + 32 * (u0 + unit_of(p, uodd, nu)) + 16 * hf);
  }
  vmwait<0>();
  asm volatile("" : "+v"(vg2[0]), "+v"(vg2[1]), "+v"(fo));
  unsigned uvv[7];
  {
    bool ok = true;
#pragma unroll
    for (int p = 0; p < 7; ++p) { asm volatile("" : "+v"(uvg[p])); uvv[p] = uvg[p][0]; ok &= uvg[p][1] == tag; }
    if (wave == 1 && !__all(ok)) sweep<7>(ws, [&](int p) { return uvl + (u0 + unit_of(p, uodd, nu)) * 256; }, [&](int) { return true; }, tag, uvv);
  }
  if (wave == 0 && !flags_up(fo, tag)) wait_flags(ws, flagoff(FL_O, mbL, lane), true, tag);
  if (wave == 2) {
    unsigned gv[2] = {vg2[0][0], vg2[1][0]};
    if (!__all(vg2[0][1] == tag && vg2[1][1] == tag)) sweep<2>(ws, goff, [&](int) { return true; }, tag, gv);
#pragma unroll
    for (int k = 0; k < 2; ++k) if (k == 0 || lane < 56) {
      const int i = 64 * k + lane, kind = i / 40, r = i - 40 * kind;
      const int ct = r >> 3, p = (nt & 1) ? (ct == 0 ? 4 : ct - 1) : ct;
      f32x2 v = unpk(gv[k]);
      if (kind == 1) v = bf16r2(v + 1.0f);
      lds_st2f(L0 + L3_VEC + kind * 320 + p * 64 + (r & 7) * 8, v);
    }
  }
  if (wave == 3 && bid == 0) {
    unsigned fv[16];
    sweep<16>(ws, [&](int k) { return flagoff(FL_KV, k, lane); }, [&](int) { return true; }, tag, fv);
    if (lane == 0) __hip_atomic_store((GAS unsigned*)(ws + WS_EPOCH), tag, __ATOMIC_RELAXED, __HIP_MEMORY_SCOPE_AGENT);
  }
  BAR();

  const unsigned b80_0 = g4 * 1024 + ((lane & 15) >> 2) * 64 + (g4 & 1) * 32 + (lane & 3) * 8;
  const unsigned b80_1 = b80_0 ^ 32u;
  const unsigned b80_2 = 4096 + 8 * lane;
  struct B80 { unsigned a, b, c; };
  auto b80 = [&](unsigned kbase) { return B80{opaque(kbase + b80_0), opaque(kbase + b80_1), opaque(kbase + b80_2)}; };
  auto w80 = [&](const B80& B, int koff, int p) { const unsigned a = (p == 4 ? B.c : ((p & 1) ? B.b : B.a) + (p >> 1) * 256) + koff; return trfrag(a, a + 512); };

  u32x2 x1p[5];
  const int so4 = 0;
  auto gu_piece = [&](int st, int i) { dma16(rgu, guo[i], rot10(so4, st) * 64 * 3456, L0 + (st % 5) * L4_STAGE + (wave >> 1) * L4_U + (wave & 1) * 7168 + i * 1024); };
  {
    u32x4 of[20];
    const unsigned ofb = WS_O + (4 * mbL + 2 * th + (wave >> 1)) * T32_D + g4 * 512 + ((wave & 1) * 16 + t16) * 16;
    const Rot R3 = mkrot(rL, 5 * rL, 20, 2048);
    auto fo20 = [&](int s) { return R3.b[s < 5 ? 0 : s < 10 ? 1 : s < 15 ? 2 : 3] + s * 2048; };
#pragma unroll
    for (int s = 0; s < P3F0; ++s) ald16_sc1(of[s], rws, ofb, fo20(s));
    f32x4 acc[5];
    const B80 B0 = b80(L0 + L3_W0), B1 = b80(L0 + L3_W0 + 10 * 5120), B2 = b80(L0 + L3_W1);
    auto rd3 = [&](int s, int p) { return s < 10 ? w80(B0, s * 5120, p) : s < L3_NPRE ? w80(B1, (s - 10) * 5120, p) : w80(B2, (s - L3_NPRE) * 5120, p); };
    s16x8 w[3][5];
#pragma unroll
    for (int p = 0; p < 5; ++p) { w[0][p] = rd3(0, p); w[1][p] = rd3(1, p); }
    SB();
    static_for<0, 20>([&](auto SS) {
      constexpr int s = SS;
      vmwait<p3_wait_frag(s)>(of[s]);
      SB();
#pragma unroll
      for (int p = 0; p < 5; ++p) {
        if (s == 0) hmfma_0(acc[p], w[s % 3][p], of[s]); else hmfma_a(acc[p], w[s % 3][p], of[s]);
        SB();
        if (p == 0 && s + P3F0 < 20) ald16_sc1(of[s + P3F0 < 20 ? s + P3F0 : 0], rws, ofb, fo20(s + P3F0));
        if (s + 2 < 20) w[(s + 2) % 3][p] = rd3(s + 2, p);
        SB();
      }
    });
    asm volatile("s_nop 7" : "+v"(acc[0]), "+v"(acc[1]), "+v"(acc[2]), "+v"(acc[3]), "+v"(acc[4]));
    BAR();
    f32x2 xv[5][2]; f32x2 s2 = {0.f, 0.f};
    u32x2 yp[5];
    const unsigned vecb = opaque(L0 + L3_VEC + g4 * 16), xrb = opaque(L0 + L3_XR + (16 * wave + t16) * 160 + g4 * 8);
    u32x2 xres[5];
#pragma unroll
    for (int p = 0; p < 5; ++p) xres[p] = *(LAS u32x2*)(size_t)(xrb + 32 * ct_of(p, nt & 1));
#pragma unroll
    for (int p = 0; p < 5; ++p) {
      const f32x4 ga = lds_f4(vecb + p * 64), sb1 = lds_f4(vecb + 320 + p * 64);
#pragma unroll
      for (int r = 0; r < 2; ++r) {
        const f32x2 at = bf16r2((f32x2){acc[p][2 * r], acc[p][2 * r + 1]});
        const f32x2 xs = unpk(xres[p][r]) + bf16r2(at * (f32x2){ga[2 * r], ga[2 * r + 1]});
        x1p[p][r] = pk2(xs.x, xs.y);
        xv[p][r] = unpk(x1p[p][r]);
        s2 += xv[p][r];
        const f32x2 y = xv[p][r] * (f32x2){sb1[2 * r], sb1[2 * r + 1]};
        yp[p][r] = pk2(y.x, y.y);
      }
    }
    const unsigned yb = WS_Y + (4 * mbL + 2 * th + (wave >> 1)) * T32_D + 10 * nt * 512 + ((wave & 1) * 16 + t16) * 16;
#pragma unroll
    for (int pp = 0; pp < 3; ++pp) {
      const int pa = 2 * pp, pb = pp < 2 ? 2 * pp + 1 : 4;
      const auto r0 = __builtin_amdgcn_permlane16_swap(yp[pa][0], yp[pb][0], false, false), r1 = __builtin_amdgcn_permlane16_swap(yp[pa][1], yp[pb][1], false, false);
      const int ct = (g4 & 1) ? ct_of(pb, nt & 1) : ct_of(pa, nt & 1);
      st16_sc1((u32x4){r0[0], r1[0], r0[1], r1[1]}, rws, (pp < 2 || !(g4 & 1)) ? yb + (2 * ct + (g4 >> 1)) * 512 : 0x40000000u, 0);
    }
    const float mt = xsum32(xsum16(s2.x + s2.y)) * (1.0f / 80);
    f32x2 m2 = {0.f, 0.f};
#pragma unroll
    for (int p = 0; p < 5; ++p)
#pragma unroll
      for (int r = 0; r < 2; ++r) { const f32x2 d = xv[p][r] - mt; m2 += d * d; }
    const float m2s = xsum32(xsum16(m2.x + m2.y));
    if (lane < 16) st_u64(ws, WS_STAT + (((mbL * 2 + th) * 8 + nt) * 64 + 16 * wave + t16) * 8, ((u64)__float_as_uint(m2s) << 32) | __float_as_uint(mt));
#pragma unroll
    for (int i = 0; i < 7; ++i) gu_piece(0, i);
    vmwait<7>();
    if (lane == 0) st_u64(ws, flagoff(FL_Y, mbL, 4 * jL + wave), flagval(tag));
  }
  BAR_RAW();

  for (int st = 1; st < 4; ++st)
#pragma unroll
    for (int i = 0; i < 7; ++i) gu_piece(st, i);
  u32x2 fy;
  ald8_sc1(fy, rws, flagoff(FL_Y, mb, lane), 0);
  if (wave == 1) {
#pragma unroll
    for (int p = 0; p < 7; ++p) lds_st1(L0 + L4_UV + (p * 64 + lane) * 4, uvv[p]);
  }
  const rsrc_t rwd = mkrsrc(wd, F_ * D_ * 2);
  unsigned wdg[8];
  {
    auto rm = [](int g, int t, int q) { return 16 * (g >> 1) + 8 * t + 4 * (g & 1) + q; };
    const unsigned b0 = w80_voff(0, lane, nt, rm), b1 = w80_voff(1, lane, nt, rm), b4 = w80_voff(4, lane, nt, rm);
#pragma unroll
    for (int i = 0; i < 8; ++i) {
      const int pc = min(8 * wave + i, 29), kl = pc / 5, pp = pc - 5 * kl;
      wdg[i] = opaque((pp == 4 ? b4 : (pp & 1) ? b1 : b0) + (kl * 32 + (pp == 4 ? 0 : 16 * (pp >> 1))) * 1280);
    }
  }
  auto wd_piece = [&](int G, int i) { const int g = G + goL; dma16(rwd, wdg[i], (g >= 9 ? g - 9 : g) * 6 * 32 * 1280, L0 + (G % 5) * 30720 + min(8 * wave + i, 29) * 1024); };
  const Rot R5 = mkrot(rD, 6 * goL, 54, 2048);
  auto fo54 = [&](int s) { return R5.b[s < 12 ? 0 : s < 30 ? 1 : s < 42 ? 2 : 3] + s * 2048; };
  vmwait<0>(); asm volatile("" : "+v"(fy));
  if (wave == 0 && !flags_up(fy, tag)) wait_flags(ws, flagoff(FL_Y, mb, lane), true, tag);
  vmwait<0>();
  BAR();
  u32x2 stv[4];
#pragma unroll
  for (int i = 0; i < 4; ++i) { const int T = 32 * wave + (lane & 31); ald8_sc1(stv[i], rws, WS_STAT + (((mb * 2 + (T >> 6)) * 8 + 4 * (lane >> 5) + i) * 64 + (T & 63)) * 8, 0); }
  u32x4 yf[40];
  const unsigned yfo = WS_Y + (4 * mb + wave) * T32_D + 16 * lane;
  const Rot R4 = mkrot(0, 0, 40, 1024);
#pragma unroll
  for (int ks = 0; ks < P4F0; ++ks) ald16_sc1(yf[ks], rws, yfo, fo40(R4, ks));
  f32x16 acc4[7];
  {
    const int h = lane >> 5, blk = (lane >> 4) & 1, q = (lane & 15) >> 2;
    const unsigned c0 = L0 + blk * L4_U + 2 * h * 896 + q * 64 + blk * 32 + (lane & 3) * 8;
    const unsigned c2 = L0 + blk * L4_U + 2 * h * 896 + 768 + q * 32 + (lane & 3) * 8;
    unsigned cb0[5], cb1[5], cb2[5];
#pragma unroll
    for (int sl = 0; sl < 5; ++sl) { cb0[sl] = opaque(c0 + sl * L4_STAGE); cb1[sl] = opaque((c0 ^ 32u) + sl * L4_STAGE); cb2[sl] = opaque(c2 + sl * L4_STAGE); }
    auto readw = [&](int ks, int p) {
      const int sl = (ks >> 2) % 5;
      const unsigned a = (p == 6 ? cb2[sl] : ((p & 1) ? cb1[sl] : cb0[sl]) + (p >> 1) * 256) + (ks & 3) * 4 * 896;
      return trfrag(a, a + 896); };
    s16x8 w[3][7];
#pragma unroll
    for (int p = 0; p < 7; ++p) { w[0][p] = readw(0, p); w[1][p] = readw(1, p); }
    SB();
    static_for<0, 40>([&](auto KS) {
      constexpr int ks = KS, c = ks % 3, n = (ks + 2) % 3;
      if constexpr ((ks & 3) == 2) {
        constexpr int st = (ks + 2) / 4;
        if constexpr (st >= 4 && st <= 9) vmwait<p4_wait_stage(st)>();
        BAR_RAW();
      }
      vmwait<p4_wait_frag(ks)>(yf[ks]);
      if constexpr (ks == 0) asm volatile("" : "+v"(stv[0]), "+v"(stv[1]), "+v"(stv[2]), "+v"(stv[3]));
      SB();
#pragma unroll
      for (int p = 0; p < 7; ++p) {
        if (ks == 0) gmfma_0(acc4[p], w[c][p], yf[ks]); else gmfma_a(acc4[p], w[c][p], yf[ks]);
        SB();
        if (p == 1 && ks + P4F0 < 40) ald16_sc1(yf[ks + P4F0 < 40 ? ks + P4F0 : 0], rws, yfo, fo40(R4, ks + P4F0));
        if (p == 3 && p4_pieces(ks) > 0) gu_piece((ks + 14) / 4, 2 * ((ks + 14) % 4));
        if (p == 5 && p4_pieces(ks) > 1) gu_piece((ks + 14) / 4, 2 * ((ks + 14) % 4) + 1);
        if (p == 3 && p4_touch(ks) > 0) wd_piece(p4_wd_before(ks) / 8, p4_wd_before(ks) % 8);
        if (p == 5 && p4_touch(ks) > 1) wd_piece((p4_wd_before(ks) + 1) / 8, (p4_wd_before(ks) + 1) % 8);
        if (ks + 2 < 40) w[n][p] = readw(ks + 2, p);
        SB();
      }
    });
  }
  asm volatile("s_nop 11" : "+v"(acc4[0]), "+v"(acc4[1]), "+v"(acc4[2]), "+v"(acc4[3]), "+v"(acc4[4]), "+v"(acc4[5]), "+v"(acc4[6]));
  {
    const int h = lane >> 5;
    float ms = 0.f;
#pragma unroll
    for (int i = 0; i < 4; ++i) ms += __uint_as_float(stv[i][0]);
    const float mean = xsum32(ms) * 0.125f;
    float m2 = 0.f;
#pragma unroll
    for (int i = 0; i < 4; ++i) { const float d = __uint_as_float(stv[i][0]) - mean; m2 += __uint_as_float(stv[i][1]) + 80.0f * d * d; }
    const float rs = rden_of(xsum32(m2) * (1.0f / D_)), nm = -rs * bf16r(mean);
    const unsigned ab = WS_ACT + (4 * mb + wave) * T32_F + 16 * lane, uvb = opaque(L0 + L4_UV + 16 * h);
#pragma unroll
    for (int p = 0; p < 7; ++p) {
      if (p < 6 || nu == 7) {
        const unsigned tb = uvb + p * 256;
        u32x4 ov;
#pragma unroll
        for (int half = 0; half < 2; ++half) {
          const f32x4 ug = lds_f4(tb + 32 * half), vg = lds_f4(tb + 64 + 32 * half), uu = lds_f4(tb + 128 + 32 * half), vu = lds_f4(tb + 192 + 32 * half);
#pragma unroll
          for (int r = 0; r < 2; ++r) {
            const f32x2 ag = {acc4[p][4 * half + 2 * r], acc4[p][4 * half + 2 * r + 1]}, au = {acc4[p][8 + 4 * half + 2 * r], acc4[p][8 + 4 * half + 2 * r + 1]};
            const f32x2 g = rs * ag + (nm * (f32x2){ug[2 * r], ug[2 * r + 1]} + (f32x2){vg[2 * r], vg[2 * r + 1]});
            const f32x2 u = rs * au + (nm * (f32x2){uu[2 * r], uu[2 * r + 1]} + (f32x2){vu[2 * r], vu[2 * r + 1]});
            const f32x2 e = g * (-LOG2E);
            const f32x2 den = (f32x2){exp2_(e.x), exp2_(e.y)} + 1.0f;
            const f32x2 a = (g * u) * (f32x2){rcpf_(den.x), rcpf_(den.y)};
            ov[2 * half + r] = pk2(a.x, a.y);
          }
        }
        st16_sc1(ov, rws, ab + (u0 + unit_of(p, uodd, nu)) * 1024, 0);
      }
    }
#pragma unroll
    for (int i = 4; i < 8; ++i) wd_piece(2, i);
#pragma unroll
    for (int i = 0; i < 3; ++i) wd_piece(3, i);
    vmwait<7>();
    if (lane == 0) st_u64(ws, flagoff(FL_ACT, mb, 4 * j + wave), flagval(tag));
  }
  BAR();

#pragma unroll
  for (int i = 3; i < 8; ++i) wd_piece(3, i);
#pragma unroll
  for (int i = 0; i < 8; ++i) wd_piece(4, i);
  if (wave == 0) wait_flags(ws, flagoff(FL_ACT, mbL, lane), true, tag);
  vmwait<0>();
  BAR();
  {
    u32x4 af[54];
    const unsigned afb = WS_ACT + (4 * mbL + 2 * th + (wave >> 1)) * T32_F + g4 * 512 + ((wave & 1) * 16 + t16) * 16;
#pragma unroll
    for (int s = 0; s < P5F0; ++s) ald16_sc1(af[s], rws, afb, fo54(s));
    f32x4 acc[5];
    const B80 B5[3] = {b80(L0), b80(L0 + 61440), b80(L0 + 122880)};
    auto rd5 = [&](int s, int p) { const int sl = s % 30; return w80(B5[sl / 12], (sl % 12) * 5120, p); };
    s16x8 w[3][5];
#pragma unroll
    for (int p = 0; p < 5; ++p) { w[0][p] = rd5(0, p); w[1][p] = rd5(1, p); }
    SB();
    static_for<0, 54>([&](auto SS) {
      constexpr int s = SS;
      if constexpr (s % 6 == 4 && s + 2 < 54) {
        constexpr int g = (s + 2) / 6;
        if constexpr (g >= 5) vmwait<p5_wait_group(g)>();
        BAR_RAW();
      }
      vmwait<p5_wait_frag(s)>(af[s]);
      SB();
#pragma unroll
      for (int p = 0; p < 5; ++p) {
        if (s == 0) hmfma_0(acc[p], w[s % 3][p], af[s]); else hmfma_a(acc[p], w[s % 3][p], af[s]);
        SB();
        constexpr int r = (s + 20) % 6, i0 = r < 2 ? 2 * r : r + 2;
        if (p == 0 && s + P5F0 < 54) ald16_sc1(af[s + P5F0 < 54 ? s + P5F0 : 0], rws, afb, fo54(s + P5F0));
        if (p == 2 && p5_pieces(s) > 0) wd_piece((s + 20) / 6, i0);
        if (p == 4 && p5_pieces(s) > 1) wd_piece((s + 20) / 6, i0 + 1);
        if (s + 2 < 54) w[(s + 2) % 3][p] = rd5(s + 2, p);
        SB();
      }
    });
    asm volatile("s_nop 7" : "+v"(acc[0]), "+v"(acc[1]), "+v"(acc[2]), "+v"(acc[3]), "+v"(acc[4]));
    u32x2 op[5];
    const unsigned gbb = opaque(L0 + L3_VEC + 640 + g4 * 16);
#pragma unroll
    for (int p = 0; p < 5; ++p) {
      const f32x4 gb = lds_f4(gbb + p * 64);
#pragma unroll
      for (int r = 0; r < 2; ++r) {
        const f32x2 dn = bf16r2((f32x2){acc[p][2 * r], acc[p][2 * r + 1]});
        const f32x2 o = unpk(x1p[p][r]) + bf16r2(dn * (f32x2){gb[2 * r], gb[2 * r + 1]});
        op[p][r] = pk2(o.x, o.y);
      }
    }
    const rsrc_t ro = mkrsrc(out, S_ * D_ * 2);
#pragma unroll
    for (int pp = 0; pp < 3; ++pp) {
      const int pa = 2 * pp, pb = pp < 2 ? 2 * pp + 1 : 4;
      const auto r0 = __builtin_amdgcn_permlane16_swap(op[pa][0], op[pb][0], false, false), r1 = __builtin_amdgcn_permlane16_swap(op[pa][1], op[pb][1], false, false);
      const int ct = (g4 & 1) ? ct_of(pb, nt & 1) : ct_of(pa, nt & 1);
      __builtin_amdgcn_raw_buffer_store_b128((u32x4){r0[0], r1[0], r0[1], r1[1]}, ro, (pp < 2 || !(g4 & 1)) ? tok3 * 1280 + 160 * nt + 32 * ct + 16 * (g4 >> 1) : 0x40000000u, 0, 16);
    }
  }
  asm volatile(".Ladaln_code_end:");
}

extern "C" void kernel_launch(void* const* d_in, const int* in_sizes, int n_in, void* d_out, int out_size, void* d_ws, size_t ws_size,
                              hipStream_t stream) {
  (void)in_sizes; (void)n_in; (void)out_size; (void)ws_size;
  adaln_block_kernel<<<256, 256, 0, stream>>>((const char*)d_in[0], (const char*)d_in[1], (const char*)d_in[2], (const char*)d_in[4], (const char*)d_in[5],
                                              (const char*)d_in[6], (const char*)d_in[7], (const char*)d_in[8], (char*)d_out, (char*)d_ws);
}
```

```cpp
#include <hip/hip_runtime.h>
#include <stdint.h>
#include <type_traits>

#define LAS __attribute__((address_space(3)))
#define GAS __attribute__((address_space(1)))
#define DI __device__ __forceinline__
typedef float f32x2 __attribute__((ext_vector_type(2)));
typedef float f32x4 __attribute__((ext_vector_type(4)));
typedef float f32x16 __attribute__((ext_vector_type(16)));
typedef unsigned u32x2 __attribute__((ext_vector_type(2)));
typedef unsigned u32x4 __attribute__((ext_vector_type(4)));
typedef short s16x4 __attribute__((ext_vector_type(4)));
typedef short s16x8 __attribute__((ext_vector_type(8)));
typedef __bf16 bf16x2 __attribute__((ext_vector_type(2)));
typedef __bf16 bf16x8 __attribute__((ext_vector_type(8)));
typedef unsigned long long u64;
typedef __amdgpu_buffer_rsrc_t rsrc_t;

constexpr int S_ = 2048, D_ = 640, F_ = 1728;
constexpr int T32_D = 80 * 512;
constexpr int T32_F = 216 * 512;

constexpr int WS_EPOCH = 0;
constexpr int WS_TMO = 256;
constexpr int WS_FLAG = 29 << 20;
constexpr int FL_XN = 0, FL_KV = 1, FL_O = 2, FL_Y = 3, FL_ACT = 4;
constexpr int WS_GRAN = 256 * 1024;
constexpr int WS_UV = 320 * 1024;
constexpr int WS_STAT = 512 * 1024;
constexpr int WS_XN = 1 << 20;
constexpr int WS_KV = 4 << 20;
constexpr int WS_O = 12 << 20;
constexpr int WS_Y = 16 << 20;
constexpr int WS_ACT = 20 << 20;
constexpr int WS_USED = 30 << 20;

constexpr int LDS_BYTES = 163840;
constexpr int L1_W = 0;
constexpr int L0_TB = 122880;
constexpr int L0_JW = 133120;
constexpr int L1_VS = 153600, L1_VA = 154880;
constexpr int L0_SC = 158720;
constexpr int L2_KV = 0;
constexpr int KV_IMG = 384 * 48;
constexpr int L3_W0 = 4 * KV_IMG;
constexpr int L3_NPRE = 15;
constexpr int L3_W1 = 0;
constexpr int L2_JW = 73728, L2_JV = 94208;
constexpr int L3_JP = 161984;
constexpr int L3_XR = 150528;
constexpr int L_DUMMY = 162816;
constexpr int L3_VEC = 161024;
constexpr int L4_STAGE = 30720, L4_U = 14464;
constexpr int L4_UV = 153600;
static_assert(L3_XR + 64 * 160 <= L3_VEC && L3_W0 + L3_NPRE * 5120 <= L3_XR, "P3 residual rows");
static_assert(L3_W0 + L3_NPRE * 5120 <= L3_VEC && L3_VEC + 960 <= LDS_BYTES, "P3 LDS map");
static_assert(30 * 5120 <= L3_VEC, "P5 LDS map");
static_assert(L4_UV + 1792 <= L3_VEC, "P4 LDS map");
static_assert(L3_VEC + 960 <= L3_JP && L3_JP + 512 <= L_DUMMY && L_DUMMY + 256 <= LDS_BYTES, "small LDS areas");

constexpr float LOG2E = 1.4426950408889634f;
constexpr float SCORE_C = 0.22360679774997896f * 1.4426950408889634f;
constexpr float NEGBIG = -1.0e30f;
constexpr unsigned SPIN_CAP = 1u << 15;

#define SB() __builtin_amdgcn_sched_barrier(0)
#define BAR() do { asm volatile("s_waitcnt lgkmcnt(0)" ::: "memory"); __builtin_amdgcn_s_barrier(); asm volatile("" ::: "memory"); } while (0)
#define BAR_RAW() do { asm volatile("" ::: "memory"); __builtin_amdgcn_s_barrier(); asm volatile("" ::: "memory"); } while (0)
#define MEMFENCE() asm volatile("" ::: "memory")
template <int N> DI void vmwait() { asm volatile("s_waitcnt vmcnt(%0)" :: "n"(N) : "memory"); }
template <int N> DI void vmwait(u32x4& d) { asm volatile("s_waitcnt vmcnt(%1)" : "+v"(d) : "n"(N) : "memory"); }

DI unsigned pk2(float a, float b) { bf16x2 v = __builtin_convertvector((f32x2){a, b}, bf16x2); return __builtin_bit_cast(unsigned, v); }
DI float blo(unsigned v) { return __uint_as_float(v << 16); }
DI float bhi(unsigned v) { return __uint_as_float(v & 0xffff0000u); }
DI float bf16r(float a) { return blo(pk2(a, a)); }
DI f32x2 bf16r2(f32x2 a) { const unsigned p = pk2(a.x, a.y); return (f32x2){blo(p), bhi(p)}; }
DI f32x2 unpk(unsigned p) { return (f32x2){blo(p), bhi(p)}; }
DI float rcpf_(float x) { return __builtin_amdgcn_rcpf(x); }
DI float exp2_(float x) { return __builtin_amdgcn_exp2f(x); }
DI float siluf(float x) { return x * rcpf_(1.0f + exp2_(-LOG2E * x)); }
DI float rden_of(float var) { return rcpf_(bf16r(__builtin_amdgcn_sqrtf(bf16r(bf16r(var) + 1e-5f)))); }
template <int CTRL> DI float dpp_add(float x) { return x + __int_as_float(__builtin_amdgcn_update_dpp(0, __float_as_int(x), CTRL, 0xf, 0xf, true)); }
DI float sum16(float x) { x = dpp_add<0xB1>(x); x = dpp_add<0x4E>(x); x = dpp_add<0x141>(x); return dpp_add<0x140>(x); }
DI float xsum16(float x) { auto r = __builtin_amdgcn_permlane16_swap(__float_as_uint(x), __float_as_uint(x), false, false); return __uint_as_float(r[0]) + __uint_as_float(r[1]); }
DI float xsum32(float x) { auto r = __builtin_amdgcn_permlane32_swap(__float_as_uint(x), __float_as_uint(x), false, false); return __uint_as_float(r[0]) + __uint_as_float(r[1]); }
DI float xmax32(float x) { auto r = __builtin_amdgcn_permlane32_swap(__float_as_uint(x), __float_as_uint(x), false, false); return fmaxf(__uint_as_float(r[0]), __uint_as_float(r[1])); }
DI rsrc_t mkrsrc(const void* p, unsigned bytes) { return __builtin_amdgcn_make_buffer_rsrc((void*)p, (short)0, (int)bytes, 0x00020000); }
DI void st16_sc1(u32x4 v, rsrc_t r, unsigned voff, unsigned soff) { __builtin_amdgcn_raw_buffer_store_b128(v, r, voff, soff, 16); }
DI void ald16_sc1(u32x4& d, rsrc_t r, unsigned voff, unsigned soff) {
  asm volatile("buffer_load_dwordx4 %0, %1, %2, %3 offen sc1" : "=v"(d) : "v"(voff), "s"(r), "s"(soff) : "memory");
}
DI void ald16(u32x4& d, rsrc_t r, unsigned voff, unsigned soff) {
  asm volatile("buffer_load_dwordx4 %0, %1, %2, %3 offen" : "=v"(d) : "v"(voff), "s"(r), "s"(soff) : "memory");
}
DI void ald4(unsigned& d, rsrc_t r, unsigned voff, unsigned soff) {
  asm volatile("buffer_load_dword %0, %1, %2, %3 offen" : "=v"(d) : "v"(voff), "s"(r), "s"(soff) : "memory");
}
DI void ald8_sc1(u32x2& d, rsrc_t r, unsigned voff, unsigned soff) {
  asm volatile("buffer_load_dwordx2 %0, %1, %2, %3 offen sc1" : "=v"(d) : "v"(voff), "s"(r), "s"(soff) : "memory");
}
DI void touch(rsrc_t r, unsigned voff, unsigned soff, unsigned ldsaddr) {
  asm volatile("s_mov_b32 m0, %0\n\ts_nop 0\n\tbuffer_load_dword %1, %2, %3 offen lds" :: "s"(ldsaddr), "v"(voff), "s"(r), "s"(soff) : "memory");
}
DI void dma16_sc1(rsrc_t r, unsigned voff, unsigned soff, unsigned ldsaddr) {
  asm volatile("s_mov_b32 m0, %0\n\ts_nop 0\n\tbuffer_load_dwordx4 %1, %2, %3 offen sc1 lds" :: "s"(ldsaddr), "v"(voff), "s"(r), "s"(soff) : "memory");
}
DI void dma16(rsrc_t r, unsigned voff, unsigned soff, unsigned ldsaddr) {
  asm volatile("s_mov_b32 m0, %0\n\ts_nop 0\n\tbuffer_load_dwordx4 %1, %2, %3 offen lds" :: "s"(ldsaddr), "v"(voff), "s"(r), "s"(soff) : "memory");
}
DI u64 ld_u64(const char* ws, unsigned off) { return __hip_atomic_load((GAS u64*)(ws + off), __ATOMIC_RELAXED, __HIP_MEMORY_SCOPE_AGENT); }
DI void st_u64(char* ws, unsigned off, u64 v) { __hip_atomic_store((GAS u64*)(ws + off), v, __ATOMIC_RELAXED, __HIP_MEMORY_SCOPE_AGENT); }
DI u64 flagval(unsigned tag) { return ((u64)tag << 32) | (u64)(~tag); }
DI unsigned flagoff(int kind, int mb, int jw) { return WS_FLAG + ((kind * 16 + mb) * 64 + jw) * 128; }
DI void publish(char* ws, int kind, int mb, int j, int wave, int lane, unsigned tag) {
  vmwait<0>();
  if (lane == 0) st_u64(ws, flagoff(kind, mb, 4 * j + wave), flagval(tag));
}
DI bool spin_fail(unsigned& spins, char* ws, unsigned tag) {
  __builtin_amdgcn_s_sleep(1);
  if (++spins > SPIN_CAP) { __hip_atomic_store((GAS unsigned*)(ws + WS_TMO), tag, __ATOMIC_RELAXED, __HIP_MEMORY_SCOPE_AGENT); return true; }
  return (spins & 127u) == 0u && (unsigned)__builtin_amdgcn_readfirstlane(__hip_atomic_load((GAS unsigned*)(ws + WS_TMO), __ATOMIC_RELAXED, __HIP_MEMORY_SCOPE_AGENT)) == tag;
}
DI bool flags_up(const u32x2& f, unsigned tag) { return __all(f[0] == ~tag && f[1] == tag); }
DI void wait_flags(char* ws, unsigned off, bool on, unsigned tag) {
  const u64 want = flagval(tag);
#pragma clang loop unroll(disable)
  for (unsigned spins = 0;;) {
    u64 v = want;
    if (on) v = ld_u64(ws, off);
    if (__all(v == want)) break;
    if (spin_fail(spins, ws, tag)) break;
  }
  MEMFENCE();
}
template <int N, class OFF, class ON> DI void sweep(char* ws, OFF off, ON on, unsigned tag, unsigned (&val)[N]) {
#pragma clang loop unroll(disable)
  for (unsigned spins = 0;;) {
    bool ok = true;
#pragma unroll
    for (int k = 0; k < N; ++k) { u64 g = (u64)tag << 32; if (on(k)) g = ld_u64(ws, off(k)); val[k] = (unsigned)g; ok &= (unsigned)(g >> 32) == tag; }
    if (__all(ok)) break;
    if (spin_fail(spins, ws, tag)) break;
  }
  MEMFENCE();
}
DI s16x8 trfrag(unsigned a0, unsigned a1) {
  const s16x4 lo = __builtin_amdgcn_ds_read_tr16_b64_v4i16((LAS s16x4*)(size_t)a0);
  const s16x4 hi = __builtin_amdgcn_ds_read_tr16_b64_v4i16((LAS s16x4*)(size_t)a1);
  return __builtin_shufflevector(lo, hi, 0, 1, 2, 3, 4, 5, 6, 7);
}
DI f32x4 lds_f4(unsigned a) { return *(LAS f32x4*)(size_t)a; }
DI u32x4 lds_u4(unsigned a) { return *(LAS u32x4*)(size_t)a; }
DI void lds_st4(unsigned a, u32x4 v) { *(LAS u32x4*)(size_t)a = v; }
DI void lds_st1(unsigned a, unsigned v) { *(LAS unsigned*)(size_t)a = v; }
DI void lds_st2f(unsigned a, f32x2 v) { *(LAS f32x2*)(size_t)a = v; }
DI unsigned opaque(unsigned v) { asm volatile("" : "+v"(v)); return v; }
DI unsigned sopaque(unsigned v) { asm volatile("" : "+s"(v)); return v; }
struct Rot { unsigned b[4]; };
DI Rot mkrot(int r, int shift, int N, int step) {
  const unsigned a = shift * step, w = N * step;
  return Rot{{sopaque(a), sopaque(a - (r >= 3 ? w : 0)), sopaque(a - (r >= 2 ? w : 0)), sopaque(a - (r >= 1 ? w : 0))}};
}
DI f32x16 mfma32(s16x8 a, u32x4 b, f32x16 c) { return __builtin_amdgcn_mfma_f32_32x32x16_bf16(__builtin_bit_cast(bf16x8, a), __builtin_bit_cast(bf16x8, b), c, 0, 0, 0); }
DI f32x4 mfma16(s16x8 a, u32x4 b, f32x4 c) { return __builtin_amdgcn_mfma_f32_16x16x32_bf16(__builtin_bit_cast(bf16x8, a), __builtin_bit_cast(bf16x8, b), c, 0, 0, 0); }
DI void smfma_c(f32x16& d, u32x4 a, u32x4 b, const f32x16& c) { asm("v_mfma_f32_32x32x16_bf16 %0, %1, %2, %3" : "=&v"(d) : "v"(a), "v"(b), "v"(c)); }
DI void smfma_0(f32x16& d, u32x4 a, u32x4 b) { asm("v_mfma_f32_32x32x16_bf16 %0, %1, %2, 0" : "=&v"(d) : "v"(a), "v"(b)); }
DI void smfma_a(f32x16& d, u32x4 a, u32x4 b) { asm("v_mfma_f32_32x32x16_bf16 %0, %1, %2, %0" : "+v"(d) : "v"(a), "v"(b)); }
DI void gmfma_0(f32x16& d, s16x8 a, u32x4 b) { asm("v_mfma_f32_32x32x16_bf16 %0, %1, %2, 0" : "=&v"(d) : "a"(a), "v"(b)); }
DI void gmfma_a(f32x16& d, s16x8 a, u32x4 b) { asm("v_mfma_f32_32x32x16_bf16 %0, %1, %2, %0" : "+v"(d) : "a"(a), "v"(b)); }
DI void hmfma_0(f32x4& d, s16x8 a, u32x4 b) { asm("v_mfma_f32_16x16x32_bf16 %0, %1, %2, 0" : "=&v"(d) : "a"(a), "v"(b)); }
DI void hmfma_a(f32x4& d, s16x8 a, u32x4 b) { asm("v_mfma_f32_16x16x32_bf16 %0, %1, %2, %0" : "+v"(d) : "a"(a), "v"(b)); }
DI void sfence(f32x16& d) { asm volatile("s_nop 11" : "+v"(d)); }
DI float fmix_lo(unsigned x, unsigned s, unsigned a) { float r; asm("v_fma_mix_f32 %0, %1, %2, %3 op_sel_hi:[1,1,1]" : "=v"(r) : "v"(x), "v"(s), "v"(a)); return r; }
DI float fmix_hi(unsigned x, unsigned s, unsigned a) { float r; asm("v_fma_mix_f32 %0, %1, %2, %3 op_sel:[1,1,1] op_sel_hi:[1,1,1]" : "=v"(r) : "v"(x), "v"(s), "v"(a)); return r; }
DI unsigned pkh(float a, float b) { return __builtin_bit_cast(unsigned, __builtin_amdgcn_cvt_pkrtz(a, b)); }


template <int NS> DI void gemv16_issue(rsrc_t rw, unsigned rowstride, unsigned srcbyte, unsigned ldsw, int lane) {
  const unsigned vo = (8 * ((lane >> 3) & 3) + 4 * (lane >> 5) + ((lane >> 1) & 3)) * rowstride + 16 * (lane & 1);
#pragma unroll 5
  for (int s = 0; s < NS; ++s) dma16(rw, vo, s * 32 * rowstride + srcbyte, ldsw + s * 1024);
}
template <int NS> DI f32x4 gemv16_run(unsigned ldsw, unsigned ldsv, int vmask, int lane) {
  f32x4 acc0 = {}, acc1 = {};
  const unsigned wa = opaque(ldsw + 8 * lane), va = opaque(ldsv + (lane & vmask) * 1280 + (lane >> 4) * 16);
  s16x8 w[3]; u32x4 v[3];
#pragma unroll
  for (int s = 0; s < 2; ++s) { w[s] = trfrag(wa + s * 1024, wa + s * 1024 + 512); v[s] = lds_u4(va + s * 64); }
  SB();
#pragma unroll
  for (int s = 0; s < NS; ++s) {
    if (s + 2 < NS) { w[(s + 2) % 3] = trfrag(wa + (s + 2) * 1024, wa + (s + 2) * 1024 + 512); v[(s + 2) % 3] = lds_u4(va + (s + 2) * 64); }
    SB();
    if (s & 1) acc1 = mfma16(w[s % 3], v[s % 3], acc1); else acc0 = mfma16(w[s % 3], v[s % 3], acc0);
    SB();
  }
  return acc0 + acc1;
}

DI int ct_of(int p, int odd) { return odd ? (p < 4 ? p + 1 : 0) : p; }
DI int unit_of(int p, int odd, int nu) { const int u = odd ? (p < 6 ? p + 1 : 0) : p; return u < nu ? u : nu - 1; }

template <class RM> DI unsigned w80_voff(int piece, int lane, int nt, RM rowmap) {
  const int odd = nt & 1;
  int g, t, q, p, hf;
  if (piece < 4) { g = piece; t = lane >> 5; const int su = (lane >> 4) & 1; q = (lane >> 2) & 3; const int c = (lane & 3) ^ (2 * (piece & 1)); p = 2 * su + (c >> 1); hf = c & 1; }
  else { t = lane >> 5; g = (lane >> 3) & 3; q = (lane >> 1) & 3; p = 4; hf = lane & 1; }
  return rowmap(g, t, q) * 1280 + 160 * nt + 32 * ct_of(p, odd) + 16 * hf;
}

template <class OPS> constexpr int vm_sum(OPS ops, int lo, int hi) { int n = 0; for (int k = lo; k <= hi; ++k) if (k >= 0) n += ops(k); return n; }
constexpr int P1F0 = 10;
constexpr int p1_pieces(int k) { return k < 24 ? 1 : 0; }
constexpr int p1_ops(int k) { return (k < 40 - P1F0 ? 1 : 0) + p1_pieces(k) + (k >= 30 && k < 38 ? 1 : 0); }
constexpr int p1_wait_frag(int f) {
  return f < P1F0 ? 8 + vm_sum(p1_ops, 0, f - 3) : p1_pieces(f - P1F0) + vm_sum(p1_ops, f - P1F0 + 1, f - 3);
}
constexpr int p1_wait_stage(int st) { return st == 2 ? 4 + vm_sum(p1_ops, 0, 5) : st == 3 ? vm_sum(p1_ops, 0, 9) : vm_sum(p1_ops, 4 * st - 12, 4 * st - 3); }
constexpr int P4F0 = 8;
constexpr int p4_pieces(int k) { return k >= 2 && k <= 25 ? ((k + 14) % 4 == 3 ? 1 : 2) : 0; }
constexpr int p4_wd_before(int k) { return k <= 26 ? 0 : k == 27 ? 2 : k == 28 ? 3 : k == 29 ? 5 : k == 30 ? 6 : k == 31 ? 8 : k == 32 ? 9 : k == 33 ? 11 : k == 34 ? 12 : k == 35 ? 14 : k <= 40 ? 16 + (k - 36) : 20; }
constexpr int p4_touch(int k) { return p4_wd_before(k + 1) - p4_wd_before(k); }
constexpr int p4_ops(int k) { return (k < 40 - P4F0 ? 1 : 0) + p4_pieces(k) + p4_touch(k); }
constexpr int p4_wait_frag(int ks) { return ks < P4F0 ? (P4F0 - 1 - ks) + vm_sum(p4_ops, 0, ks - 1) : p4_pieces(ks - P4F0) + p4_touch(ks - P4F0) + vm_sum(p4_ops, ks - P4F0 + 1, ks - 1); }
constexpr int p4_wait_stage(int st) { return vm_sum(p4_ops, 4 * st - 10, 4 * st - 3); }
constexpr int P5F0 = 12;
constexpr int p5_pieces(int k) { return k >= 10 && k <= 33 ? ((k + 20) % 6 < 2 ? 2 : 1) : 0; }
constexpr int p5_ops(int k) { return (k < 54 - P5F0 ? 1 : 0) + p5_pieces(k); }
constexpr int p5_wait_frag(int s) { return s < P5F0 ? (P5F0 - 1 - s) + vm_sum(p5_ops, 0, s - 1) : p5_pieces(s - P5F0) + vm_sum(p5_ops, s - P5F0 + 1, s - 1); }
constexpr int p5_wait_group(int g) { return vm_sum(p5_ops, 6 * g - 14, 6 * g - 3); }
constexpr int P3F0 = 6;
constexpr int p3_ops(int k) { return k < 20 - P3F0 ? 1 : 0; }
constexpr int p3_wait_frag(int s) { return s < P3F0 ? (P3F0 - 1 - s) + vm_sum(p3_ops, 0, s - 1) : vm_sum(p3_ops, s - P3F0 + 1, s - 1); }

template <int I, int N, class Fn> DI void static_for(Fn&& f) { if constexpr (I < N) { f(std::integral_constant<int, I>{}); static_for<I + 1, N>(f); } }

__global__ __launch_bounds__(256, 1) void adaln_block_kernel(const char* __restrict__ x, const char* __restrict__ wqkv, const char* __restrict__ wo,
                                                             const char* __restrict__ wg, const char* __restrict__ wu, const char* __restrict__ wd,
                                                             const char* __restrict__ cvec, const char* __restrict__ wmod, char* out, char* ws) {
  __shared__ __attribute__((aligned(16))) char smem[LDS_BYTES];
  const unsigned L0 = (unsigned)(size_t)(LAS char*)smem;
  const int tid = threadIdx.x, lane = tid & 63;
  const int wave = __builtin_amdgcn_readfirstlane(tid >> 6);
  const int bid = blockIdx.x, xcd = bid & 7, ii = bid >> 3;
  const int mbL = 2 * xcd + (ii >> 4), jL = ii & 15;
  const int mb = 4 * (xcd >> 1) + (ii >> 3), j = 8 * (xcd & 1) + (ii & 7);
  const int rW = mb & 3, rL = 0, rD = 2 * (mbL & 1) + (jL & 1);
  const int soW = (5 * rW) >> 1;
  const int goL = rD == 3 ? 7 : 2 * rD;
  const rsrc_t rx = mkrsrc(x, S_ * D_ * 2), rws = mkrsrc(ws, WS_USED);

  unsigned epoch, tag;
  const rsrc_t rwq = mkrsrc(wqkv, D_ * 3 * D_ * 2);
  unsigned wqo[4];
  auto rot10 = [&](int so, int st) { const int g = st + so; return g >= 10 ? g - 10 : g; };
  const int so1 = (int)sopaque(soW);
  auto wq_piece = [&](int st, int i) { dma16(rwq, wqo[i], rot10(so1, st) * 64 * 3840, L0 + L1_W + (st * 15 + min(4 * wave + i, 14)) * 1024); };
  auto fo40 = [&](const Rot& R, int ks) { return R.b[ks < 12 ? 0 : ks < 20 ? 1 : ks < 32 ? 2 : 3] + ks * 1024; };
  auto wq_issue = [&](int st) {
#pragma unroll
    for (int i = 0; i < 4; ++i) wq_piece(st, i);
  };
  {
    const int c32 = lane & 31, tok = 128 * mbL + 8 * jL + 2 * wave + (lane >> 5);
    u32x4 xv[3], c4 = {}; unsigned c1 = 0;
    ald16(xv[0], rx, tok * 1280 + c32 * 16, 0);
    ald16(xv[1], rx, tok * 1280 + c32 * 16 + 512, 0);
    ald16(xv[2], rx, c32 < 16 ? tok * 1280 + c32 * 16 + 1024 : 0x40000000u, 0);
    if (wave == 3) {
      u64 pc; int len;
      asm volatile("s_getpc_b64 %0\n.Ladaln_code_begin:\n\ts_mov_b32 %1, .Ladaln_code_end-.Ladaln_code_begin" : "=s"(pc), "=s"(len));
      len = len > 0 && len < (1 << 18) ? len : 0;
      touch(mkrsrc((const void*)pc, len), (ii * 40 + min(lane, 39)) * 64, 0, L0 + L_DUMMY);
    }
    const int munit = ii < 10 ? 10 * xcd + ii : 80 + 20 * xcd + (ii - 10);
    const bool mjob = wave == 0 && ii < 30;
    if (mjob) {
      const rsrc_t rc = mkrsrc(cvec, D_ * 2);
      ald16(c4, rc, 20 * lane, 0); ald4(c1, rc, 20 * lane + 16, 0);
    }
#pragma unroll
    for (int i = 0; i < 4; ++i) {
      const int idx = 64 * min(4 * wave + i, 14) + lane, rq = idx / 60, rem = idx - 60 * rq;
      int q, ch;
      if (rem < 48) { q = (rem >> 2) & 3; ch = 4 * (rem >> 4) + (rem & 3); } else { const int r2 = rem - 48; q = r2 / 3; ch = 12 + r2 - 3 * q; }
      const int seg = ch / 5;
      wqo[i] = (4 * rq + q) * 3840 + seg * 1280 + 80 * j + (ch - 5 * seg) * 16;
    }
    asm volatile("buffer_load_dword %0, %1, %2, 0 offen sc1" : "=v"(epoch) : "v"(WS_EPOCH), "s"(rws) : "memory");
    if (mjob) {
      gemv16_issue<20>(mkrsrc(wmod, D_ * 6 * D_ * 2), 7680, 32 * munit, L0 + L0_JW, lane);
      vmwait<21>();
    } else {
      vmwait<1>();
    }
    asm volatile("" : "+v"(xv[0]), "+v"(xv[1]), "+v"(xv[2]), "+v"(c4), "+v"(c1));
    f32x2 s2 = {0.f, 0.f};
#pragma unroll
    for (int i = 0; i < 3; ++i)
#pragma unroll
      for (int e = 0; e < 4; ++e) s2 += unpk(xv[i][e]);
    const float mean = xsum16(sum16(s2.x + s2.y)) * (1.0f / D_);
    f32x2 q2 = {0.f, 0.f}, q3 = {0.f, 0.f};
#pragma unroll
    for (int e = 0; e < 4; ++e) {
      const f32x2 d0 = unpk(xv[0][e]) - mean, d1 = unpk(xv[1][e]) - mean, d2 = unpk(xv[2][e]) - mean;
      q2 += d0 * d0; q2 += d1 * d1; q3 += d2 * d2;
    }
    const float var = xsum16(sum16(q2.x + q2.y + (c32 < 16 ? q3.x + q3.y : 0.f))) * (1.0f / D_);
    const float mq = bf16r(mean), rden = rden_of(var);
#pragma unroll
    for (int i = 0; i < 3; ++i) {
      u32x4 o;
#pragma unroll
      for (int e = 0; e < 4; ++e) { const f32x2 d = bf16r2(bf16r2(unpk(xv[i][e]) - mq) * rden); o[e] = pkh(d.x, d.y); }
      const int slot = c32 + 32 * i;
      if (i < 2 || c32 < 16) lds_st4(L0 + L0_TB + slot * 128 + (((2 * wave + (lane >> 5)) ^ (slot & 7)) * 16), o);
    }
    BAR();
#pragma unroll
    for (int u = 0; u < 3; ++u) {
      const int i = 256 * u + tid, slot = i >> 3, tk = (i & 7) ^ (slot & 7);
      const unsigned voff = (u < 2 || tid < 128) ? WS_XN + (4 * mbL + (jL >> 2)) * T32_D + slot * 512 + (8 * (jL & 3) + tk) * 16 : 0x40000000u;
      st16_sc1(lds_u4(L0 + L0_TB + min(i, 639) * 16), rws, voff, 0);
    }
    wq_issue(0);
    vmwait<4>();
    asm volatile("" : "+v"(epoch));
    tag = (unsigned)__builtin_amdgcn_readfirstlane(epoch) + 1u;
    if (lane == 0) st_u64(ws, flagoff(FL_XN, mbL, 4 * jL + wave), flagval(tag));
    if (mjob) {
      const unsigned cc[5] = {c4[0], c4[1], c4[2], c4[3], c1};
#pragma unroll
      for (int r = 0; r < 5; ++r) lds_st1(L0 + L0_SC + 20 * lane + 4 * r, pk2(siluf(blo(cc[r])), siluf(bhi(cc[r]))));
      asm volatile("s_waitcnt lgkmcnt(0)" ::: "memory");
      const f32x4 d = gemv16_run<20>(L0 + L0_JW, L0 + L0_SC, 0, lane);
      const unsigned pv = (lane & 1) ? pk2(d[2], d[3]) : pk2(d[0], d[1]);
      if ((lane & 15) < 2) st_u64(ws, WS_GRAN + (8 * munit + 2 * (lane >> 4) + (lane & 1)) * 8, ((u64)tag << 32) | pv);
    }
    wq_issue(1);
  }
  if (wave == 0) wait_flags(ws, flagoff(FL_XN, mb, lane), true, tag);
  BAR_RAW();
  u32x4 xf[40];
  const unsigned xfo = WS_XN + (4 * mb + wave) * T32_D + 16 * lane;
  const Rot R1 = mkrot(rW, 4 * soW, 40, 1024);
  {
    const int g0 = 160 * wave + lane;
    u32x2 ga[3];
#pragma unroll
    for (int ks = 0; ks < P1F0; ++ks) ald16_sc1(xf[ks], rws, xfo, fo40(R1, ks));
#pragma unroll
    for (int k = 0; k < 3; ++k) ald8_sc1(ga[k], rws, WS_GRAN + (k < 2 || lane < 32 ? g0 + 64 * k : g0) * 8, 0);
    wq_issue(2); wq_issue(3);
    vmwait<8>();
    asm volatile("" : "+v"(ga[0]), "+v"(ga[1]), "+v"(ga[2]));
    unsigned gv[3] = {ga[0][0], ga[1][0], ga[2][0]};
    if (!__all(ga[0][1] == tag && ga[1][1] == tag && ga[2][1] == tag))
      sweep<3>(ws, [&](int k) { return WS_GRAN + (g0 + 64 * k) * 8; }, [&](int k) { return k < 2 || lane < 32; }, tag, gv);
#pragma unroll
    for (int k = 0; k < 3; ++k) if (k < 2 || lane < 32) {
      const int g = g0 + 64 * k;
      f32x2 v = unpk(gv[k]);
      if (g >= 320) v = bf16r2(v + 1.0f);
      const int gp = g >= 320 ? g - 320 : g;
      int kp = (gp >> 3) - 4 * soW; kp += kp < 0 ? 40 : 0;
      lds_st1(L0 + (g >= 320 ? L1_VS : L1_VA) + kp * 32 + (gp & 7) * 4, pkh(v.x, v.y));
    }
  }
  BAR();

  const int jidx = 32 * (xcd >> 1) + ii, jper = (xcd & 1) ? 52 : 56;
  const bool ujob = jidx < 2 * jper;
  const int jmat = jidx >= jper, junit = ((xcd & 1) ? 56 : 0) + jidx - jmat * jper;
  auto jgi = [&](int k) { const int i = 64 * k + lane; return i < 80 ? 1280 + 80 * wave + i : i < 160 ? 960 + 80 * wave + i - 80 : 1280; };
  const rsrc_t rjw = mkrsrc(jmat ? wu : wg, D_ * F_ * 2);
  const unsigned jvo = (8 * ((lane >> 3) & 3) + 4 * (lane >> 5) + ((lane >> 1) & 3)) * 3456 + 16 * (lane & 1);
  const unsigned jso = sopaque(32 * (ujob ? junit : 0) + wave * 160 * 3456);
  u32x2 jg[3];
  f32x16 acc1[4];
  {
    const int h = lane >> 5;
    const unsigned wb = opaque(L0 + L1_W + 2 * h * 960 + ((lane & 15) >> 2) * 64 + ((lane >> 4) & 1) * 32 + (lane & 3) * 8);
    const unsigned wb3 = opaque(L0 + L1_W + 2 * h * 960 + 768 + ((lane & 15) >> 2) * 48 + ((lane >> 4) & 1) * 32 + (lane & 3) * 8);
    const unsigned wbh = opaque(wb + 61440), wb3h = opaque(wb3 + 61440), wbg = opaque(wb + 122880), wb3g = opaque(wb3 + 122880);
    const unsigned vb = opaque(L0 + L1_VS + h * 16);
    s16x8 w[3][4]; u32x4 hf[2], vs[2], va[2]; float ft[2][8];
    auto readw = [&](int ks, int b) {
      const int kl = ks % 16; const unsigned base = ks < 16 ? (b < 3 ? wb : wb3) : ks < 32 ? (b < 3 ? wbh : wb3h) : (b < 3 ? wbg : wb3g);
      const unsigned a = base + (b < 3 ? b * 256 : 0) + kl * 3840; return trfrag(a, a + 960); };
    auto reads = [&](int ks) { vs[ks & 1] = lds_u4(vb + ks * 32); };
    auto reada = [&](int ks) { va[ks & 1] = lds_u4(vb + 1280 + ks * 32); };
    auto fstage = [&](int ks, int e) {
      ft[ks & 1][2 * e] = fmix_lo(xf[ks][e], vs[ks & 1][e], va[ks & 1][e]);
      ft[ks & 1][2 * e + 1] = fmix_hi(xf[ks][e], vs[ks & 1][e], va[ks & 1][e]); };
    auto cstage = [&](int ks, int e) { hf[ks & 1][e] = pk2(ft[ks & 1][2 * e], ft[ks & 1][2 * e + 1]); };
#pragma unroll
    for (int b = 0; b < 4; ++b) { w[0][b] = readw(0, b); w[1][b] = readw(1, b); }
    reads(0); reada(0); reads(1); reada(1);
    vmwait<p1_wait_frag(0)>(xf[0]);
#pragma unroll
    for (int e = 0; e < 4; ++e) fstage(0, e);
#pragma unroll
    for (int e = 0; e < 4; ++e) cstage(0, e);
    vmwait<p1_wait_frag(1)>(xf[1]);
#pragma unroll
    for (int e = 0; e < 4; ++e) fstage(1, e);
    reads(2); reada(2);
    asm volatile("s_nop 1" : "+v"(hf[0]));
    SB();
    static_for<0, 40>([&](auto KS) {
      constexpr int ks = KS, c = ks & 1, wc = ks % 3, wn = (ks + 2) % 3;
      constexpr bool needf = ks + 2 < 40, needs = (ks & 3) == 2 && (ks + 2) / 4 >= 2 && ks + 2 < 40;
      constexpr int nf = needf ? p1_wait_frag(ks + 2) : 63, ns = needs ? p1_wait_stage((ks + 2) / 4) : 63;
      if constexpr (needs) { vmwait<(nf < ns ? nf : ns)>(); BAR_RAW(); }
      if constexpr (needf) vmwait<nf>(xf[ks + 2]);
      SB();
      if constexpr (ks == 0) gmfma_0(acc1[0], w[wc][0], hf[c]); else gmfma_a(acc1[0], w[wc][0], hf[c]);
      SB();
      if constexpr (ks + 2 < 40) w[wn][0] = readw(ks + 2, 0);
      if constexpr (ks + 1 < 40) { cstage(ks + 1, 0); cstage(ks + 1, 1); }
      if constexpr (ks + 3 < 40) reads(ks + 3);
      SB();
      if constexpr (ks == 0) gmfma_0(acc1[1], w[wc][1], hf[c]); else gmfma_a(acc1[1], w[wc][1], hf[c]);
      SB();
      if constexpr (ks + P1F0 < 40) ald16_sc1(xf[ks + P1F0], rws, xfo, fo40(R1, ks + P1F0));
      if constexpr (ks + 2 < 40) w[wn][1] = readw(ks + 2, 1);
      if constexpr (ks + 1 < 40) { cstage(ks + 1, 2); cstage(ks + 1, 3); }
      if constexpr (ks + 3 < 40) reada(ks + 3);
      SB();
      if constexpr (ks == 0) gmfma_0(acc1[2], w[wc][2], hf[c]); else gmfma_a(acc1[2], w[wc][2], hf[c]);
      SB();
      if constexpr (ks + 2 < 40) w[wn][2] = readw(ks + 2, 2);
      if constexpr (ks + 2 < 40) { fstage(ks + 2, 0); fstage(ks + 2, 1); }
      SB();
      if constexpr (ks == 0) gmfma_0(acc1[3], w[wc][3], hf[c]); else gmfma_a(acc1[3], w[wc][3], hf[c]);
      SB();
      if constexpr (ks < 24) wq_piece(ks / 4 + 4, ks & 3);
      if constexpr (ks >= 30 && ks < 35) dma16(rjw, jvo, jso + (ks - 30) * 32 * 3456, L0 + L2_JW + wave * 5120 + (ks - 30) * 1024);
      if constexpr (ks >= 35 && ks < 38) ald8_sc1(jg[ks - 35], rws, WS_GRAN + jgi(ks - 35) * 8, 0);
      if constexpr (ks + 2 < 40) w[wn][3] = readw(ks + 2, 3);
      if constexpr (ks + 2 < 40) { fstage(ks + 2, 2); fstage(ks + 2, 3); }
      SB();
    });
  }
  asm volatile("s_nop 11" : "+v"(acc1[0]), "+v"(acc1[1]), "+v"(acc1[2]), "+v"(acc1[3]));

  u32x4 qa0, qa1, qb0, qb1;
  {
    const int h = lane >> 5, t = lane & 31;
    u32x2 P[15];
#pragma unroll
    for (int i = 0; i < 15; ++i) { const f32x16& a = acc1[i >> 2]; const int r = 4 * (i & 3); P[i] = (u32x2){pk2(a[r], a[r + 1]), pk2(a[r + 2], a[r + 3])}; }
    const u32x2 one = {0x00003f80u, 0u}, zero = {0u, 0u};
    auto cat = [](u32x2 a, u32x2 b) { return (u32x4){a[0], a[1], b[0], b[1]}; };
    auto sel = [&](u32x2 a1, u32x2 a0) { return (u32x2){h ? a1[0] : a0[0], h ? a1[1] : a0[1]}; };
    const unsigned row = L0 + L2_KV + (256 + 32 * wave + t) * 48;
    lds_st4(row + 16 * h, cat(P[5], P[6]));
    lds_st4(row + KV_IMG + 16 * (h ^ 1), cat(sel(P[7], P[8]), sel(P[8], P[9])));
    lds_st4(row + h * KV_IMG + 32, cat(sel(P[9], P[7]), zero));
    lds_st4(row + 2 * KV_IMG + 16 * h, cat(P[10], P[11]));
    lds_st4(row + 3 * KV_IMG + 16 * (h ^ 1), cat(sel(P[12], P[13]), sel(P[13], P[14])));
    lds_st4(row + (2 + h) * KV_IMG + 32, cat(sel(P[14], P[12]), one));
    asm volatile("s_waitcnt lgkmcnt(0)" ::: "memory");
#pragma unroll
    for (int u = 0; u < 6; ++u) {
      const int i = 64 * u + lane, a = i / 96, o = (i - 96 * a) * 16;
      const u32x4 v = lds_u4(L0 + L2_KV + a * KV_IMG + (256 + 32 * wave) * 48 + o);
      st16_sc1(v, rws, WS_KV + ((mb * 16 + j) * 4 + a) * 6144 + wave * 1536 + o, 0);
    }
    qa0 = cat(P[0], P[1]);
    qa1 = cat(sel(zero, P[2]), sel(zero, one));
    qb0 = cat(sel(P[2], P[3]), sel(P[3], P[4]));
    qb1 = cat(sel(P[4], zero), sel(one, zero));
    publish(ws, FL_KV, mb, j, wave, lane, tag);
  }
  asm volatile("" : "+v"(jg[0]), "+v"(jg[1]), "+v"(jg[2]));
  {
    unsigned gv[3] = {jg[0][0], jg[1][0], jg[2][0]};
    if (!__all(jg[0][1] == tag && jg[1][1] == tag && jg[2][1] == tag))
      sweep<3>(ws, [&](int k) { return WS_GRAN + jgi(k) * 8; }, [&](int) { return true; }, tag, gv);
#pragma unroll
    for (int k = 0; k < 3; ++k) if (k < 2 || lane < 32) {
      const int i = 64 * k + lane;
      unsigned v = gv[k];
      if (i < 80) { const f32x2 t = unpk(v) + 1.0f; v = pk2(t.x, t.y); }
      lds_st1(L0 + L2_JV + (i < 80 ? 320 * wave + 4 * i : 1280 + 320 * wave + 4 * (i - 80)), v);
    }
    asm volatile("s_waitcnt lgkmcnt(0)" ::: "memory");
    const f32x4 d = gemv16_run<5>(L0 + L2_JW + wave * 5120, L0 + L2_JV + wave * 320, 1, lane);
    if ((lane & 15) < 2) lds_st4(L0 + L3_JP + wave * 128 + (2 * (lane >> 4) + (lane & 1)) * 16, __builtin_bit_cast(u32x4, d));
  }

  {
    const int blk = wave >> 1, src = mb - 2 + blk;
    if (src >= 0) {
      wait_flags(ws, flagoff(FL_KV, src, 4 * j + (lane & 3)), lane < 4, tag);
#pragma unroll
      for (int i = 0; i < 12; ++i) {
        const int a = 2 * (wave & 1) + i / 6, sub = i % 6;
        dma16_sc1(rws, 16 * lane, WS_KV + ((src * 16 + j) * 4 + a) * 6144 + sub * 1024, L0 + L2_KV + a * KV_IMG + blk * 6144 + sub * 1024);
      }
    } else {
#pragma unroll
      for (int i = 0; i < 12; ++i) {
        const int a = 2 * (wave & 1) + i / 6, o = (i % 6) * 1024 + 16 * lane;
        lds_st4(L0 + L2_KV + a * KV_IMG + blk * 6144 + o, (u32x4){0u, 0u, ((o % 48) == 32 && a < 2) ? 0x0000f149u : 0u, 0u});
      }
    }
  }
  const int th = jL & 1, nt = jL >> 1;
  const rsrc_t rwo = mkrsrc(wo, D_ * D_ * 2);
  unsigned woo[7], wol[7];
#pragma unroll
  for (int i = 0; i < 7; ++i) {
    const int pi = min(wave + 4 * i, 24), kc = pi / 5, pc = pi - 5 * kc;
    auto rowmap = [&](int g, int t, int q) { const int k = 32 * kc + 8 * g + 4 * t + q, hp = k / 40, r = k - 40 * hp; return 40 * hp + (r < 20 ? r : r < 24 ? r + 16 : r - 4); };
    woo[i] = opaque(w80_voff(pc, lane, nt, rowmap));
    wol[i] = pi * 1024;
  }
  auto wo_piece = [&](int gam, int i) { dma16(rwo, woo[i], ((gam + rL) & 3) * 160 * 1280, L0 + (gam < 3 ? L3_W0 + gam * 25600 : L3_W1) + wol[i]); };
  vmwait<0>();
  BAR();
  if (ujob && wave == 1) {
    const unsigned pa = L0 + L3_JP + (2 * (lane >> 4) + (lane & 1)) * 16;
    const f32x4 d = (lds_f4(pa) + lds_f4(pa + 128)) + (lds_f4(pa + 256) + lds_f4(pa + 384));
    if ((lane & 15) < 2) {
#pragma unroll
      for (int r = 0; r < 4; ++r) st_u64(ws, WS_UV + ((jmat * 108 + junit) * 32 + (lane & 15) * 16 + 4 * (lane >> 4) + r) * 8, ((u64)tag << 32) | __float_as_uint(d[r]));
    }
  }


  {
    const rsrc_t rgt = mkrsrc(wave < 2 ? wg : wu, D_ * F_ * 2);
    const rsrc_t rdt = mkrsrc(wd, F_ * D_ * 2);
    const unsigned tdo = (135 * bid + min(64 * wave + lane, 134)) * 64;
    unsigned xro[3];
#pragma unroll
    for (int i = 0; i < 3; ++i) { const int idx = 64 * min(3 * wave + i, 9) + lane, row = idx / 10; xro[i] = (128 * mbL + 64 * th + row) * 1280 + 160 * nt + (idx - 10 * row) * 16; }
    const unsigned tgo = (20 * ii + 10 * (wave & 1) + (lane >> 5)) * 3456 + (xcd & 1) * 1792 + 64 * min(lane & 31, (xcd & 1) ? 25 : 27);
    const int h = lane >> 5, t = lane & 31;
    f32x16 mfirst, mdiag;
#pragma unroll
    for (int r = 0; r < 16; ++r) { const int key = (r & 3) + 8 * (r >> 2) + 4 * h; mfirst[r] = key > t ? 0.f : NEGBIG; mdiag[r] = key > t ? NEGBIG : 0.f; }
    const unsigned krow = L0 + L2_KV + (32 * wave + t) * 48;
    const unsigned ka0 = opaque(krow + 16 * h), ka1 = opaque(krow + 32), kb0 = opaque(krow + KV_IMG + 16 * (h ^ 1)), kb1 = opaque(krow + KV_IMG + 32);
    const unsigned vrow = L0 + L2_KV + 2 * KV_IMG + (32 * wave + 4 * h + ((lane & 15) >> 2)) * 48 + ((lane >> 4) & 1) * 32 + (lane & 3) * 8;
    const unsigned vra = opaque(vrow), vrb = opaque(vrow + KV_IMG);
    f32x16 sa[9], sb[9];
    float e[8];
    auto pfma = [&](const f32x16& s, int half, float mc) {
#pragma unroll
      for (int i = 0; i < 8; i += 2) {
        const f32x2 r = __builtin_elementwise_fma((f32x2){s[8 * half + i], s[8 * half + i + 1]}, (f32x2){SCORE_C, SCORE_C}, (f32x2){mc, mc});
        e[i] = r.x; e[i + 1] = r.y;
      } };
    auto pexp = [&]() {
#pragma unroll
      for (int i = 0; i < 8; ++i) e[i] = exp2_(e[i]); };
    auto pcvt = [&]() { return (u32x4){pk2(e[0], e[1]), pk2(e[2], e[3]), pk2(e[4], e[5]), pk2(e[6], e[7])}; };
    auto qk0 = [&](f32x16& s, int kb, u32x4 a0, u32x4 q0) { if (kb == 0) smfma_c(s, a0, q0, mfirst); else if (kb == 8) smfma_c(s, a0, q0, mdiag); else smfma_0(s, a0, q0); };
    auto maxhalf = [&](float (&m)[4], const f32x16& s, int half) {
#pragma unroll
      for (int r = 8 * half; r < 8 * half + 8; r += 2) m[(r >> 1) & 3] = fmaxf(fmaxf(m[(r >> 1) & 3], s[r]), s[r + 1]); };
    auto vfrag = [&](unsigned base, int kb, int half) { const unsigned a = base + (32 * kb + 16 * half) * 48; return trfrag(a, a + 8 * 48); };
    auto maxblk = [&](float (&m)[4], const f32x16& s) {
#pragma unroll
      for (int r = 0; r < 16; r += 2) m[(r >> 1) & 3] = fmaxf(fmaxf(m[(r >> 1) & 3], s[r]), s[r + 1]); };
    u32x4 k0 = lds_u4(ka0), k1 = lds_u4(ka1);
    float mx[4] = {NEGBIG, NEGBIG, NEGBIG, NEGBIG};
#pragma unroll
    for (int kb = 0; kb < 9; ++kb) {
      u32x4 n0, n1;
      SB();
      qk0(sa[kb], kb, k0, qa0);
      SB();
      n0 = kb < 8 ? lds_u4(ka0 + (kb + 1) * 1536) : lds_u4(kb0);
      if (kb == 2) asm volatile("s_nop 3" : "+v"(sa[0]));
      if (kb >= 2) maxhalf(mx, sa[kb - 2], 0);
      SB();
      smfma_a(sa[kb], k1, qa1);
      SB();
      n1 = kb < 8 ? lds_u4(ka1 + (kb + 1) * 1536) : lds_u4(kb1);
      if (kb >= 2) { maxhalf(mx, sa[kb - 2], 1); wo_piece(2, kb - 2); }
      k0 = n0; k1 = n1;
    }
    SB();
    asm volatile("s_nop 7" : "+v"(sa[7]));
    maxblk(mx, sa[7]);
    sfence(sa[8]);
    maxblk(mx, sa[8]);
    const float mca = -SCORE_C * xmax32(fmaxf(fmaxf(mx[0], mx[1]), fmaxf(mx[2], mx[3])));
    f32x16 oa = {}, ob = {};
    s16x8 v0 = vfrag(vra, 0, 0), v1 = vfrag(vra, 0, 1);
#pragma unroll
    for (int r = 0; r < 4; ++r) mx[r] = NEGBIG;
    SB();
#pragma unroll
    for (int kb = 0; kb < 9; ++kb) {
      qk0(sb[kb], kb, k0, qb0);
      SB();
      if (kb < 8) k0 = lds_u4(kb0 + (kb + 1) * 1536);
      pfma(sa[kb], 0, mca); pexp();
      SB();
      smfma_a(sb[kb], k1, qb1);
      SB();
      if (kb < 8) k1 = lds_u4(kb1 + (kb + 1) * 1536);
      const u32x4 p0 = pcvt();
      pfma(sa[kb], 1, mca);
      SB();
      oa = mfma32(v0, p0, oa);
      SB();
      v0 = kb < 8 ? vfrag(vra, kb + 1, 0) : vfrag(vrb, 0, 0);
      pexp();
      const u32x4 p1 = pcvt();
      if (kb >= 1) maxhalf(mx, sb[kb - 1], 0);
      SB();
      oa = mfma32(v1, p1, oa);
      SB();
      v1 = kb < 8 ? vfrag(vra, kb + 1, 1) : vfrag(vrb, 0, 1);
      if (kb >= 1) maxhalf(mx, sb[kb - 1], 1);
      if (kb < 7) wo_piece(0, kb);
      if (kb < 5) touch(rgt, tgo, kb * 2 * 3456, L0 + L_DUMMY);
      if (kb == 5) touch(rdt, tdo, 0, L0 + L_DUMMY);
      if (kb >= 6) dma16(rx, xro[kb >= 6 ? kb - 6 : 0], 0, L0 + L3_XR + min(3 * wave + kb - 6, 9) * 1024);
      SB();
    }
    sfence(sb[8]);
    maxblk(mx, sb[8]);
    const float mcb = -SCORE_C * xmax32(fmaxf(fmaxf(mx[0], mx[1]), fmaxf(mx[2], mx[3])));
    SB();
    pfma(sb[0], 0, mcb); pexp();
    SB();
#pragma unroll
    for (int kb = 0; kb < 9; ++kb) {
      const u32x4 p0 = pcvt();
      pfma(sb[kb], 1, mcb);
      SB();
      ob = mfma32(v0, p0, ob);
      SB();
      if (kb < 8) v0 = vfrag(vrb, kb + 1, 0);
      pexp();
      const u32x4 p1 = pcvt();
      if (kb < 8) pfma(sb[kb + 1], 0, mcb);
      SB();
      ob = mfma32(v1, p1, ob);
      SB();
      if (kb < 8) { v1 = vfrag(vrb, kb + 1, 1); pexp(); }
      if (kb < 7) wo_piece(1, kb);
      SB();
    }
    const auto la = __builtin_amdgcn_permlane32_swap(__float_as_uint(oa[8]), __float_as_uint(oa[8]), false, false);
    const auto lb = __builtin_amdgcn_permlane32_swap(__float_as_uint(ob[8]), __float_as_uint(ob[8]), false, false);
    const float ia = rcpf_(__uint_as_float(la[1])), ib = rcpf_(__uint_as_float(lb[1]));
    const unsigned obase = WS_O + (4 * mb + wave) * T32_D + 5 * j * 512;
    u32x4 va, vb2, vc;
#pragma unroll
    for (int e = 0; e < 4; ++e) { va[e] = pk2(oa[2 * e] * ia, oa[2 * e + 1] * ia); vb2[e] = pk2(ob[2 * e] * ib, ob[2 * e + 1] * ib); }
    vc = (u32x4){pk2(oa[8] * ia, oa[9] * ia), pk2(oa[10] * ia, oa[11] * ia), pk2(ob[8] * ib, ob[9] * ib), pk2(ob[10] * ib, ob[11] * ib)};
    st16_sc1(va, rws, obase + 16 * lane, 0);
    st16_sc1(vb2, rws, obase + 3 * 512 + 16 * lane, 0);
    st16_sc1(vc, rws, lane < 32 ? obase + 2 * 512 + 16 * lane : 0x40000000u, 0);
    publish(ws, FL_O, mb, j, wave, lane, tag);
  }
  BAR();
  const int t16 = lane & 15, g4 = lane >> 4;
  const int tok3 = 128 * mbL + 64 * th + 16 * wave + t16;
  auto goff = [&](int k) { const int i = min(64 * k + lane, 119), kind = i / 40, r = i - 40 * kind; return WS_GRAN + ((kind == 0 ? 640 : kind == 1 ? 1280 : 1600) + 40 * nt + r) * 8; };
  u32x2 vg2[2];
#pragma unroll
  for (int i = 0; i < 7; ++i) wo_piece(3, i);
#pragma unroll
  for (int k = 0; k < 2; ++k) ald8_sc1(vg2[k], rws, goff(k), 0);
  const int nu = j < 12 ? 7 : 6, u0 = j < 12 ? 7 * j : 84 + 6 * (j - 12), uodd = u0 & 1;
  u32x2 fo, uvg[7];
  const unsigned uvl = WS_UV + ((lane >> 5) * 108 * 32 + (lane & 31)) * 8;
#pragma unroll
  for (int p = 0; p < 7; ++p) ald8_sc1(uvg[p], rws, uvl, (u0 + unit_of(p, uodd, nu)) * 256);
  ald8_sc1(fo, rws, flagoff(FL_O, mbL, lane), 0);
  const rsrc_t rgu = mkrsrc(wave < 2 ? wg : wu, D_ * F_ * 2);
  unsigned guo[7];
#pragma unroll
  for (int i = 0; i < 7; ++i) {
    const int isup = wave >> 1, idx = 64 * (7 * (wave & 1) + i) + lane, rq = idx / 56, rem = idx - 56 * rq;
    int q, p, hf;
    if (rem < 48) { q = (rem >> 2) & 3; const int c = (rem & 3) ^ (isup ? 2 : 0); p = 2 * (rem >> 4) + (c >> 1); hf = c & 1; } else { q = (rem - 48) >> 1; p = 6; hf = rem & 1; }
    guo[i] = opaque((4 * rq + q) * 3456 + 32 * (u0 + unit_of(p, uodd, nu)) + 16 * hf);
  }
  vmwait<0>();
  asm volatile("" : "+v"(vg2[0]), "+v"(vg2[1]), "+v"(fo));
  unsigned uvv[7];
  {
    bool ok = true;
#pragma unroll
    for (int p = 0; p < 7; ++p) { asm volatile("" : "+v"(uvg[p])); uvv[p] = uvg[p][0]; ok &= uvg[p][1] == tag; }
    if (wave == 1 && !__all(ok)) sweep<7>(ws, [&](int p) { return uvl + (u0 + unit_of(p, uodd, nu)) * 256; }, [&](int) { return true; }, tag, uvv);
  }
  if (wave == 0 && !flags_up(fo, tag)) wait_flags(ws, flagoff(FL_O, mbL, lane), true, tag);
  if (wave == 2) {
    unsigned gv[2] = {vg2[0][0], vg2[1][0]};
    if (!__all(vg2[0][1] == tag && vg2[1][1] == tag)) sweep<2>(ws, goff, [&](int) { return true; }, tag, gv);
#pragma unroll
    for (int k = 0; k < 2; ++k) if (k == 0 || lane < 56) {
      const int i = 64 * k + lane, kind = i / 40, r = i - 40 * kind;
      const int ct = r >> 3, p = (nt & 1) ? (ct == 0 ? 4 : ct - 1) : ct;
      f32x2 v = unpk(gv[k]);
      if (kind == 1) v = bf16r2(v + 1.0f);
      lds_st2f(L0 + L3_VEC + kind * 320 + p * 64 + (r & 7) * 8, v);
    }
  }
  if (wave == 3 && bid == 0) {
    unsigned fv[16];
    sweep<16>(ws, [&](int k) { return flagoff(FL_KV, k, lane); }, [&](int) { return true; }, tag, fv);
    if (lane == 0) __hip_atomic_store((GAS unsigned*)(ws + WS_EPOCH), tag, __ATOMIC_RELAXED, __HIP_MEMORY_SCOPE_AGENT);
  }
  BAR();

  const unsigned b80_0 = g4 * 1024 + ((lane & 15) >> 2) * 64 + (g4 & 1) * 32 + (lane & 3) * 8;
  const unsigned b80_1 = b80_0 ^ 32u;
  const unsigned b80_2 = 4096 + 8 * lane;
  struct B80 { unsigned a, b, c; };
  auto b80 = [&](unsigned kbase) { return B80{opaque(kbase + b80_0), opaque(kbase + b80_1), opaque(kbase + b80_2)}; };
  auto w80 = [&](const B80& B, int koff, int p) { const unsigned a = (p == 4 ? B.c : ((p & 1) ? B.b : B.a) + (p >> 1) * 256) + koff; return trfrag(a, a + 512); };

  u32x2 x1p[5];
  const int so4 = 0;
  auto gu_piece = [&](int st, int i) { dma16(rgu, guo[i], rot10(so4, st) * 64 * 3456, L0 + (st % 5) * L4_STAGE + (wave >> 1) * L4_U + (wave & 1) * 7168 + i * 1024); };
  {
    u32x4 of[20];
    const unsigned ofb = WS_O + (4 * mbL + 2 * th + (wave >> 1)) * T32_D + g4 * 512 + ((wave & 1) * 16 + t16) * 16;
    const Rot R3 = mkrot(rL, 5 * rL, 20, 2048);
    auto fo20 = [&](int s) { return R3.b[s < 5 ? 0 : s < 10 ? 1 : s < 15 ? 2 : 3] + s * 2048; };
#pragma unroll
    for (int s = 0; s < P3F0; ++s) ald16_sc1(of[s], rws, ofb, fo20(s));
    f32x4 acc[5];
    const B80 B0 = b80(L0 + L3_W0), B1 = b80(L0 + L3_W0 + 10 * 5120), B2 = b80(L0 + L3_W1);
    auto rd3 = [&](int s, int p) { return s < 10 ? w80(B0, s * 5120, p) : s < L3_NPRE ? w80(B1, (s - 10) * 5120, p) : w80(B2, (s - L3_NPRE) * 5120, p); };
    s16x8 w[3][5];
#pragma unroll
    for (int p = 0; p < 5; ++p) { w[0][p] = rd3(0, p); w[1][p] = rd3(1, p); }
    SB();
    static_for<0, 20>([&](auto SS) {
      constexpr int s = SS;
      vmwait<p3_wait_frag(s)>(of[s]);
      SB();
#pragma unroll
      for (int p = 0; p < 5; ++p) {
        if (s == 0) hmfma_0(acc[p], w[s % 3][p], of[s]); else hmfma_a(acc[p], w[s % 3][p], of[s]);
        SB();
        if (p == 0 && s + P3F0 < 20) ald16_sc1(of[s + P3F0 < 20 ? s + P3F0 : 0], rws, ofb, fo20(s + P3F0));
        if (s + 2 < 20) w[(s + 2) % 3][p] = rd3(s + 2, p);
        SB();
      }
    });
    asm volatile("s_nop 7" : "+v"(acc[0]), "+v"(acc[1]), "+v"(acc[2]), "+v"(acc[3]), "+v"(acc[4]));
    BAR();
    f32x2 xv[5][2]; f32x2 s2 = {0.f, 0.f};
    u32x2 yp[5];
    const unsigned vecb = opaque(L0 + L3_VEC + g4 * 16), xrb = opaque(L0 + L3_XR + (16 * wave + t16) * 160 + g4 * 8);
    u32x2 xres[5];
#pragma unroll
    for (int p = 0; p < 5; ++p) xres[p] = *(LAS u32x2*)(size_t)(xrb + 32 * ct_of(p, nt & 1));
#pragma unroll
    for (int p = 0; p < 5; ++p) {
      const f32x4 ga = lds_f4(vecb + p * 64), sb1 = lds_f4(vecb + 320 + p * 64);
#pragma unroll
      for (int r = 0; r < 2; ++r) {
        const f32x2 at = bf16r2((f32x2){acc[p][2 * r], acc[p][2 * r + 1]});
        const f32x2 xs = unpk(xres[p][r]) + bf16r2(at * (f32x2){ga[2 * r], ga[2 * r + 1]});
        x1p[p][r] = pk2(xs.x, xs.y);
        xv[p][r] = unpk(x1p[p][r]);
        s2 += xv[p][r];
        const f32x2 y = xv[p][r] * (f32x2){sb1[2 * r], sb1[2 * r + 1]};
        yp[p][r] = pk2(y.x, y.y);
      }
    }
    const unsigned yb = WS_Y + (4 * mbL + 2 * th + (wave >> 1)) * T32_D + 10 * nt * 512 + ((wave & 1) * 16 + t16) * 16;
#pragma unroll
    for (int pp = 0; pp < 3; ++pp) {
      const int pa = 2 * pp, pb = pp < 2 ? 2 * pp + 1 : 4;
      const auto r0 = __builtin_amdgcn_permlane16_swap(yp[pa][0], yp[pb][0], false, false), r1 = __builtin_amdgcn_permlane16_swap(yp[pa][1], yp[pb][1], false, false);
      const int ct = (g4 & 1) ? ct_of(pb, nt & 1) : ct_of(pa, nt & 1);
      st16_sc1((u32x4){r0[0], r1[0], r0[1], r1[1]}, rws, (pp < 2 || !(g4 & 1)) ? yb + (2 * ct + (g4 >> 1)) * 512 : 0x40000000u, 0);
    }
    const float mt = xsum32(xsum16(s2.x + s2.y)) * (1.0f / 80);
    f32x2 m2 = {0.f, 0.f};
#pragma unroll
    for (int p = 0; p < 5; ++p)
#pragma unroll
      for (int r = 0; r < 2; ++r) { const f32x2 d = xv[p][r] - mt; m2 += d * d; }
    const float m2s = xsum32(xsum16(m2.x + m2.y));
    if (lane < 16) st_u64(ws, WS_STAT + (((mbL * 2 + th) * 8 + nt) * 64 + 16 * wave + t16) * 8, ((u64)__float_as_uint(m2s) << 32) | __float_as_uint(mt));
#pragma unroll
    for (int i = 0; i < 7; ++i) gu_piece(0, i);
    vmwait<7>();
    if (lane == 0) st_u64(ws, flagoff(FL_Y, mbL, 4 * jL + wave), flagval(tag));
  }
  BAR_RAW();

  for (int st = 1; st < 4; ++st)
#pragma unroll
    for (int i = 0; i < 7; ++i) gu_piece(st, i);
  u32x2 fy;
  ald8_sc1(fy, rws, flagoff(FL_Y, mb, lane), 0);
  if (wave == 1) {
#pragma unroll
    for (int p = 0; p < 7; ++p) lds_st1(L0 + L4_UV + (p * 64 + lane) * 4, uvv[p]);
  }
  const rsrc_t rwd = mkrsrc(wd, F_ * D_ * 2);
  unsigned wdg[8];
  {
    auto rm = [](int g, int t, int q) { return 16 * (g >> 1) + 8 * t + 4 * (g & 1) + q; };
    const unsigned b0 = w80_voff(0, lane, nt, rm), b1 = w80_voff(1, lane, nt, rm), b4 = w80_voff(4, lane, nt, rm);
#pragma unroll
    for (int i = 0; i < 8; ++i) {
      const int pc = min(8 * wave + i, 29), kl = pc / 5, pp = pc - 5 * kl;
      wdg[i] = opaque((pp == 4 ? b4 : (pp & 1) ? b1 : b0) + (kl * 32 + (pp == 4 ? 0 : 16 * (pp >> 1))) * 1280);
    }
  }
  auto wd_piece = [&](int G, int i) { const int g = G + goL; dma16(rwd, wdg[i], (g >= 9 ? g - 9 : g) * 6 * 32 * 1280, L0 + (G % 5) * 30720 + min(8 * wave + i, 29) * 1024); };
  const Rot R5 = mkrot(rD, 6 * goL, 54, 2048);
  auto fo54 = [&](int s) { return R5.b[s < 12 ? 0 : s < 30 ? 1 : s < 42 ? 2 : 3] + s * 2048; };
  vmwait<0>(); asm volatile("" : "+v"(fy));
  if (wave == 0 && !flags_up(fy, tag)) wait_flags(ws, flagoff(FL_Y, mb, lane), true, tag);
  vmwait<0>();
  BAR();
  u32x2 stv[4];
#pragma unroll
  for (int i = 0; i < 4; ++i) { const int T = 32 * wave + (lane & 31); ald8_sc1(stv[i], rws, WS_STAT + (((mb * 2 + (T >> 6)) * 8 + 4 * (lane >> 5) + i) * 64 + (T & 63)) * 8, 0); }
  u32x4 yf[40];
  const unsigned yfo = WS_Y + (4 * mb + wave) * T32_D + 16 * lane;
  const Rot R4 = mkrot(0, 0, 40, 1024);
#pragma unroll
  for (int ks = 0; ks < P4F0; ++ks) ald16_sc1(yf[ks], rws, yfo, fo40(R4, ks));
  f32x16 acc4[7];
  {
    const int h = lane >> 5, blk = (lane >> 4) & 1, q = (lane & 15) >> 2;
    const unsigned c0 = L0 + blk * L4_U + 2 * h * 896 + q * 64 + blk * 32 + (lane & 3) * 8;
    const unsigned c2 = L0 + blk * L4_U + 2 * h * 896 + 768 + q * 32 + (lane & 3) * 8;
    unsigned cb0[5], cb1[5], cb2[5];
#pragma unroll
    for (int sl = 0; sl < 5; ++sl) { cb0[sl] = opaque(c0 + sl * L4_STAGE); cb1[sl] = opaque((c0 ^ 32u) + sl * L4_STAGE); cb2[sl] = opaque(c2 + sl * L4_STAGE); }
    auto readw = [&](int ks, int p) {
      const int sl = (ks >> 2) % 5;
      const unsigned a = (p == 6 ? cb2[sl] : ((p & 1) ? cb1[sl] : cb0[sl]) + (p >> 1) * 256) + (ks & 3) * 4 * 896;
      return trfrag(a, a + 896); };
    s16x8 w[3][7];
#pragma unroll
    for (int p = 0; p < 7; ++p) { w[0][p] = readw(0, p); w[1][p] = readw(1, p); }
    SB();
    static_for<0, 40>([&](auto KS) {
      constexpr int ks = KS, c = ks % 3, n = (ks + 2) % 3;
      if constexpr ((ks & 3) == 2) {
        constexpr int st = (ks + 2) / 4;
        if constexpr (st >= 4 && st <= 9) vmwait<p4_wait_stage(st)>();
        BAR_RAW();
      }
      vmwait<p4_wait_frag(ks)>(yf[ks]);
      if constexpr (ks == 0) asm volatile("" : "+v"(stv[0]), "+v"(stv[1]), "+v"(stv[2]), "+v"(stv[3]));
      SB();
#pragma unroll
      for (int p = 0; p < 7; ++p) {
        if (ks == 0) gmfma_0(acc4[p], w[c][p], yf[ks]); else gmfma_a(acc4[p], w[c][p], yf[ks]);
        SB();
        if (p == 1 && ks + P4F0 < 40) ald16_sc1(yf[ks + P4F0 < 40 ? ks + P4F0 : 0], rws, yfo, fo40(R4, ks + P4F0));
        if (p == 3 && p4_pieces(ks) > 0) gu_piece((ks + 14) / 4, 2 * ((ks + 14) % 4));
        if (p == 5 && p4_pieces(ks) > 1) gu_piece((ks + 14) / 4, 2 * ((ks + 14) % 4) + 1);
        if (p == 3 && p4_touch(ks) > 0) wd_piece(p4_wd_before(ks) / 8, p4_wd_before(ks) % 8);
        if (p == 5 && p4_touch(ks) > 1) wd_piece((p4_wd_before(ks) + 1) / 8, (p4_wd_before(ks) + 1) % 8);
        if (ks + 2 < 40) w[n][p] = readw(ks + 2, p);
        SB();
      }
    });
  }
  asm volatile("s_nop 11" : "+v"(acc4[0]), "+v"(acc4[1]), "+v"(acc4[2]), "+v"(acc4[3]), "+v"(acc4[4]), "+v"(acc4[5]), "+v"(acc4[6]));
  {
    const int h = lane >> 5;
    float ms = 0.f;
#pragma unroll
    for (int i = 0; i < 4; ++i) ms += __uint_as_float(stv[i][0]);
    const float mean = xsum32(ms) * 0.125f;
    float m2 = 0.f;
#pragma unroll
    for (int i = 0; i < 4; ++i) { const float d = __uint_as_float(stv[i][0]) - mean; m2 += __uint_as_float(stv[i][1]) + 80.0f * d * d; }
    const float rs = rden_of(xsum32(m2) * (1.0f / D_)), nm = -rs * bf16r(mean);
    const unsigned ab = WS_ACT + (4 * mb + wave) * T32_F + 16 * lane, uvb = opaque(L0 + L4_UV + 16 * h);
#pragma unroll
    for (int p = 0; p < 7; ++p) {
      if (p < 6 || nu == 7) {
        const unsigned tb = uvb + p * 256;
        u32x4 ov;
#pragma unroll
        for (int half = 0; half < 2; ++half) {
          const f32x4 ug = lds_f4(tb + 32 * half), vg = lds_f4(tb + 64 + 32 * half), uu = lds_f4(tb + 128 + 32 * half), vu = lds_f4(tb + 192 + 32 * half);
#pragma unroll
          for (int r = 0; r < 2; ++r) {
            const f32x2 ag = {acc4[p][4 * half + 2 * r], acc4[p][4 * half + 2 * r + 1]}, au = {acc4[p][8 + 4 * half + 2 * r], acc4[p][8 + 4 * half + 2 * r + 1]};
            const f32x2 g = rs * ag + (nm * (f32x2){ug[2 * r], ug[2 * r + 1]} + (f32x2){vg[2 * r], vg[2 * r + 1]});
            const f32x2 u = rs * au + (nm * (f32x2){uu[2 * r], uu[2 * r + 1]} + (f32x2){vu[2 * r], vu[2 * r + 1]});
            const f32x2 e = g * (-LOG2E);
            const f32x2 den = (f32x2){exp2_(e.x), exp2_(e.y)} + 1.0f;
            const f32x2 a = (g * u) * (f32x2){rcpf_(den.x), rcpf_(den.y)};
            ov[2 * half + r] = pk2(a.x, a.y);
          }
        }
        st16_sc1(ov, rws, ab + (u0 + unit_of(p, uodd, nu)) * 1024, 0);
      }
    }
#pragma unroll
    for (int i = 4; i < 8; ++i) wd_piece(2, i);
#pragma unroll
    for (int i = 0; i < 3; ++i) wd_piece(3, i);
    vmwait<7>();
    if (lane == 0) st_u64(ws, flagoff(FL_ACT, mb, 4 * j + wave), flagval(tag));
  }
  BAR();

#pragma unroll
  for (int i = 3; i < 8; ++i) wd_piece(3, i);
#pragma unroll
  for (int i = 0; i < 8; ++i) wd_piece(4, i);
  if (wave == 0) wait_flags(ws, flagoff(FL_ACT, mbL, lane), true, tag);
  vmwait<0>();
  BAR();
  {
    u32x4 af[54];
    const unsigned afb = WS_ACT + (4 * mbL + 2 * th + (wave >> 1)) * T32_F + g4 * 512 + ((wave & 1) * 16 + t16) * 16;
#pragma unroll
    for (int s = 0; s < P5F0; ++s) ald16_sc1(af[s], rws, afb, fo54(s));
    f32x4 acc[5];
    const B80 B5[3] = {b80(L0), b80(L0 + 61440), b80(L0 + 122880)};
    auto rd5 = [&](int s, int p) { const int sl = s % 30; return w80(B5[sl / 12], (sl % 12) * 5120, p); };
    s16x8 w[3][5];
#pragma unroll
    for (int p = 0; p < 5; ++p) { w[0][p] = rd5(0, p); w[1][p] = rd5(1, p); }
    SB();
    static_for<0, 54>([&](auto SS) {
      constexpr int s = SS;
      if constexpr (s % 6 == 4 && s + 2 < 54) {
        constexpr int g = (s + 2) / 6;
        if constexpr (g >= 5) vmwait<p5_wait_group(g)>();
        BAR_RAW();
      }
      vmwait<p5_wait_frag(s)>(af[s]);
      SB();
#pragma unroll
      for (int p = 0; p < 5; ++p) {
        if (s == 0) hmfma_0(acc[p], w[s % 3][p], af[s]); else hmfma_a(acc[p], w[s % 3][p], af[s]);
        SB();
        constexpr int r = (s + 20) % 6, i0 = r < 2 ? 2 * r : r + 2;
        if (p == 0 && s + P5F0 < 54) ald16_sc1(af[s + P5F0 < 54 ? s + P5F0 : 0], rws, afb, fo54(s + P5F0));
        if (p == 2 && p5_pieces(s) > 0) wd_piece((s + 20) / 6, i0);
        if (p == 4 && p5_pieces(s) > 1) wd_piece((s + 20) / 6, i0 + 1);
        if (s + 2 < 54) w[(s + 2) % 3][p] = rd5(s + 2, p);
        SB();
      }
    });
    asm volatile("s_nop 7" : "+v"(acc[0]), "+v"(acc[1]), "+v"(acc[2]), "+v"(acc[3]), "+v"(acc[4]));
    u32x2 op[5];
    const unsigned gbb = opaque(L0 + L3_VEC + 640 + g4 * 16);
#pragma unroll
    for (int p = 0; p < 5; ++p) {
      const f32x4 gb = lds_f4(gbb + p * 64);
#pragma unroll
      for (int r = 0; r < 2; ++r) {
        const f32x2 dn = bf16r2((f32x2){acc[p][2 * r], acc[p][2 * r + 1]});
        const f32x2 o = unpk(x1p[p][r]) + bf16r2(dn * (f32x2){gb[2 * r], gb[2 * r + 1]});
        op[p][r] = pk2(o.x, o.y);
      }
    }
    const rsrc_t ro = mkrsrc(out, S_ * D_ * 2);
#pragma unroll
    for (int pp = 0; pp < 3; ++pp) {
      const int pa = 2 * pp, pb = pp < 2 ? 2 * pp + 1 : 4;
      const auto r0 = __builtin_amdgcn_permlane16_swap(op[pa][0], op[pb][0], false, false), r1 = __builtin_amdgcn_permlane16_swap(op[pa][1], op[pb][1], false, false);
      const int ct = (g4 & 1) ? ct_of(pb, nt & 1) : ct_of(pa, nt & 1);
      __builtin_amdgcn_raw_buffer_store_b128((u32x4){r0[0], r1[0], r0[1], r1[1]}, ro, (pp < 2 || !(g4 & 1)) ? tok3 * 1280 + 160 * nt + 32 * ct + 16 * (g4 >> 1) : 0x40000000u, 0, 16);
    }
  }
  asm volatile(".Ladaln_code_end:");
}

extern "C" void kernel_launch(void* const* d_in, const int* in_sizes, int n_in, void* d_out, int out_size, void* d_ws, size_t ws_size,
                              hipStream_t stream) {
  (void)in_sizes; (void)n_in; (void)out_size; (void)ws_size;
  adaln_block_kernel<<<256, 256, 0, stream>>>((const char*)d_in[0], (const char*)d_in[1], (const char*)d_in[2], (const char*)d_in[4], (const char*)d_in[5],
                                              (const char*)d_in[6], (const char*)d_in[7], (const char*)d_in[8], (char*)d_out, (char*)d_ws);
}
```

```cpp
#include <hip/hip_runtime.h>
#include <stdint.h>
#include <type_traits>

#define LAS __attribute__((address_space(3)))
#define GAS __attribute__((address_space(1)))
#define DI __device__ __forceinline__
typedef float f32x2 __attribute__((ext_vector_type(2)));
typedef float f32x4 __attribute__((ext_vector_type(4)));
typedef float f32x16 __attribute__((ext_vector_type(16)));
typedef unsigned u32x2 __attribute__((ext_vector_type(2)));
typedef unsigned u32x4 __attribute__((ext_vector_type(4)));
typedef short s16x4 __attribute__((ext_vector_type(4)));
typedef short s16x8 __attribute__((ext_vector_type(8)));
typedef __bf16 bf16x2 __attribute__((ext_vector_type(2)));
typedef __bf16 bf16x8 __attribute__((ext_vector_type(8)));
typedef unsigned long long u64;
typedef __amdgpu_buffer_rsrc_t rsrc_t;

constexpr int S_ = 2048, D_ = 640, F_ = 1728;
constexpr int T32_D = 80 * 512;
constexpr int T32_F = 216 * 512;

constexpr int WS_EPOCH = 0;
constexpr int WS_TMO = 256;
constexpr int WS_FLAG = 29 << 20;
constexpr int FL_XN = 0, FL_KV = 1, FL_O = 2, FL_Y = 3, FL_ACT = 4;
constexpr int WS_GRAN = 256 * 1024;
constexpr int WS_UV = 320 * 1024;
constexpr int WS_STAT = 512 * 1024;
constexpr int WS_XN = 1 << 20;
constexpr int WS_KV = 4 << 20;
constexpr int WS_O = 12 << 20;
constexpr int WS_Y = 16 << 20;
constexpr int WS_ACT = 20 << 20;
constexpr int WS_USED = 30 << 20;

constexpr int LDS_BYTES = 163840;
constexpr int L1_W = 0;
constexpr int L0_TB = 122880;
constexpr int L0_JW = 133120;
constexpr int L1_VS = 153600, L1_VA = 154880;
constexpr int L0_SC = 158720;
constexpr int L2_KV = 0;
constexpr int KV_IMG = 384 * 48;
constexpr int L3_W0 = 4 * KV_IMG;
constexpr int L3_NPRE = 15;
constexpr int L3_W1 = 0;
constexpr int L2_JW = 73728, L2_JV = 94208;
constexpr int L3_JP = 161984;
constexpr int L3_XR = 150528;
constexpr int L_DUMMY = 162816;
constexpr int L3_VEC = 161024;
constexpr int L4_STAGE = 30720, L4_U = 14464;
constexpr int L4_UV = 153600;
static_assert(L3_XR + 64 * 160 <= L3_VEC && L3_W0 + L3_NPRE * 5120 <= L3_XR, "P3 residual rows");
static_assert(L3_W0 + L3_NPRE * 5120 <= L3_VEC && L3_VEC + 960 <= LDS_BYTES, "P3 LDS map");
static_assert(30 * 5120 <= L3_VEC, "P5 LDS map");
static_assert(L4_UV + 1792 <= L3_VEC, "P4 LDS map");
static_assert(L3_VEC + 960 <= L3_JP && L3_JP + 512 <= L_DUMMY && L_DUMMY + 256 <= LDS_BYTES, "small LDS areas");

constexpr float LOG2E = 1.4426950408889634f;
constexpr float SCORE_C = 0.22360679774997896f * 1.4426950408889634f;
constexpr float NEGBIG = -1.0e30f;
constexpr unsigned SPIN_CAP = 1u << 15;

#define SB() __builtin_amdgcn_sched_barrier(0)
#define BAR() do { asm volatile("s_waitcnt lgkmcnt(0)" ::: "memory"); __builtin_amdgcn_s_barrier(); asm volatile("" ::: "memory"); } while (0)
#define BAR_RAW() do { asm volatile("" ::: "memory"); __builtin_amdgcn_s_barrier(); asm volatile("" ::: "memory"); } while (0)
#define MEMFENCE() asm volatile("" ::: "memory")
template <int N> DI void vmwait() { asm volatile("s_waitcnt vmcnt(%0)" :: "n"(N) : "memory"); }
template <int N> DI void vmwait(u32x4& d) { asm volatile("s_waitcnt vmcnt(%1)" : "+v"(d) : "n"(N) : "memory"); }

DI unsigned pk2(float a, float b) { bf16x2 v = __builtin_convertvector((f32x2){a, b}, bf16x2); return __builtin_bit_cast(unsigned, v); }
DI float blo(unsigned v) { return __uint_as_float(v << 16); }
DI float bhi(unsigned v) { return __uint_as_float(v & 0xffff0000u); }
DI float bf16r(float a) { return blo(pk2(a, a)); }
DI f32x2 bf16r2(f32x2 a) { const unsigned p = pk2(a.x, a.y); return (f32x2){blo(p), bhi(p)}; }
DI f32x2 unpk(unsigned p) { return (f32x2){blo(p), bhi(p)}; }
DI float rcpf_(float x) { return __builtin_amdgcn_rcpf(x); }
DI float exp2_(float x) { return __builtin_amdgcn_exp2f(x); }
DI float siluf(float x) { return x * rcpf_(1.0f + exp2_(-LOG2E * x)); }
DI float rden_of(float var) { return rcpf_(bf16r(__builtin_amdgcn_sqrtf(bf16r(bf16r(var) + 1e-5f)))); }
template <int CTRL> DI float dpp_add(float x) { return x + __int_as_float(__builtin_amdgcn_update_dpp(0, __float_as_int(x), CTRL, 0xf, 0xf, true)); }
DI float sum16(float x) { x = dpp_add<0xB1>(x); x = dpp_add<0x4E>(x); x = dpp_add<0x141>(x); return dpp_add<0x140>(x); }
DI float xsum16(float x) { auto r = __builtin_amdgcn_permlane16_swap(__float_as_uint(x), __float_as_uint(x), false, false); return __uint_as_float(r[0]) + __uint_as_float(r[1]); }
DI float xsum32(float x) { auto r = __builtin_amdgcn_permlane32_swap(__float_as_uint(x), __float_as_uint(x), false, false); return __uint_as_float(r[0]) + __uint_as_float(r[1]); }
DI float xmax32(float x) { auto r = __builtin_amdgcn_permlane32_swap(__float_as_uint(x), __float_as_uint(x), false, false); return fmaxf(__uint_as_float(r[0]), __uint_as_float(r[1])); }
DI rsrc_t mkrsrc(const void* p, unsigned bytes) { return __builtin_amdgcn_make_buffer_rsrc((void*)p, (short)0, (int)bytes, 0x00020000); }
DI void st16_sc1(u32x4 v, rsrc_t r, unsigned voff, unsigned soff) { __builtin_amdgcn_raw_buffer_store_b128(v, r, voff, soff, 16); }
DI void ald16_sc1(u32x4& d, rsrc_t r, unsigned voff, unsigned soff) {
  asm volatile("buffer_load_dwordx4 %0, %1, %2, %3 offen sc1" : "=v"(d) : "v"(voff), "s"(r), "s"(soff) : "memory");
}
DI void ald16(u32x4& d, rsrc_t r, unsigned voff, unsigned soff) {
  asm volatile("buffer_load_dwordx4 %0, %1, %2, %3 offen" : "=v"(d) : "v"(voff), "s"(r), "s"(soff) : "memory");
}
DI void ald4(unsigned& d, rsrc_t r, unsigned voff, unsigned soff) {
  asm volatile("buffer_load_dword %0, %1, %2, %3 offen" : "=v"(d) : "v"(voff), "s"(r), "s"(soff) : "memory");
}
DI void ald8_sc1(u32x2& d, rsrc_t r, unsigned voff, unsigned soff) {
  asm volatile("buffer_load_dwordx2 %0, %1, %2, %3 offen sc1" : "=v"(d) : "v"(voff), "s"(r), "s"(soff) : "memory");
}
DI void touch(rsrc_t r, unsigned voff, unsigned soff, unsigned ldsaddr) {
  asm volatile("s_mov_b32 m0, %0\n\ts_nop 0\n\tbuffer_load_dword %1, %2, %3 offen lds" :: "s"(ldsaddr), "v"(voff), "s"(r), "s"(soff) : "memory");
}
DI void dma16_sc1(rsrc_t r, unsigned voff, unsigned soff, unsigned ldsaddr) {
  asm volatile("s_mov_b32 m0, %0\n\ts_nop 0\n\tbuffer_load_dwordx4 %1, %2, %3 offen sc1 lds" :: "s"(ldsaddr), "v"(voff), "s"(r), "s"(soff) : "memory");
}
DI void dma16(rsrc_t r, unsigned voff, unsigned soff, unsigned ldsaddr) {
  asm volatile("s_mov_b32 m0, %0\n\ts_nop 0\n\tbuffer_load_dwordx4 %1, %2, %3 offen lds" :: "s"(ldsaddr), "v"(voff), "s"(r), "s"(soff) : "memory");
}
DI u64 ld_u64(const char* ws, unsigned off) { return __hip_atomic_load((GAS u64*)(ws + off), __ATOMIC_RELAXED, __HIP_MEMORY_SCOPE_AGENT); }
DI void st_u64(char* ws, unsigned off, u64 v) { __hip_atomic_store((GAS u64*)(ws + off), v, __ATOMIC_RELAXED, __HIP_MEMORY_SCOPE_AGENT); }
DI u64 flagval(unsigned tag) { return ((u64)tag << 32) | (u64)(~tag); }
DI unsigned flagoff(int kind, int mb, int jw) { return WS_FLAG + ((kind * 16 + mb) * 64 + jw) * 128; }
DI void publish(char* ws, int kind, int mb, int j, int wave, int lane, unsigned tag) {
  vmwait<0>();
  if (lane == 0) st_u64(ws, flagoff(kind, mb, 4 * j + wave), flagval(tag));
}
DI bool spin_fail(unsigned& spins, char* ws, unsigned tag) {
  __builtin_amdgcn_s_sleep(1);
  if (++spins > SPIN_CAP) { __hip_atomic_store((GAS unsigned*)(ws + WS_TMO), tag, __ATOMIC_RELAXED, __HIP_MEMORY_SCOPE_AGENT); return true; }
  return (spins & 127u) == 0u && (unsigned)__builtin_amdgcn_readfirstlane(__hip_atomic_load((GAS unsigned*)(ws + WS_TMO), __ATOMIC_RELAXED, __HIP_MEMORY_SCOPE_AGENT)) == tag;
}
DI bool flags_up(const u32x2& f, unsigned tag) { return __all(f[0] == ~tag && f[1] == tag); }
DI void wait_flags(char* ws, unsigned off, bool on, unsigned tag) {
  const u64 want = flagval(tag);
#pragma clang loop unroll(disable)
  for (unsigned spins = 0;;) {
    u64 v = want;
    if (on) v = ld_u64(ws, off);
    if (__all(v == want)) break;
    if (spin_fail(spins, ws, tag)) break;
  }
  MEMFENCE();
}
template <int N, class OFF, class ON> DI void sweep(char* ws, OFF off, ON on, unsigned tag, unsigned (&val)[N]) {
#pragma clang loop unroll(disable)
  for (unsigned spins = 0;;) {
    bool ok = true;
#pragma unroll
    for (int k = 0; k < N; ++k) { u64 g = (u64)tag << 32; if (on(k)) g = ld_u64(ws, off(k)); val[k] = (unsigned)g; ok &= (unsigned)(g >> 32) == tag; }
    if (__all(ok)) break;
    if (spin_fail(spins, ws, tag)) break;
  }
  MEMFENCE();
}
DI s16x8 trfrag(unsigned a0, unsigned a1) {
  const s16x4 lo = __builtin_amdgcn_ds_read_tr16_b64_v4i16((LAS s16x4*)(size_t)a0);
  const s16x4 hi = __builtin_amdgcn_ds_read_tr16_b64_v4i16((LAS s16x4*)(size_t)a1);
  return __builtin_shufflevector(lo, hi, 0, 1, 2, 3, 4, 5, 6, 7);
}
DI f32x4 lds_f4(unsigned a) { return *(LAS f32x4*)(size_t)a; }
DI u32x4 lds_u4(unsigned a) { return *(LAS u32x4*)(size_t)a; }
DI void lds_st4(unsigned a, u32x4 v) { *(LAS u32x4*)(size_t)a = v; }
DI void lds_st1(unsigned a, unsigned v) { *(LAS unsigned*)(size_t)a = v; }
DI void lds_st2f(unsigned a, f32x2 v) { *(LAS f32x2*)(size_t)a = v; }
DI unsigned opaque(unsigned v) { asm volatile("" : "+v"(v)); return v; }
DI unsigned sopaque(unsigned v) { asm volatile("" : "+s"(v)); return v; }
struct Rot { unsigned b[4]; };
DI Rot mkrot(int r, int shift, int N, int step) {
  const unsigned a = shift * step, w = N * step;
  return Rot{{sopaque(a), sopaque(a - (r >= 3 ? w : 0)), sopaque(a - (r >= 2 ? w : 0)), sopaque(a - (r >= 1 ? w : 0))}};
}
DI f32x16 mfma32(s16x8 a, u32x4 b, f32x16 c) { return __builtin_amdgcn_mfma_f32_32x32x16_bf16(__builtin_bit_cast(bf16x8, a), __builtin_bit_cast(bf16x8, b), c, 0, 0, 0); }
DI f32x4 mfma16(s16x8 a, u32x4 b, f32x4 c) { return __builtin_amdgcn_mfma_f32_16x16x32_bf16(__builtin_bit_cast(bf16x8, a), __builtin_bit_cast(bf16x8, b), c, 0, 0, 0); }
DI void smfma_c(f32x16& d, u32x4 a, u32x4 b, const f32x16& c) { asm("v_mfma_f32_32x32x16_bf16 %0, %1, %2, %3" : "=&v"(d) : "v"(a), "v"(b), "v"(c)); }
DI void smfma_0(f32x16& d, u32x4 a, u32x4 b) { asm("v_mfma_f32_32x32x16_bf16 %0, %1, %2, 0" : "=&v"(d) : "v"(a), "v"(b)); }
DI void smfma_a(f32x16& d, u32x4 a, u32x4 b) { asm("v_mfma_f32_32x32x16_bf16 %0, %1, %2, %0" : "+v"(d) : "v"(a), "v"(b)); }
DI void gmfma_0(f32x16& d, s16x8 a, u32x4 b) { asm("v_mfma_f32_32x32x16_bf16 %0, %1, %2, 0" : "=&v"(d) : "a"(a), "v"(b)); }
DI void gmfma_a(f32x16& d, s16x8 a, u32x4 b) { asm("v_mfma_f32_32x32x16_bf16 %0, %1, %2, %0" : "+v"(d) : "a"(a), "v"(b)); }
DI void hmfma_0(f32x4& d, s16x8 a, u32x4 b) { asm("v_mfma_f32_16x16x32_bf16 %0, %1, %2, 0" : "=&v"(d) : "a"(a), "v"(b)); }
DI void hmfma_a(f32x4& d, s16x8 a, u32x4 b) { asm("v_mfma_f32_16x16x32_bf16 %0, %1, %2, %0" : "+v"(d) : "a"(a), "v"(b)); }
DI void sfence(f32x16& d) { asm volatile("s_nop 11" : "+v"(d)); }
DI float fmix_lo(unsigned x, unsigned s, unsigned a) { float r; asm("v_fma_mix_f32 %0, %1, %2, %3 op_sel_hi:[1,1,1]" : "=v"(r) : "v"(x), "v"(s), "v"(a)); return r; }
DI float fmix_hi(unsigned x, unsigned s, unsigned a) { float r; asm("v_fma_mix_f32 %0, %1, %2, %3 op_sel:[1,1,1] op_sel_hi:[1,1,1]" : "=v"(r) : "v"(x), "v"(s), "v"(a)); return r; }
DI unsigned pkh(float a, float b) { return __builtin_bit_cast(unsigned, __builtin_amdgcn_cvt_pkrtz(a, b)); }


template <int NS> DI void gemv16_issue(rsrc_t rw, unsigned rowstride, unsigned srcbyte, unsigned ldsw, int lane) {
  const unsigned vo = (8 * ((lane >> 3) & 3) + 4 * (lane >> 5) + ((lane >> 1) & 3)) * rowstride + 16 * (lane & 1);
#pragma unroll 5
  for (int s = 0; s < NS; ++s) dma16(rw, vo, s * 32 * rowstride + srcbyte, ldsw + s * 1024);
}
template <int NS> DI f32x4 gemv16_run(unsigned ldsw, unsigned ldsv, int vmask, int lane) {
  f32x4 acc0 = {}, acc1 = {};
  const unsigned wa = opaque(ldsw + 8 * lane), va = opaque(ldsv + (lane & vmask) * 1280 + (lane >> 4) * 16);
  s16x8 w[3]; u32x4 v[3];
#pragma unroll
  for (int s = 0; s < 2; ++s) { w[s] = trfrag(wa + s * 1024, wa + s * 1024 + 512); v[s] = lds_u4(va + s * 64); }
  SB();
#pragma unroll
  for (int s = 0; s < NS; ++s) {
    if (s + 2 < NS) { w[(s + 2) % 3] = trfrag(wa + (s + 2) * 1024, wa + (s + 2) * 1024 + 512); v[(s + 2) % 3] = lds_u4(va + (s + 2) * 64); }
    SB();
    if (s & 1) acc1 = mfma16(w[s % 3], v[s % 3], acc1); else acc0 = mfma16(w[s % 3], v[s % 3], acc0);
    SB();
  }
  return acc0 + acc1;
}

DI int ct_of(int p, int odd) { return odd ? (p < 4 ? p + 1 : 0) : p; }
DI int unit_of(int p, int odd, int nu) { const int u = odd ? (p < 6 ? p + 1 : 0) : p; return u < nu ? u : nu - 1; }

template <class RM> DI unsigned w80_voff(int piece, int lane, int nt, RM rowmap) {
  const int odd = nt & 1;
  int g, t, q, p, hf;
  if (piece < 4) { g = piece; t = lane >> 5; const int su = (lane >> 4) & 1; q = (lane >> 2) & 3; const int c = (lane & 3) ^ (2 * (piece & 1)); p = 2 * su + (c >> 1); hf = c & 1; }
  else { t = lane >> 5; g = (lane >> 3) & 3; q = (lane >> 1) & 3; p = 4; hf = lane & 1; }
  return rowmap(g, t, q) * 1280 + 160 * nt + 32 * ct_of(p, odd) + 16 * hf;
}

template <class OPS> constexpr int vm_sum(OPS ops, int lo, int hi) { int n = 0; for (int k = lo; k <= hi; ++k) if (k >= 0) n += ops(k); return n; }
constexpr int P1F0 = 10;
constexpr int p1_pieces(int k) { return k < 20 ? 1 : 0; }
constexpr int p1_ops(int k) { return (k < 40 - P1F0 ? 1 : 0) + p1_pieces(k) + (k >= 30 && k < 38 ? 1 : 0); }
constexpr int p1_wait_frag(int f) {
  return f < P1F0 ? 4 + vm_sum(p1_ops, 0, f - 3) : p1_pieces(f - P1F0) + vm_sum(p1_ops, f - P1F0 + 1, f - 3);
}
constexpr int p1_wait_stage(int st) { return st == 4 ? vm_sum(p1_ops, 0, 13) : vm_sum(p1_ops, 4 * st - 16, 4 * st - 3); }
constexpr int P4F0 = 8;
constexpr int p4_pieces(int k) { return k >= 2 && k <= 25 ? ((k + 14) % 4 == 3 ? 1 : 2) : 0; }
constexpr int p4_wd_before(int k) { return k <= 26 ? 0 : k == 27 ? 2 : k == 28 ? 3 : k == 29 ? 5 : k == 30 ? 6 : k == 31 ? 8 : k == 32 ? 9 : k == 33 ? 11 : k == 34 ? 12 : k == 35 ? 14 : k <= 40 ? 16 + (k - 36) : 20; }
constexpr int p4_touch(int k) { return p4_wd_before(k + 1) - p4_wd_before(k); }
constexpr int p4_ops(int k) { return (k < 40 - P4F0 ? 1 : 0) + p4_pieces(k) + p4_touch(k); }
constexpr int p4_wait_frag(int ks) { return ks < P4F0 ? (P4F0 - 1 - ks) + vm_sum(p4_ops, 0, ks - 1) : p4_pieces(ks - P4F0) + p4_touch(ks - P4F0) + vm_sum(p4_ops, ks - P4F0 + 1, ks - 1); }
constexpr int p4_wait_stage(int st) { return vm_sum(p4_ops, 4 * st - 10, 4 * st - 3); }
constexpr int P5F0 = 12;
constexpr int p5_pieces(int k) { return k >= 10 && k <= 33 ? ((k + 20) % 6 < 2 ? 2 : 1) : 0; }
constexpr int p5_ops(int k) { return (k < 54 - P5F0 ? 1 : 0) + p5_pieces(k); }
constexpr int p5_wait_frag(int s) { return s < P5F0 ? (P5F0 - 1 - s) + vm_sum(p5_ops, 0, s - 1) : p5_pieces(s - P5F0) + vm_sum(p5_ops, s - P5F0 + 1, s - 1); }
constexpr int p5_wait_group(int g) { return vm_sum(p5_ops, 6 * g - 14, 6 * g - 3); }
constexpr int P3F0 = 6;
constexpr int p3_ops(int k) { return k < 20 - P3F0 ? 1 : 0; }
constexpr int p3_wait_frag(int s) { return s < P3F0 ? (P3F0 - 1 - s) + vm_sum(p3_ops, 0, s - 1) : vm_sum(p3_ops, s - P3F0 + 1, s - 1); }

template <int I, int N, class Fn> DI void static_for(Fn&& f) { if constexpr (I < N) { f(std::integral_constant<int, I>{}); static_for<I + 1, N>(f); } }

__global__ __launch_bounds__(256, 1) void adaln_block_kernel(const char* __restrict__ x, const char* __restrict__ wqkv, const char* __restrict__ wo,
                                                             const char* __restrict__ wg, const char* __restrict__ wu, const char* __restrict__ wd,
                                                             const char* __restrict__ cvec, const char* __restrict__ wmod, char* out, char* ws) {
  __shared__ __attribute__((aligned(16))) char smem[LDS_BYTES];
  const unsigned L0 = (unsigned)(size_t)(LAS char*)smem;
  const int tid = threadIdx.x, lane = tid & 63;
  const int wave = __builtin_amdgcn_readfirstlane(tid >> 6);
  const int bid = blockIdx.x, xcd = bid & 7, ii = bid >> 3;
  const int mbL = 2 * xcd + (ii >> 4), jL = ii & 15;
  const int mb = 4 * (xcd >> 1) + (ii >> 3), j = 8 * (xcd & 1) + (ii & 7);
  const int rW = mb & 3, rL = 0, rD = 2 * (mbL & 1) + (jL & 1);
  const int soW = (5 * rW) >> 1;
  const int goL = rD == 3 ? 7 : 2 * rD;
  const rsrc_t rx = mkrsrc(x, S_ * D_ * 2), rws = mkrsrc(ws, WS_USED);

  unsigned epoch, tag;
  const rsrc_t rwq = mkrsrc(wqkv, D_ * 3 * D_ * 2);
  unsigned wqo[4];
  auto rot10 = [&](int so, int st) { const int g = st + so; return g >= 10 ? g - 10 : g; };
  const int so1 = (int)sopaque(soW);
  auto wq_piece = [&](int st, int i) { dma16(rwq, wqo[i], rot10(so1, st) * 64 * 3840, L0 + L1_W + (st * 15 + min(4 * wave + i, 14)) * 1024); };
  auto fo40 = [&](const Rot& R, int ks) { return R.b[ks < 12 ? 0 : ks < 20 ? 1 : ks < 32 ? 2 : 3] + ks * 1024; };
  auto wq_issue = [&](int st) {
#pragma unroll
    for (int i = 0; i < 4; ++i) wq_piece(st, i);
  };
  {
    const int c32 = lane & 31, tok = 128 * mbL + 8 * jL + 2 * wave + (lane >> 5);
    u32x4 xv[3], c4 = {}; unsigned c1 = 0;
    ald16(xv[0], rx, tok * 1280 + c32 * 16, 0);
    ald16(xv[1], rx, tok * 1280 + c32 * 16 + 512, 0);
    ald16(xv[2], rx, c32 < 16 ? tok * 1280 + c32 * 16 + 1024 : 0x40000000u, 0);
    if (wave == 3) {
      u64 pc; int len;
      asm volatile("s_getpc_b64 %0\n.Ladaln_code_begin:\n\ts_mov_b32 %1, .Ladaln_code_end-.Ladaln_code_begin" : "=s"(pc), "=s"(len));
      len = len > 0 && len < (1 << 18) ? len : 0;
      touch(mkrsrc((const void*)pc, len), (ii * 40 + min(lane, 39)) * 64, 0, L0 + L_DUMMY);
    }
    const int munit = ii < 10 ? 10 * xcd + ii : 80 + 20 * xcd + (ii - 10);
    const bool mjob = wave == 0 && ii < 30;
    if (mjob) {
      const rsrc_t rc = mkrsrc(cvec, D_ * 2);
      ald16(c4, rc, 20 * lane, 0); ald4(c1, rc, 20 * lane + 16, 0);
    }
#pragma unroll
    for (int i = 0; i < 4; ++i) {
      const int idx = 64 * min(4 * wave + i, 14) + lane, rq = idx / 60, rem = idx - 60 * rq;
      int q, ch;
      if (rem < 48) { q = (rem >> 2) & 3; ch = 4 * (rem >> 4) + (rem & 3); } else { const int r2 = rem - 48; q = r2 / 3; ch = 12 + r2 - 3 * q; }
      const int seg = ch / 5;
      wqo[i] = (4 * rq + q) * 3840 + seg * 1280 + 80 * j + (ch - 5 * seg) * 16;
    }
    asm volatile("buffer_load_dword %0, %1, %2, 0 offen sc1" : "=v"(epoch) : "v"(WS_EPOCH), "s"(rws) : "memory");
    if (mjob) {
      gemv16_issue<20>(mkrsrc(wmod, D_ * 6 * D_ * 2), 7680, 32 * munit, L0 + L0_JW, lane);
      vmwait<21>();
    } else {
      vmwait<1>();
    }
    asm volatile("" : "+v"(xv[0]), "+v"(xv[1]), "+v"(xv[2]), "+v"(c4), "+v"(c1));
    f32x2 s2 = {0.f, 0.f};
#pragma unroll
    for (int i = 0; i < 3; ++i)
#pragma unroll
      for (int e = 0; e < 4; ++e) s2 += unpk(xv[i][e]);
    const float mean = xsum16(sum16(s2.x + s2.y)) * (1.0f / D_);
    f32x2 q2 = {0.f, 0.f}, q3 = {0.f, 0.f};
#pragma unroll
    for (int e = 0; e < 4; ++e) {
      const f32x2 d0 = unpk(xv[0][e]) - mean, d1 = unpk(xv[1][e]) - mean, d2 = unpk(xv[2][e]) - mean;
      q2 += d0 * d0; q2 += d1 * d1; q3 += d2 * d2;
    }
    const float var = xsum16(sum16(q2.x + q2.y + (c32 < 16 ? q3.x + q3.y : 0.f))) * (1.0f / D_);
    const float mq = bf16r(mean), rden = rden_of(var);
#pragma unroll
    for (int i = 0; i < 3; ++i) {
      u32x4 o;
#pragma unroll
      for (int e = 0; e < 4; ++e) { const f32x2 d = bf16r2(bf16r2(unpk(xv[i][e]) - mq) * rden); o[e] = pkh(d.x, d.y); }
      const int slot = c32 + 32 * i;
      if (i < 2 || c32 < 16) lds_st4(L0 + L0_TB + slot * 128 + (((2 * wave + (lane >> 5)) ^ (slot & 7)) * 16), o);
    }
    BAR();
#pragma unroll
    for (int u = 0; u < 3; ++u) {
      const int i = 256 * u + tid, slot = i >> 3, tk = (i & 7) ^ (slot & 7);
      const unsigned voff = (u < 2 || tid < 128) ? WS_XN + (4 * mbL + (jL >> 2)) * T32_D + slot * 512 + (8 * (jL & 3) + tk) * 16 : 0x40000000u;
      st16_sc1(lds_u4(L0 + L0_TB + min(i, 639) * 16), rws, voff, 0);
    }
    wq_issue(0);
    vmwait<4>();
    asm volatile("" : "+v"(epoch));
    tag = (unsigned)__builtin_amdgcn_readfirstlane(epoch) + 1u;
    if (lane == 0) st_u64(ws, flagoff(FL_XN, mbL, 4 * jL + wave), flagval(tag));
    if (mjob) {
      const unsigned cc[5] = {c4[0], c4[1], c4[2], c4[3], c1};
#pragma unroll
      for (int r = 0; r < 5; ++r) lds_st1(L0 + L0_SC + 20 * lane + 4 * r, pk2(siluf(blo(cc[r])), siluf(bhi(cc[r]))));
      asm volatile("s_waitcnt lgkmcnt(0)" ::: "memory");
      const f32x4 d = gemv16_run<20>(L0 + L0_JW, L0 + L0_SC, 0, lane);
      const unsigned pv = (lane & 1) ? pk2(d[2], d[3]) : pk2(d[0], d[1]);
      if ((lane & 15) < 2) st_u64(ws, WS_GRAN + (8 * munit + 2 * (lane >> 4) + (lane & 1)) * 8, ((u64)tag << 32) | pv);
    }
    wq_issue(1); wq_issue(2); wq_issue(3);
  }
  if (wave == 0) wait_flags(ws, flagoff(FL_XN, mb, lane), true, tag);
  BAR_RAW();
  u32x4 xf[40];
  const unsigned xfo = WS_XN + (4 * mb + wave) * T32_D + 16 * lane;
  const Rot R1 = mkrot(rW, 4 * soW, 40, 1024);
  {
    const int g0 = 160 * wave + lane;
    u32x2 ga[3];
#pragma unroll
    for (int ks = 0; ks < P1F0; ++ks) ald16_sc1(xf[ks], rws, xfo, fo40(R1, ks));
#pragma unroll
    for (int k = 0; k < 3; ++k) ald8_sc1(ga[k], rws, WS_GRAN + (k < 2 || lane < 32 ? g0 + 64 * k : g0) * 8, 0);
    wq_issue(4);
    vmwait<4>();
    asm volatile("" : "+v"(ga[0]), "+v"(ga[1]), "+v"(ga[2]));
    unsigned gv[3] = {ga[0][0], ga[1][0], ga[2][0]};
    if (!__all(ga[0][1] == tag && ga[1][1] == tag && ga[2][1] == tag))
      sweep<3>(ws, [&](int k) { return WS_GRAN + (g0 + 64 * k) * 8; }, [&](int k) { return k < 2 || lane < 32; }, tag, gv);
#pragma unroll
    for (int k = 0; k < 3; ++k) if (k < 2 || lane < 32) {
      const int g = g0 + 64 * k;
      f32x2 v = unpk(gv[k]);
      if (g >= 320) v = bf16r2(v + 1.0f);
      const int gp = g >= 320 ? g - 320 : g;
      int kp = (gp >> 3) - 4 * soW; kp += kp < 0 ? 40 : 0;
      lds_st1(L0 + (g >= 320 ? L1_VS : L1_VA) + kp * 32 + (gp & 7) * 4, pkh(v.x, v.y));
    }
  }
  BAR();

  const int jidx = 32 * (xcd >> 1) + ii, jper = (xcd & 1) ? 52 : 56;
  const bool ujob = jidx < 2 * jper;
  const int jmat = jidx >= jper, junit = ((xcd & 1) ? 56 : 0) + jidx - jmat * jper;
  auto jgi = [&](int k) { const int i = 64 * k + lane; return i < 80 ? 1280 + 80 * wave + i : i < 160 ? 960 + 80 * wave + i - 80 : 1280; };
  const rsrc_t rjw = mkrsrc(jmat ? wu : wg, D_ * F_ * 2);
  const unsigned jvo = (8 * ((lane >> 3) & 3) + 4 * (lane >> 5) + ((lane >> 1) & 3)) * 3456 + 16 * (lane & 1);
  const unsigned jso = sopaque(32 * (ujob ? junit : 0) + wave * 160 * 3456);
  u32x2 jg[3];
  f32x16 acc1[4];
  {
    const int h = lane >> 5;
    const unsigned wb = opaque(L0 + L1_W + 2 * h * 960 + ((lane & 15) >> 2) * 64 + ((lane >> 4) & 1) * 32 + (lane & 3) * 8);
    const unsigned wb3 = opaque(L0 + L1_W + 2 * h * 960 + 768 + ((lane & 15) >> 2) * 48 + ((lane >> 4) & 1) * 32 + (lane & 3) * 8);
    const unsigned wbh = opaque(wb + 61440), wb3h = opaque(wb3 + 61440), wbg = opaque(wb + 122880), wb3g = opaque(wb3 + 122880);
    const unsigned vb = opaque(L0 + L1_VS + h * 16);
    s16x8 w[3][4]; u32x4 hf[2], vs[2], va[2]; float ft[2][8];
    auto readw = [&](int ks, int b) {
      const int kl = ks % 16; const unsigned base = ks < 16 ? (b < 3 ? wb : wb3) : ks < 32 ? (b < 3 ? wbh : wb3h) : (b < 3 ? wbg : wb3g);
      const unsigned a = base + (b < 3 ? b * 256 : 0) + kl * 3840; return trfrag(a, a + 960); };
    auto reads = [&](int ks) { vs[ks & 1] = lds_u4(vb + ks * 32); };
    auto reada = [&](int ks) { va[ks & 1] = lds_u4(vb + 1280 + ks * 32); };
    auto fstage = [&](int ks, int e) {
      ft[ks & 1][2 * e] = fmix_lo(xf[ks][e], vs[ks & 1][e], va[ks & 1][e]);
      ft[ks & 1][2 * e + 1] = fmix_hi(xf[ks][e], vs[ks & 1][e], va[ks & 1][e]); };
    auto cstage = [&](int ks, int e) { hf[ks & 1][e] = pk2(ft[ks & 1][2 * e], ft[ks & 1][2 * e + 1]); };
#pragma unroll
    for (int b = 0; b < 4; ++b) { w[0][b] = readw(0, b); w[1][b] = readw(1, b); }
    reads(0); reada(0); reads(1); reada(1);
    vmwait<p1_wait_frag(0)>(xf[0]);
#pragma unroll
    for (int e = 0; e < 4; ++e) fstage(0, e);
#pragma unroll
    for (int e = 0; e < 4; ++e) cstage(0, e);
    vmwait<p1_wait_frag(1)>(xf[1]);
#pragma unroll
    for (int e = 0; e < 4; ++e) fstage(1, e);
    reads(2); reada(2);
    asm volatile("s_nop 1" : "+v"(hf[0]));
    SB();
    static_for<0, 40>([&](auto KS) {
      constexpr int ks = KS, c = ks & 1, wc = ks % 3, wn = (ks + 2) % 3;
      constexpr bool needf = ks + 2 < 40, needs = (ks & 3) == 2 && (ks + 2) / 4 >= 4 && ks + 2 < 40;
      constexpr int nf = needf ? p1_wait_frag(ks + 2) : 63, ns = needs ? p1_wait_stage((ks + 2) / 4) : 63;
      if constexpr (needs) { vmwait<(nf < ns ? nf : ns)>(); BAR_RAW(); }
      if constexpr (needf) vmwait<nf>(xf[ks + 2]);
      SB();
      if constexpr (ks == 0) gmfma_0(acc1[0], w[wc][0], hf[c]); else gmfma_a(acc1[0], w[wc][0], hf[c]);
      SB();
      if constexpr (ks + 2 < 40) w[wn][0] = readw(ks + 2, 0);
      if constexpr (ks + 1 < 40) { cstage(ks + 1, 0); cstage(ks + 1, 1); }
      if constexpr (ks + 3 < 40) reads(ks + 3);
      SB();
      if constexpr (ks == 0) gmfma_0(acc1[1], w[wc][1], hf[c]); else gmfma_a(acc1[1], w[wc][1], hf[c]);
      SB();
      if constexpr (ks + P1F0 < 40) ald16_sc1(xf[ks + P1F0], rws, xfo, fo40(R1, ks + P1F0));
      if constexpr (ks + 2 < 40) w[wn][1] = readw(ks + 2, 1);
      if constexpr (ks + 1 < 40) { cstage(ks + 1, 2); cstage(ks + 1, 3); }
      if constexpr (ks + 3 < 40) reada(ks + 3);
      SB();
      if constexpr (ks == 0) gmfma_0(acc1[2], w[wc][2], hf[c]); else gmfma_a(acc1[2], w[wc][2], hf[c]);
      SB();
      if constexpr (ks + 2 < 40) w[wn][2] = readw(ks + 2, 2);
      if constexpr (ks + 2 < 40) { fstage(ks + 2, 0); fstage(ks + 2, 1); }
      SB();
      if constexpr (ks == 0) gmfma_0(acc1[3], w[wc][3], hf[c]); else gmfma_a(acc1[3], w[wc][3], hf[c]);
      SB();
      if constexpr (ks < 20) wq_piece(ks / 4 + 5, ks & 3);
      if constexpr (ks >= 30 && ks < 35) dma16(rjw, jvo, jso + (ks - 30) * 32 * 3456, L0 + L2_JW + wave * 5120 + (ks - 30) * 1024);
      if constexpr (ks >= 35 && ks < 38) ald8_sc1(jg[ks - 35], rws, WS_GRAN + jgi(ks - 35) * 8, 0);
      if constexpr (ks + 2 < 40) w[wn][3] = readw(ks + 2, 3);
      if constexpr (ks + 2 < 40) { fstage(ks + 2, 2); fstage(ks + 2, 3); }
      SB();
    });
  }
  asm volatile("s_nop 11" : "+v"(acc1[0]), "+v"(acc1[1]), "+v"(acc1[2]), "+v"(acc1[3]));

  u32x4 qa0, qa1, qb0, qb1;
  {
    const int h = lane >> 5, t = lane & 31;
    u32x2 P[15];
#pragma unroll
    for (int i = 0; i < 15; ++i) { const f32x16& a = acc1[i >> 2]; const int r = 4 * (i & 3); P[i] = (u32x2){pk2(a[r], a[r + 1]), pk2(a[r + 2], a[r + 3])}; }
    const u32x2 one = {0x00003f80u, 0u}, zero = {0u, 0u};
    auto cat = [](u32x2 a, u32x2 b) { return (u32x4){a[0], a[1], b[0], b[1]}; };
    auto sel = [&](u32x2 a1, u32x2 a0) { return (u32x2){h ? a1[0] : a0[0], h ? a1[1] : a0[1]}; };
    const unsigned row = L0 + L2_KV + (256 + 32 * wave + t) * 48;
    lds_st4(row + 16 * h, cat(P[5], P[6]));
    lds_st4(row + KV_IMG + 16 * (h ^ 1), cat(sel(P[7], P[8]), sel(P[8], P[9])));
    lds_st4(row + h * KV_IMG + 32, cat(sel(P[9], P[7]), zero));
    lds_st4(row + 2 * KV_IMG + 16 * h, cat(P[10], P[11]));
    lds_st4(row + 3 * KV_IMG + 16 * (h ^ 1), cat(sel(P[12], P[13]), sel(P[13], P[14])));
    lds_st4(row + (2 + h) * KV_IMG + 32, cat(sel(P[14], P[12]), one));
    asm volatile("s_waitcnt lgkmcnt(0)" ::: "memory");
#pragma unroll
    for (int u = 0; u < 6; ++u) {
      const int i = 64 * u + lane, a = i / 96, o = (i - 96 * a) * 16;
      const u32x4 v = lds_u4(L0 + L2_KV + a * KV_IMG + (256 + 32 * wave) * 48 + o);
      st16_sc1(v, rws, WS_KV + ((mb * 16 + j) * 4 + a) * 6144 + wave * 1536 + o, 0);
    }
    qa0 = cat(P[0], P[1]);
    qa1 = cat(sel(zero, P[2]), sel(zero, one));
    qb0 = cat(sel(P[2], P[3]), sel(P[3], P[4]));
    qb1 = cat(sel(P[4], zero), sel(one, zero));
    publish(ws, FL_KV, mb, j, wave, lane, tag);
  }
  asm volatile("" : "+v"(jg[0]), "+v"(jg[1]), "+v"(jg[2]));
  {
    unsigned gv[3] = {jg[0][0], jg[1][0], jg[2][0]};
    if (!__all(jg[0][1] == tag && jg[1][1] == tag && jg[2][1] == tag))
      sweep<3>(ws, [&](int k) { return WS_GRAN + jgi(k) * 8; }, [&](int) { return true; }, tag, gv);
#pragma unroll
    for (int k = 0; k < 3; ++k) if (k < 2 || lane < 32) {
      const int i = 64 * k + lane;
      unsigned v = gv[k];
      if (i < 80) { const f32x2 t = unpk(v) + 1.0f; v = pk2(t.x, t.y); }
      lds_st1(L0 + L2_JV + (i < 80 ? 320 * wave + 4 * i : 1280 + 320 * wave + 4 * (i - 80)), v);
    }
    asm volatile("s_waitcnt lgkmcnt(0)" ::: "memory");
    const f32x4 d = gemv16_run<5>(L0 + L2_JW + wave * 5120, L0 + L2_JV + wave * 320, 1, lane);
    if ((lane & 15) < 2) lds_st4(L0 + L3_JP + wave * 128 + (2 * (lane >> 4) + (lane & 1)) * 16, __builtin_bit_cast(u32x4, d));
  }

  {
    const int blk = wave >> 1, src = mb - 2 + blk;
    if (src >= 0) {
      wait_flags(ws, flagoff(FL_KV, src, 4 * j + (lane & 3)), lane < 4, tag);
#pragma unroll
      for (int i = 0; i < 12; ++i) {
        const int a = 2 * (wave & 1) + i / 6, sub = i % 6;
        dma16_sc1(rws, 16 * lane, WS_KV + ((src * 16 + j) * 4 + a) * 6144 + sub * 1024, L0 + L2_KV + a * KV_IMG + blk * 6144 + sub * 1024);
      }
    } else {
#pragma unroll
      for (int i = 0; i < 12; ++i) {
        const int a = 2 * (wave & 1) + i / 6, o = (i % 6) * 1024 + 16 * lane;
        lds_st4(L0 + L2_KV + a * KV_IMG + blk * 6144 + o, (u32x4){0u, 0u, ((o % 48) == 32 && a < 2) ? 0x0000f149u : 0u, 0u});
      }
    }
  }
  const int th = jL & 1, nt = jL >> 1;
  const rsrc_t rwo = mkrsrc(wo, D_ * D_ * 2);
  unsigned woo[7], wol[7];
#pragma unroll
  for (int i = 0; i < 7; ++i) {
    const int pi = min(wave + 4 * i, 24), kc = pi / 5, pc = pi - 5 * kc;
    auto rowmap = [&](int g, int t, int q) { const int k = 32 * kc + 8 * g + 4 * t + q, hp = k / 40, r = k - 40 * hp; return 40 * hp + (r < 20 ? r : r < 24 ? r + 16 : r - 4); };
    woo[i] = opaque(w80_voff(pc, lane, nt, rowmap));
    wol[i] = pi * 1024;
  }
  auto wo_piece = [&](int gam, int i) { dma16(rwo, woo[i], ((gam + rL) & 3) * 160 * 1280, L0 + (gam < 3 ? L3_W0 + gam * 25600 : L3_W1) + wol[i]); };
  vmwait<0>();
  BAR();
  if (ujob && wave == 1) {
    const unsigned pa = L0 + L3_JP + (2 * (lane >> 4) + (lane & 1)) * 16;
    const f32x4 d = (lds_f4(pa) + lds_f4(pa + 128)) + (lds_f4(pa + 256) + lds_f4(pa + 384));
    if ((lane & 15) < 2) {
#pragma unroll
      for (int r = 0; r < 4; ++r) st_u64(ws, WS_UV + ((jmat * 108 + junit) * 32 + (lane & 15) * 16 + 4 * (lane >> 4) + r) * 8, ((u64)tag << 32) | __float_as_uint(d[r]));
    }
  }


  {
    const rsrc_t rgt = mkrsrc(wave < 2 ? wg : wu, D_ * F_ * 2);
    const rsrc_t rdt = mkrsrc(wd, F_ * D_ * 2);
    const unsigned tdo = (135 * bid + min(64 * wave + lane, 134)) * 64;
    unsigned xro[3];
#pragma unroll
    for (int i = 0; i < 3; ++i) { const int idx = 64 * min(3 * wave + i, 9) + lane, row = idx / 10; xro[i] = (128 * mbL + 64 * th + row) * 1280 + 160 * nt + (idx - 10 * row) * 16; }
    const unsigned tgo = (20 * ii + 10 * (wave & 1) + (lane >> 5)) * 3456 + (xcd & 1) * 1792 + 64 * min(lane & 31, (xcd & 1) ? 25 : 27);
    const int h = lane >> 5, t = lane & 31;
    f32x16 mfirst, mdiag;
#pragma unroll
    for (int r = 0; r < 16; ++r) { const int key = (r & 3) + 8 * (r >> 2) + 4 * h; mfirst[r] = key > t ? 0.f : NEGBIG; mdiag[r] = key > t ? NEGBIG : 0.f; }
    const unsigned krow = L0 + L2_KV + (32 * wave + t) * 48;
    const unsigned ka0 = opaque(krow + 16 * h), ka1 = opaque(krow + 32), kb0 = opaque(krow + KV_IMG + 16 * (h ^ 1)), kb1 = opaque(krow + KV_IMG + 32);
    const unsigned vrow = L0 + L2_KV + 2 * KV_IMG + (32 * wave + 4 * h + ((lane & 15) >> 2)) * 48 + ((lane >> 4) & 1) * 32 + (lane & 3) * 8;
    const unsigned vra = opaque(vrow), vrb = opaque(vrow + KV_IMG);
    f32x16 sa[9], sb[9];
    float e[8];
    auto pfma = [&](const f32x16& s, int half, float mc) {
#pragma unroll
      for (int i = 0; i < 8; i += 2) {
        const f32x2 r = __builtin_elementwise_fma((f32x2){s[8 * half + i], s[8 * half + i + 1]}, (f32x2){SCORE_C, SCORE_C}, (f32x2){mc, mc});
        e[i] = r.x; e[i + 1] = r.y;
      } };
    auto pexp = [&]() {
#pragma unroll
      for (int i = 0; i < 8; ++i) e[i] = exp2_(e[i]); };
    auto pcvt = [&]() { return (u32x4){pk2(e[0], e[1]), pk2(e[2], e[3]), pk2(e[4], e[5]), pk2(e[6], e[7])}; };
    auto qk0 = [&](f32x16& s, int kb, u32x4 a0, u32x4 q0) { if (kb == 0) smfma_c(s, a0, q0, mfirst); else if (kb == 8) smfma_c(s, a0, q0, mdiag); else smfma_0(s, a0, q0); };
    auto maxhalf = [&](float (&m)[4], const f32x16& s, int half) {
#pragma unroll
      for (int r = 8 * half; r < 8 * half + 8; r += 2) m[(r >> 1) & 3] = fmaxf(fmaxf(m[(r >> 1) & 3], s[r]), s[r + 1]); };
    auto vfrag = [&](unsigned base, int kb, int half) { const unsigned a = base + (32 * kb + 16 * half) * 48; return trfrag(a, a + 8 * 48); };
    auto maxblk = [&](float (&m)[4], const f32x16& s) {
#pragma unroll
      for (int r = 0; r < 16; r += 2) m[(r >> 1) & 3] = fmaxf(fmaxf(m[(r >> 1) & 3], s[r]), s[r + 1]); };
    u32x4 k0 = lds_u4(ka0), k1 = lds_u4(ka1);
    float mx[4] = {NEGBIG, NEGBIG, NEGBIG, NEGBIG};
#pragma unroll
    for (int kb = 0; kb < 9; ++kb) {
      u32x4 n0, n1;
      SB();
      qk0(sa[kb], kb, k0, qa0);
      SB();
      n0 = kb < 8 ? lds_u4(ka0 + (kb + 1) * 1536) : lds_u4(kb0);
      if (kb == 2) asm volatile("s_nop 3" : "+v"(sa[0]));
      if (kb >= 2) maxhalf(mx, sa[kb - 2], 0);
      SB();
      smfma_a(sa[kb], k1, qa1);
      SB();
      n1 = kb < 8 ? lds_u4(ka1 + (kb + 1) * 1536) : lds_u4(kb1);
      if (kb >= 2) { maxhalf(mx, sa[kb - 2], 1); wo_piece(2, kb - 2); }
      k0 = n0; k1 = n1;
    }
    SB();
    asm volatile("s_nop 7" : "+v"(sa[7]));
    maxblk(mx, sa[7]);
    sfence(sa[8]);
    maxblk(mx, sa[8]);
    const float mca = -SCORE_C * xmax32(fmaxf(fmaxf(mx[0], mx[1]), fmaxf(mx[2], mx[3])));
    f32x16 oa = {}, ob = {};
    s16x8 v0 = vfrag(vra, 0, 0), v1 = vfrag(vra, 0, 1);
#pragma unroll
    for (int r = 0; r < 4; ++r) mx[r] = NEGBIG;
    SB();
#pragma unroll
    for (int kb = 0; kb < 9; ++kb) {
      qk0(sb[kb], kb, k0, qb0);
      SB();
      if (kb < 8) k0 = lds_u4(kb0 + (kb + 1) * 1536);
      pfma(sa[kb], 0, mca); pexp();
      SB();
      smfma_a(sb[kb], k1, qb1);
      SB();
      if (kb < 8) k1 = lds_u4(kb1 + (kb + 1) * 1536);
      const u32x4 p0 = pcvt();
      pfma(sa[kb], 1, mca);
      SB();
      oa = mfma32(v0, p0, oa);
      SB();
      v0 = kb < 8 ? vfrag(vra, kb + 1, 0) : vfrag(vrb, 0, 0);
      pexp();
      const u32x4 p1 = pcvt();
      if (kb >= 1) maxhalf(mx, sb[kb - 1], 0);
      SB();
      oa = mfma32(v1, p1, oa);
      SB();
      v1 = kb < 8 ? vfrag(vra, kb + 1, 1) : vfrag(vrb, 0, 1);
      if (kb >= 1) maxhalf(mx, sb[kb - 1], 1);
      if (kb < 7) wo_piece(0, kb);
      if (kb < 5) touch(rgt, tgo, kb * 2 * 3456, L0 + L_DUMMY);
      if (kb == 5) touch(rdt, tdo, 0, L0 + L_DUMMY);
      if (kb >= 6) dma16(rx, xro[kb >= 6 ? kb - 6 : 0], 0, L0 + L3_XR + min(3 * wave + kb - 6, 9) * 1024);
      SB();
    }
    sfence(sb[8]);
    maxblk(mx, sb[8]);
    const float mcb = -SCORE_C * xmax32(fmaxf(fmaxf(mx[0], mx[1]), fmaxf(mx[2], mx[3])));
    SB();
    pfma(sb[0], 0, mcb); pexp();
    SB();
#pragma unroll
    for (int kb = 0; kb < 9; ++kb) {
      const u32x4 p0 = pcvt();
      pfma(sb[kb], 1, mcb);
      SB();
      ob = mfma32(v0, p0, ob);
      SB();
      if (kb < 8) v0 = vfrag(vrb, kb + 1, 0);
      pexp();
      const u32x4 p1 = pcvt();
      if (kb < 8) pfma(sb[kb + 1], 0, mcb);
      SB();
      ob = mfma32(v1, p1, ob);
      SB();
      if (kb < 8) { v1 = vfrag(vrb, kb + 1, 1); pexp(); }
      if (kb < 7) wo_piece(1, kb);
      SB();
    }
    const auto la = __builtin_amdgcn_permlane32_swap(__float_as_uint(oa[8]), __float_as_uint(oa[8]), false, false);
    const auto lb = __builtin_amdgcn_permlane32_swap(__float_as_uint(ob[8]), __float_as_uint(ob[8]), false, false);
    const float ia = rcpf_(__uint_as_float(la[1])), ib = rcpf_(__uint_as_float(lb[1]));
    const unsigned obase = WS_O + (4 * mb + wave) * T32_D + 5 * j * 512;
    u32x4 va, vb2, vc;
#pragma unroll
    for (int e = 0; e < 4; ++e) { va[e] = pk2(oa[2 * e] * ia, oa[2 * e + 1] * ia); vb2[e] = pk2(ob[2 * e] * ib, ob[2 * e + 1] * ib); }
    vc = (u32x4){pk2(oa[8] * ia, oa[9] * ia), pk2(oa[10] * ia, oa[11] * ia), pk2(ob[8] * ib, ob[9] * ib), pk2(ob[10] * ib, ob[11] * ib)};
    st16_sc1(va, rws, obase + 16 * lane, 0);
    st16_sc1(vb2, rws, obase + 3 * 512 + 16 * lane, 0);
    st16_sc1(vc, rws, lane < 32 ? obase + 2 * 512 + 16 * lane : 0x40000000u, 0);
    publish(ws, FL_O, mb, j, wave, lane, tag);
  }
  BAR();
  const int t16 = lane & 15, g4 = lane >> 4;
  const int tok3 = 128 * mbL + 64 * th + 16 * wave + t16;
  auto goff = [&](int k) { const int i = min(64 * k + lane, 119), kind = i / 40, r = i - 40 * kind; return WS_GRAN + ((kind == 0 ? 640 : kind == 1 ? 1280 : 1600) + 40 * nt + r) * 8; };
  u32x2 vg2[2];
#pragma unroll
  for (int i = 0; i < 7; ++i) wo_piece(3, i);
#pragma unroll
  for (int k = 0; k < 2; ++k) ald8_sc1(vg2[k], rws, goff(k), 0);
  const int nu = j < 12 ? 7 : 6, u0 = j < 12 ? 7 * j : 84 + 6 * (j - 12), uodd = u0 & 1;
  u32x2 fo, uvg[7];
  const unsigned uvl = WS_UV + ((lane >> 5) * 108 * 32 + (lane & 31)) * 8;
#pragma unroll
  for (int p = 0; p < 7; ++p) ald8_sc1(uvg[p], rws, uvl, (u0 + unit_of(p, uodd, nu)) * 256);
  ald8_sc1(fo, rws, flagoff(FL_O, mbL, lane), 0);
  const rsrc_t rgu = mkrsrc(wave < 2 ? wg : wu, D_ * F_ * 2);
  unsigned guo[7];
#pragma unroll
  for (int i = 0; i < 7; ++i) {
    const int isup = wave >> 1, idx = 64 * (7 * (wave & 1) + i) + lane, rq = idx / 56, rem = idx - 56 * rq;
    int q, p, hf;
    if (rem < 48) { q = (rem >> 2) & 3; const int c = (rem & 3) ^ (isup ? 2 : 0); p = 2 * (rem >> 4) + (c >> 1); hf = c & 1; } else { q = (rem - 48) >> 1; p = 6; hf = rem & 1; }
    guo[i] = opaque((4 * rq + q) * 3456 + 32 * (u0 + unit_of(p, uodd, nu)) + 16 * hf);
  }
  vmwait<0>();
  asm volatile("" : "+v"(vg2[0]), "+v"(vg2[1]), "+v"(fo));
  unsigned uvv[7];
  {
    bool ok = true;
#pragma unroll
    for (int p = 0; p < 7; ++p) { asm volatile("" : "+v"(uvg[p])); uvv[p] = uvg[p][0]; ok &= uvg[p][1] == tag; }
    if (wave == 1 && !__all(ok)) sweep<7>(ws, [&](int p) { return uvl + (u0 + unit_of(p, uodd, nu)) * 256; }, [&](int) { return true; }, tag, uvv);
  }
  if (wave == 0 && !flags_up(fo, tag)) wait_flags(ws, flagoff(FL_O, mbL, lane), true, tag);
  if (wave == 2) {
    unsigned gv[2] = {vg2[0][0], vg2[1][0]};
    if (!__all(vg2[0][1] == tag && vg2[1][1] == tag)) sweep<2>(ws, goff, [&](int) { return true; }, tag, gv);
#pragma unroll
    for (int k = 0; k < 2; ++k) if (k == 0 || lane < 56) {
      const int i = 64 * k + lane, kind = i / 40, r = i - 40 * kind;
      const int ct = r >> 3, p = (nt & 1) ? (ct == 0 ? 4 : ct - 1) : ct;
      f32x2 v = unpk(gv[k]);
      if (kind == 1) v = bf16r2(v + 1.0f);
      lds_st2f(L0 + L3_VEC + kind * 320 + p * 64 + (r & 7) * 8, v);
    }
  }
  if (wave == 3 && bid == 0) {
    unsigned fv[16];
    sweep<16>(ws, [&](int k) { return flagoff(FL_KV, k, lane); }, [&](int) { return true; }, tag, fv);
    if (lane == 0) __hip_atomic_store((GAS unsigned*)(ws + WS_EPOCH), tag, __ATOMIC_RELAXED, __HIP_MEMORY_SCOPE_AGENT);
  }
  BAR();

  const unsigned b80_0 = g4 * 1024 + ((lane & 15) >> 2) * 64 + (g4 & 1) * 32 + (lane & 3) * 8;
  const unsigned b80_1 = b80_0 ^ 32u;
  const unsigned b80_2 = 4096 + 8 * lane;
  struct B80 { unsigned a, b, c; };
  auto b80 = [&](unsigned kbase) { return B80{opaque(kbase + b80_0), opaque(kbase + b80_1), opaque(kbase + b80_2)}; };
  auto w80 = [&](const B80& B, int koff, int p) { const unsigned a = (p == 4 ? B.c : ((p & 1) ? B.b : B.a) + (p >> 1) * 256) + koff; return trfrag(a, a + 512); };

  u32x2 x1p[5];
  const int so4 = 0;
  auto gu_piece = [&](int st, int i) { dma16(rgu, guo[i], rot10(so4, st) * 64 * 3456, L0 + (st % 5) * L4_STAGE + (wave >> 1) * L4_U + (wave & 1) * 7168 + i * 1024); };
  {
    u32x4 of[20];
    const unsigned ofb = WS_O + (4 * mbL + 2 * th + (wave >> 1)) * T32_D + g4 * 512 + ((wave & 1) * 16 + t16) * 16;
    const Rot R3 = mkrot(rL, 5 * rL, 20, 2048);
    auto fo20 = [&](int s) { return R3.b[s < 5 ? 0 : s < 10 ? 1 : s < 15 ? 2 : 3] + s * 2048; };
#pragma unroll
    for (int s = 0; s < P3F0; ++s) ald16_sc1(of[s], rws, ofb, fo20(s));
    f32x4 acc[5];
    const B80 B0 = b80(L0 + L3_W0), B1 = b80(L0 + L3_W0 + 10 * 5120), B2 = b80(L0 + L3_W1);
    auto rd3 = [&](int s, int p) { return s < 10 ? w80(B0, s * 5120, p) : s < L3_NPRE ? w80(B1, (s - 10) * 5120, p) : w80(B2, (s - L3_NPRE) * 5120, p); };
    s16x8 w[3][5];
#pragma unroll
    for (int p = 0; p < 5; ++p) { w[0][p] = rd3(0, p); w[1][p] = rd3(1, p); }
    SB();
    static_for<0, 20>([&](auto SS) {
      constexpr int s = SS;
      vmwait<p3_wait_frag(s)>(of[s]);
      SB();
#pragma unroll
      for (int p = 0; p < 5; ++p) {
        if (s == 0) hmfma_0(acc[p], w[s % 3][p], of[s]); else hmfma_a(acc[p], w[s % 3][p], of[s]);
        SB();
        if (p == 0 && s + P3F0 < 20) ald16_sc1(of[s + P3F0 < 20 ? s + P3F0 : 0], rws, ofb, fo20(s + P3F0));
        if (s + 2 < 20) w[(s + 2) % 3][p] = rd3(s + 2, p);
        SB();
      }
    });
    asm volatile("s_nop 7" : "+v"(acc[0]), "+v"(acc[1]), "+v"(acc[2]), "+v"(acc[3]), "+v"(acc[4]));
    BAR();
    f32x2 xv[5][2]; f32x2 s2 = {0.f, 0.f};
    u32x2 yp[5];
    const unsigned vecb = opaque(L0 + L3_VEC + g4 * 16), xrb = opaque(L0 + L3_XR + (16 * wave + t16) * 160 + g4 * 8);
    u32x2 xres[5];
#pragma unroll
    for (int p = 0; p < 5; ++p) xres[p] = *(LAS u32x2*)(size_t)(xrb + 32 * ct_of(p, nt & 1));
#pragma unroll
    for (int p = 0; p < 5; ++p) {
      const f32x4 ga = lds_f4(vecb + p * 64), sb1 = lds_f4(vecb + 320 + p * 64);
#pragma unroll
      for (int r = 0; r < 2; ++r) {
        const f32x2 at = bf16r2((f32x2){acc[p][2 * r], acc[p][2 * r + 1]});
        const f32x2 xs = unpk(xres[p][r]) + bf16r2(at * (f32x2){ga[2 * r], ga[2 * r + 1]});
        x1p[p][r] = pk2(xs.x, xs.y);
        xv[p][r] = unpk(x1p[p][r]);
        s2 += xv[p][r];
        const f32x2 y = xv[p][r] * (f32x2){sb1[2 * r], sb1[2 * r + 1]};
        yp[p][r] = pk2(y.x, y.y);
      }
    }
    const unsigned yb = WS_Y + (4 * mbL + 2 * th + (wave >> 1)) * T32_D + 10 * nt * 512 + ((wave & 1) * 16 + t16) * 16;
#pragma unroll
    for (int pp = 0; pp < 3; ++pp) {
      const int pa = 2 * pp, pb = pp < 2 ? 2 * pp + 1 : 4;
      const auto r0 = __builtin_amdgcn_permlane16_swap(yp[pa][0], yp[pb][0], false, false), r1 = __builtin_amdgcn_permlane16_swap(yp[pa][1], yp[pb][1], false, false);
      const int ct = (g4 & 1) ? ct_of(pb, nt & 1) : ct_of(pa, nt & 1);
      st16_sc1((u32x4){r0[0], r1[0], r0[1], r1[1]}, rws, (pp < 2 || !(g4 & 1)) ? yb + (2 * ct + (g4 >> 1)) * 512 : 0x40000000u, 0);
    }
    const float mt = xsum32(xsum16(s2.x + s2.y)) * (1.0f / 80);
    f32x2 m2 = {0.f, 0.f};
#pragma unroll
    for (int p = 0; p < 5; ++p)
#pragma unroll
      for (int r = 0; r < 2; ++r) { const f32x2 d = xv[p][r] - mt; m2 += d * d; }
    const float m2s = xsum32(xsum16(m2.x + m2.y));
    if (lane < 16) st_u64(ws, WS_STAT + (((mbL * 2 + th) * 8 + nt) * 64 + 16 * wave + t16) * 8, ((u64)__float_as_uint(m2s) << 32) | __float_as_uint(mt));
#pragma unroll
    for (int i = 0; i < 7; ++i) gu_piece(0, i);
    vmwait<7>();
    if (lane == 0) st_u64(ws, flagoff(FL_Y, mbL, 4 * jL + wave), flagval(tag));
  }
  BAR_RAW();

  for (int st = 1; st < 4; ++st)
#pragma unroll
    for (int i = 0; i < 7; ++i) gu_piece(st, i);
  u32x2 fy;
  ald8_sc1(fy, rws, flagoff(FL_Y, mb, lane), 0);
  if (wave == 1) {
#pragma unroll
    for (int p = 0; p < 7; ++p) lds_st1(L0 + L4_UV + (p * 64 + lane) * 4, uvv[p]);
  }
  const rsrc_t rwd = mkrsrc(wd, F_ * D_ * 2);
  unsigned wdg[8];
  {
    auto rm = [](int g, int t, int q) { return 16 * (g >> 1) + 8 * t + 4 * (g & 1) + q; };
    const unsigned b0 = w80_voff(0, lane, nt, rm), b1 = w80_voff(1, lane, nt, rm), b4 = w80_voff(4, lane, nt, rm);
#pragma unroll
    for (int i = 0; i < 8; ++i) {
      const int pc = min(8 * wave + i, 29), kl = pc / 5, pp = pc - 5 * kl;
      wdg[i] = opaque((pp == 4 ? b4 : (pp & 1) ? b1 : b0) + (kl * 32 + (pp == 4 ? 0 : 16 * (pp >> 1))) * 1280);
    }
  }
  auto wd_piece = [&](int G, int i) { const int g = G + goL; dma16(rwd, wdg[i], (g >= 9 ? g - 9 : g) * 6 * 32 * 1280, L0 + (G % 5) * 30720 + min(8 * wave + i, 29) * 1024); };
  const Rot R5 = mkrot(rD, 6 * goL, 54, 2048);
  auto fo54 = [&](int s) { return R5.b[s < 12 ? 0 : s < 30 ? 1 : s < 42 ? 2 : 3] + s * 2048; };
  vmwait<0>(); asm volatile("" : "+v"(fy));
  if (wave == 0 && !flags_up(fy, tag)) wait_flags(ws, flagoff(FL_Y, mb, lane), true, tag);
  vmwait<0>();
  BAR();
  u32x2 stv[4];
#pragma unroll
  for (int i = 0; i < 4; ++i) { const int T = 32 * wave + (lane & 31); ald8_sc1(stv[i], rws, WS_STAT + (((mb * 2 + (T >> 6)) * 8 + 4 * (lane >> 5) + i) * 64 + (T & 63)) * 8, 0); }
  u32x4 yf[40];
  const unsigned yfo = WS_Y + (4 * mb + wave) * T32_D + 16 * lane;
  const Rot R4 = mkrot(0, 0, 40, 1024);
#pragma unroll
  for (int ks = 0; ks < P4F0; ++ks) ald16_sc1(yf[ks], rws, yfo, fo40(R4, ks));
  f32x16 acc4[7];
  {
    const int h = lane >> 5, blk = (lane >> 4) & 1, q = (lane & 15) >> 2;
    const unsigned c0 = L0 + blk * L4_U + 2 * h * 896 + q * 64 + blk * 32 + (lane & 3) * 8;
    const unsigned c2 = L0 + blk * L4_U + 2 * h * 896 + 768 + q * 32 + (lane & 3) * 8;
    unsigned cb0[5], cb1[5], cb2[5];
#pragma unroll
    for (int sl = 0; sl < 5; ++sl) { cb0[sl] = opaque(c0 + sl * L4_STAGE); cb1[sl] = opaque((c0 ^ 32u) + sl * L4_STAGE); cb2[sl] = opaque(c2 + sl * L4_STAGE); }
    auto readw = [&](int ks, int p) {
      const int sl = (ks >> 2) % 5;
      const unsigned a = (p == 6 ? cb2[sl] : ((p & 1) ? cb1[sl] : cb0[sl]) + (p >> 1) * 256) + (ks & 3) * 4 * 896;
      return trfrag(a, a + 896); };
    s16x8 w[3][7];
#pragma unroll
    for (int p = 0; p < 7; ++p) { w[0][p] = readw(0, p); w[1][p] = readw(1, p); }
    SB();
    static_for<0, 40>([&](auto KS) {
      constexpr int ks = KS, c = ks % 3, n = (ks + 2) % 3;
      if constexpr ((ks & 3) == 2) {
        constexpr int st = (ks + 2) / 4;
        if constexpr (st >= 4 && st <= 9) vmwait<p4_wait_stage(st)>();
        BAR_RAW();
      }
      vmwait<p4_wait_frag(ks)>(yf[ks]);
      if constexpr (ks == 0) asm volatile("" : "+v"(stv[0]), "+v"(stv[1]), "+v"(stv[2]), "+v"(stv[3]));
      SB();
#pragma unroll
      for (int p = 0; p < 7; ++p) {
        if (ks == 0) gmfma_0(acc4[p], w[c][p], yf[ks]); else gmfma_a(acc4[p], w[c][p], yf[ks]);
        SB();
        if (p == 1 && ks + P4F0 < 40) ald16_sc1(yf[ks + P4F0 < 40 ? ks + P4F0 : 0], rws, yfo, fo40(R4, ks + P4F0));
        if (p == 3 && p4_pieces(ks) > 0) gu_piece((ks + 14) / 4, 2 * ((ks + 14) % 4));
        if (p == 5 && p4_pieces(ks) > 1) gu_piece((ks + 14) / 4, 2 * ((ks + 14) % 4) + 1);
        if (p == 3 && p4_touch(ks) > 0) wd_piece(p4_wd_before(ks) / 8, p4_wd_before(ks) % 8);
        if (p == 5 && p4_touch(ks) > 1) wd_piece((p4_wd_before(ks) + 1) / 8, (p4_wd_before(ks) + 1) % 8);
        if (ks + 2 < 40) w[n][p] = readw(ks + 2, p);
        SB();
      }
    });
  }
  asm volatile("s_nop 11" : "+v"(acc4[0]), "+v"(acc4[1]), "+v"(acc4[2]), "+v"(acc4[3]), "+v"(acc4[4]), "+v"(acc4[5]), "+v"(acc4[6]));
  {
    const int h = lane >> 5;
    float ms = 0.f;
#pragma unroll
    for (int i = 0; i < 4; ++i) ms += __uint_as_float(stv[i][0]);
    const float mean = xsum32(ms) * 0.125f;
    float m2 = 0.f;
#pragma unroll
    for (int i = 0; i < 4; ++i) { const float d = __uint_as_float(stv[i][0]) - mean; m2 += __uint_as_float(stv[i][1]) + 80.0f * d * d; }
    const float rs = rden_of(xsum32(m2) * (1.0f / D_)), nm = -rs * bf16r(mean);
    const unsigned ab = WS_ACT + (4 * mb + wave) * T32_F + 16 * lane, uvb = opaque(L0 + L4_UV + 16 * h);
#pragma unroll
    for (int p = 0; p < 7; ++p) {
      if (p < 6 || nu == 7) {
        const unsigned tb = uvb + p * 256;
        u32x4 ov;
#pragma unroll
        for (int half = 0; half < 2; ++half) {
          const f32x4 ug = lds_f4(tb + 32 * half), vg = lds_f4(tb + 64 + 32 * half), uu = lds_f4(tb + 128 + 32 * half), vu = lds_f4(tb + 192 + 32 * half);
#pragma unroll
          for (int r = 0; r < 2; ++r) {
            const f32x2 ag = {acc4[p][4 * half + 2 * r], acc4[p][4 * half + 2 * r + 1]}, au = {acc4[p][8 + 4 * half + 2 * r], acc4[p][8 + 4 * half + 2 * r + 1]};
            const f32x2 g = rs * ag + (nm * (f32x2){ug[2 * r], ug[2 * r + 1]} + (f32x2){vg[2 * r], vg[2 * r + 1]});
            const f32x2 u = rs * au + (nm * (f32x2){uu[2 * r], uu[2 * r + 1]} + (f32x2){vu[2 * r], vu[2 * r + 1]});
            const f32x2 e = g * (-LOG2E);
            const f32x2 den = (f32x2){exp2_(e.x), exp2_(e.y)} + 1.0f;
            const f32x2 a = (g * u) * (f32x2){rcpf_(den.x), rcpf_(den.y)};
            ov[2 * half + r] = pk2(a.x, a.y);
          }
        }
        st16_sc1(ov, rws, ab + (u0 + unit_of(p, uodd, nu)) * 1024, 0);
      }
    }
#pragma unroll
    for (int i = 4; i < 8; ++i) wd_piece(2, i);
#pragma unroll
    for (int i = 0; i < 3; ++i) wd_piece(3, i);
    vmwait<7>();
    if (lane == 0) st_u64(ws, flagoff(FL_ACT, mb, 4 * j + wave), flagval(tag));
  }
  BAR();

#pragma unroll
  for (int i = 3; i < 8; ++i) wd_piece(3, i);
#pragma unroll
  for (int i = 0; i < 8; ++i) wd_piece(4, i);
  if (wave == 0) wait_flags(ws, flagoff(FL_ACT, mbL, lane), true, tag);
  vmwait<0>();
  BAR();
  {
    u32x4 af[54];
    const unsigned afb = WS_ACT + (4 * mbL + 2 * th + (wave >> 1)) * T32_F + g4 * 512 + ((wave & 1) * 16 + t16) * 16;
#pragma unroll
    for (int s = 0; s < P5F0; ++s) ald16_sc1(af[s], rws, afb, fo54(s));
    f32x4 acc[5];
    const B80 B5[3] = {b80(L0), b80(L0 + 61440), b80(L0 + 122880)};
    auto rd5 = [&](int s, int p) { const int sl = s % 30; return w80(B5[sl / 12], (sl % 12) * 5120, p); };
    s16x8 w[3][5];
#pragma unroll
    for (int p = 0; p < 5; ++p) { w[0][p] = rd5(0, p); w[1][p] = rd5(1, p); }
    SB();
    static_for<0, 54>([&](auto SS) {
      constexpr int s = SS;
      if constexpr (s % 6 == 4 && s + 2 < 54) {
        constexpr int g = (s + 2) / 6;
        if constexpr (g >= 5) vmwait<p5_wait_group(g)>();
        BAR_RAW();
      }
      vmwait<p5_wait_frag(s)>(af[s]);
      SB();
#pragma unroll
      for (int p = 0; p < 5; ++p) {
        if (s == 0) hmfma_0(acc[p], w[s % 3][p], af[s]); else hmfma_a(acc[p], w[s % 3][p], af[s]);
        SB();
        constexpr int r = (s + 20) % 6, i0 = r < 2 ? 2 * r : r + 2;
        if (p == 0 && s + P5F0 < 54) ald16_sc1(af[s + P5F0 < 54 ? s + P5F0 : 0], rws, afb, fo54(s + P5F0));
        if (p == 2 && p5_pieces(s) > 0) wd_piece((s + 20) / 6, i0);
        if (p == 4 && p5_pieces(s) > 1) wd_piece((s + 20) / 6, i0 + 1);
        if (s + 2 < 54) w[(s + 2) % 3][p] = rd5(s + 2, p);
        SB();
      }
    });
    asm volatile("s_nop 7" : "+v"(acc[0]), "+v"(acc[1]), "+v"(acc[2]), "+v"(acc[3]), "+v"(acc[4]));
    u32x2 op[5];
    const unsigned gbb = opaque(L0 + L3_VEC + 640 + g4 * 16);
#pragma unroll
    for (int p = 0; p < 5; ++p) {
      const f32x4 gb = lds_f4(gbb + p * 64);
#pragma unroll
      for (int r = 0; r < 2; ++r) {
        const f32x2 dn = bf16r2((f32x2){acc[p][2 * r], acc[p][2 * r + 1]});
        const f32x2 o = unpk(x1p[p][r]) + bf16r2(dn * (f32x2){gb[2 * r], gb[2 * r + 1]});
        op[p][r] = pk2(o.x, o.y);
      }
    }
    const rsrc_t ro = mkrsrc(out, S_ * D_ * 2);
#pragma unroll
    for (int pp = 0; pp < 3; ++pp) {
      const int pa = 2 * pp, pb = pp < 2 ? 2 * pp + 1 : 4;
      const auto r0 = __builtin_amdgcn_permlane16_swap(op[pa][0], op[pb][0], false, false), r1 = __builtin_amdgcn_permlane16_swap(op[pa][1], op[pb][1], false, false);
      const int ct = (g4 & 1) ? ct_of(pb, nt & 1) : ct_of(pa, nt & 1);
      __builtin_amdgcn_raw_buffer_store_b128((u32x4){r0[0], r1[0], r0[1], r1[1]}, ro, (pp < 2 || !(g4 & 1)) ? tok3 * 1280 + 160 * nt + 32 * ct + 16 * (g4 >> 1) : 0x40000000u, 0, 16);
    }
  }
  asm volatile(".Ladaln_code_end:");
}

extern "C" void kernel_launch(void* const* d_in, const int* in_sizes, int n_in, void* d_out, int out_size, void* d_ws, size_t ws_size,
                              hipStream_t stream) {
  (void)in_sizes; (void)n_in; (void)out_size; (void)ws_size;
  adaln_block_kernel<<<256, 256, 0, stream>>>((const char*)d_in[0], (const char*)d_in[1], (const char*)d_in[2], (const char*)d_in[4], (const char*)d_in[5],
                                              (const char*)d_in[6], (const char*)d_in[7], (const char*)d_in[8], (char*)d_out, (char*)d_ws);
}
```
